```python
import torch

def _softmax(x, axis=-1):
    return torch.softmax(x, dim=axis)

def _gelu(x):
    return torch.nn.functional.gelu(x, approximate='tanh')

def _silu(x):
    return torch.nn.functional.silu(x)

def _sigmoid(x):
    return torch.sigmoid(x)

def _layernorm(x, axis=-1, eps=1e-05):
    m = x.mean(dim=axis, keepdim=True)
    v = x.var(dim=axis, keepdim=True, unbiased=False)
    return (x - m) / torch.sqrt(v + eps)

def _scatter_add(tgt, idx, src, axis=0):
    return tgt.clone().index_add_(axis, idx, src)

def _segment_sum(src, seg, n):
    out = torch.zeros((n,) + src.shape[1:], dtype=src.dtype)
    return out.index_add_(0, seg, src)

def _pad(x, pw):
    flat = tuple((v for pair in reversed(pw) for v in pair))
    return torch.nn.functional.pad(x, flat)
kernel_name = 'parallel_sum__mha_block__mixer_gmlp__prompt'

def setup_inputs():

    _w = lambda a: (lambda t: t.to(torch.bfloat16) if t.is_floating_point() else t)(torch.as_tensor(a))

    def _A_setup():
        B, S, D, H = (1, 2048, 512, 32)
        return {'x': _w((lambda *s: torch.randn(*s, dtype=torch.bfloat16) * 0.05)(B, S, D)), 'wqkv': _w((lambda *s: torch.randn(*s, dtype=torch.bfloat16) * 0.2)(D, 3 * D)), 'wo': _w((lambda *s: torch.randn(*s, dtype=torch.bfloat16) * 0.2)(D, D))}
    _A_in = _A_setup()

    def _B_setup():
        B, S, D, F = (1, 2048, 512, 1792)
        Fh = F // 2
        return {'x': _w((lambda *s: torch.randn(*s, dtype=torch.bfloat16) * 0.05)(B, S, D)), 'w_in': _w((lambda *s: torch.randn(*s, dtype=torch.bfloat16) * 0.8)(D, 2 * Fh)), 'w_sp': _w(0.01 * (lambda *s: torch.randn(*s, dtype=torch.bfloat16) * 0.8)(S, S)), 'b_sp': _w(1.0 + 0.01 * (lambda *s: torch.randn(*s, dtype=torch.bfloat16) * 0.2)(S)), 'w_out': _w((lambda *s: torch.randn(*s, dtype=torch.bfloat16) * 0.2)(Fh, D))}
    _B_in = _B_setup()
    return {'A_x': _A_in['x'], 'A_wqkv': _A_in['wqkv'], 'A_wo': _A_in['wo'], 'B_x': _B_in['x'], 'B_w_in': _B_in['w_in'], 'B_w_sp': _B_in['w_sp'], 'B_b_sp': _B_in['b_sp'], 'B_w_out': _B_in['w_out']}

def reference(A_x, A_wqkv, A_wo, B_x, B_w_in, B_w_sp, B_b_sp, B_w_out):

    def _A_ref(x, wqkv, wo):
        B, S, D = x.shape
        H = 32
        Dh = D // H
        qkv = (lambda x, *d: x.permute(*d))((x @ wqkv).reshape(B, S, 3, H, Dh), 2, 0, 3, 1, 4)
        q, k, v = (qkv[0], qkv[1], qkv[2])
        scores = torch.einsum('bhqd,bhkd->bhqk', q, k) * Dh ** (-0.5)
        p = _softmax(scores, axis=-1)
        o = torch.einsum('bhqk,bhkd->bhqd', p, v)
        return (lambda x, *d: x.permute(*d))(o, 0, 2, 1, 3).reshape(B, S, D) @ wo

    def _B_ref(x, w_in, w_sp, b_sp, w_out):
        B, S, D = x.shape
        F = w_in.shape[-1]
        Fh = F // 2
        z = _gelu(torch.einsum('bsd,df->bsf', x, w_in))
        z1 = z[..., :Fh].clone()
        z2 = _layernorm(z[..., Fh:], axis=-1)
        g = torch.einsum('ts,bsf->btf', w_sp, z2) + b_sp[None, :, None]
        return torch.einsum('bsf,fd->bsd', z1 * g, w_out)
    x = A_x
    xn = _layernorm(x, axis=-1)
    return x + _A_ref(xn, A_wqkv, A_wo) + _B_ref(xn, B_w_in, B_w_sp, B_b_sp, B_w_out)
```

```cpp
#include <hip/hip_runtime.h>
#include <hip/hip_bf16.h>
typedef unsigned short u16;
__device__ inline float bf2f(u16 v) { return __uint_as_float((unsigned)v << 16); }
__device__ inline u16 f2bf(float f) { unsigned u = __float_as_uint(f); u += 0x7FFF + ((u >> 16) & 1); return (u16)(u >> 16); }
constexpr int S = 2048, D = 512, H = 32, DH = 16, F = 1792, FH = 896;
__global__ void k_ln(const u16* x, float* xn) {
    int r = blockIdx.x; __shared__ float red[2][64];
    float s = 0, ss = 0;
    for (int i = threadIdx.x; i < D; i += 64) { float v = bf2f(x[r * D + i]); s += v; }
    for (int o = 32; o; o >>= 1) s += __shfl_xor(s, o);
    float m = s / D;
    for (int i = threadIdx.x; i < D; i += 64) { float v = bf2f(x[r * D + i]) - m; ss += v * v; }
    for (int o = 32; o; o >>= 1) ss += __shfl_xor(ss, o);
    float rs = rsqrtf(ss / D + 1e-5f);
    for (int i = threadIdx.x; i < D; i += 64) xn[r * D + i] = (bf2f(x[r * D + i]) - m) * rs;
}
template<int MODE> __global__ void k_gemm(const float* A, const u16* B, float* C, int M, int N, int K) {
    __shared__ float As[16][17], Bs[16][17];
    int tx = threadIdx.x & 15, ty = threadIdx.x >> 4; int m = blockIdx.y * 16 + ty, n = blockIdx.x * 16 + tx; float acc = 0;
    for (int k0 = 0; k0 < K; k0 += 16) { As[ty][tx] = A[m * K + k0 + tx]; Bs[ty][tx] = bf2f(B[(k0 + ty) * N + n]); __syncthreads();
        for (int k = 0; k < 16; ++k) acc += As[ty][k] * Bs[k][tx]; __syncthreads(); }
    if (MODE == 1) { float u = 0.7978845608f * (acc + 0.044715f * acc * acc * acc); acc = 0.5f * acc * (1.f + tanhf(u)); }
    C[m * N + n] = bf2f(f2bf(acc));
}
__global__ void k_attn(const float* qkv, float* o) {
    int q = blockIdx.x * 64 + threadIdx.x, h = blockIdx.y; float qq[16], acc[16]; float m = -1e30f, l = 0;
    for (int d = 0; d < 16; ++d) { qq[d] = qkv[q * 1536 + h * 16 + d] * 0.25f; acc[d] = 0; }
    for (int k = 0; k < S; ++k) { float s = 0; for (int d = 0; d < 16; ++d) s += qq[d] * qkv[k * 1536 + 512 + h * 16 + d];
        float mn = fmaxf(m, s); float a = __expf(m - mn), p = __expf(s - mn); l = l * a + p; for (int d = 0; d < 16; ++d) acc[d] = acc[d] * a + p * qkv[k * 1536 + 1024 + h * 16 + d]; m = mn; }
    for (int d = 0; d < 16; ++d) o[q * 512 + h * 16 + d] = bf2f(f2bf(acc[d] / l));
}
__global__ void k_ln2(const float* z, u16* z2n) {
    int r = blockIdx.x; float s = 0, ss = 0;
    for (int i = threadIdx.x; i < FH; i += 64) s += z[r * F + FH + i];
    for (int o = 32; o; o >>= 1) s += __shfl_xor(s, o);
    float m = s / FH;
    for (int i = threadIdx.x; i < FH; i += 64) { float v = z[r * F + FH + i] - m; ss += v * v; }
    for (int o = 32; o; o >>= 1) ss += __shfl_xor(ss, o);
    float rs = rsqrtf(ss / FH + 1e-5f);
    for (int i = threadIdx.x; i < FH; i += 64) z2n[r * FH + i] = f2bf((z[r * F + FH + i] - m) * rs);
}
__global__ void k_cvt(const u16* a, float* b, int n) { int i = blockIdx.x * 256 + threadIdx.x; if (i < n) b[i] = bf2f(a[i]); }
__global__ void k_gate(const float* z, const float* g, const u16* b, float* hh) { int i = blockIdx.x * 256 + threadIdx.x; int t = i / FH, f = i % FH; hh[i] = bf2f(f2bf(z[t * F + f] * (g[i] + bf2f(b[t])))); }
__global__ void k_fin(const u16* x, const float* a, const float* b, u16* out) { int i = blockIdx.x * 256 + threadIdx.x; out[i] = f2bf(bf2f(x[i]) + a[i] + b[i]); }
extern "C" void kernel_launch(void* const* d_in, const int* in_sizes, int n_in, void* d_out, int out_size, void* d_ws, size_t ws_size, hipStream_t stream) {
    const u16 *x = (const u16*)d_in[0], *wqkv = (const u16*)d_in[1], *wo = (const u16*)d_in[2], *win = (const u16*)d_in[4], *wsp = (const u16*)d_in[5], *bsp = (const u16*)d_in[6], *wout = (const u16*)d_in[7];
    float* ws = (float*)d_ws; float* xn = ws; float* qkv = xn + S * D; float* z = qkv + S * 1536; float* o = z + S * F; float* wspf = o + S * D; float* g = wspf + S * S; float* hh = g + S * FH; float* ya = hh + S * FH; float* yb = ya + S * D; u16* z2n = (u16*)(yb + S * D);
    k_ln<<<S, 64, 0, stream>>>(x, xn);
    k_gemm<0><<<dim3(1536 / 16, S / 16), 256, 0, stream>>>(xn, wqkv, qkv, S, 1536, D);
    k_gemm<1><<<dim3(F / 16, S / 16), 256, 0, stream>>>(xn, win, z, S, F, D);
    k_attn<<<dim3(S / 64, H), 64, 0, stream>>>(qkv, o);
    k_ln2<<<S, 64, 0, stream>>>(z, z2n);
    k_cvt<<<S * S / 256, 256, 0, stream>>>(wsp, wspf, S * S);
    k_gemm<0><<<dim3(FH / 16, S / 16), 256, 0, stream>>>(wspf, z2n, g, S, FH, S);
    k_gate<<<S * FH / 256, 256, 0, stream>>>(z, g, bsp, hh);
    k_gemm<0><<<dim3(D / 16, S / 16), 256, 0, stream>>>(o, wo, ya, S, D, D);
    k_gemm<0><<<dim3(D / 16, S / 16), 256, 0, stream>>>(hh, wout, yb, S, D, FH);
    k_fin<<<S * D / 256, 256, 0, stream>>>(x, ya, yb, (u16*)d_out);
}
```

```cpp
#include <hip/hip_runtime.h>
constexpr int LDS_BYTES = 163840;
__global__ __launch_bounds__(512, 2) void fused_block(const void* x, const void* wqkv, const void* wo, const void* win, const void* wsp, const void* bsp, const void* wout, void* out, void* ws) {
    unsigned tid = threadIdx.x, bid = blockIdx.x; auto args = __builtin_amdgcn_kernarg_segment_ptr();
    asm volatile(
        "s_load_dwordx2 s[8:9], s[4:5], 0\n"
        "s_load_dwordx2 s[16:17], s[4:5], 24\n"
        "s_load_dwordx2 s[12:13], s[4:5], 8\n"
        "s_load_dwordx2 s[20:21], s[4:5], 64\n"
        "s_load_dwordx2 s[24:25], s[4:5], 32\n"
        "s_load_dwordx2 s[44:45], s[4:5], 16\n"
        "s_load_dwordx2 s[46:47], s[4:5], 48\n"
        "s_load_dwordx2 s[36:37], s[4:5], 40\n"
        "s_load_dwordx2 s[32:33], s[4:5], 56\n"
        "v_readfirstlane_b32 s48, v0\n"
        "v_and_b32 v1, 63, v0\n"
        "s_lshr_b32 s48, s48, 6\n"
        "s_and_b32 s49, s6, 7\n"
        "s_lshr_b32 s50, s6, 3\n"
        "s_and_b32 s51, s48, 3\n"
        "s_lshr_b32 s52, s48, 2\n"
        "s_and_b32 s62, s50, 3\n"
        "s_lshr_b32 s63, s50, 2\n"
        "s_lshr_b32 s53, s49, 1\n"
        "s_lshl2_add_u32 s53, s53, s62\n"
        "s_and_b32 s64, s49, 1\n"
        "s_mul_i32 s54, s64, 7\n"
        "s_add_u32 s54, s54, s63\n"
        "s_add_u32 s64, s64, 14\n"
        "s_cmp_lt_u32 s63, 7\n"
        "s_cselect_b32 s54, s54, s64\n"
        "s_lshl1_add_u32 s57, s62, s63\n"
        "s_and_b32 s57, s57, 7\n"
        "s_lshl_b32 s56, s48, 10\n"
        "s_lshl_b32 s64, s48, 3\n"
        "s_lshl_b32 s65, s48, 2\n"
        "s_lshl_b32 s62, s53, 17\n"
        "s_lshl_b32 s63, s54, 7\n"
        "v_lshrrev_b32 v121, 3, v1\n"
        "v_add_u32 v121, s64, v121\n"
        "v_and_b32 v123, 7, v1\n"
        "v_lshrrev_b32 v122, 4, v1\n"
        "v_add_u32 v122, s65, v122\n"
        "v_and_b32 v122, 7, v122\n"
        "v_xor_b32 v123, v123, v122\n"
        "v_lshl_add_u32 v7, v123, 4, s62\n"
        "v_lshl_add_u32 v7, v121, 10, v7\n"
        "s_and_b32 s65, s48, 1\n"
        "s_lshl_b32 s65, s65, 1\n"
        "v_and_b32 v123, 1, v1\n"
        "v_lshl_add_u32 v120, v123, 4, s63\n"
        "v_bfe_u32 v124, v1, 1, 2\n"
        "v_bfe_u32 v123, v1, 4, 1\n"
        "v_or_b32 v123, s65, v123\n"
        "v_xor_b32 v124, v124, v123\n"
        "v_lshl_add_u32 v120, v124, 5, v120\n"
        "v_mul_u32_u24 v4, 0xe00, v121\n"
        "v_add_u32 v4, v4, v120\n"
        "s_cmp_lt_u32 s54, 14\n"
        "s_cselect_b32 s18, 1835008, 0\n"
        "s_cselect_b32 s42, 16252928, 0\n"
        "s_mov_b32 s10, 2097152\n"
        "s_mov_b32 s11, 0x20000\n"
        "s_mov_b32 s19, 0x20000\n"
        "s_waitcnt lgkmcnt(0)\n"
        "s_add_u32 m0, s56, 0\n"
        "s_add_u32 s72, s57, 0\n"
        "s_and_b32 s72, s72, 7\n"
        "s_lshl_b32 s83, s72, 7\n"
        "buffer_load_dwordx4 v7, s[8:11], s83 offen lds\n"
        "s_add_u32 m0, s56, 8192\n"
        "s_add_u32 s83, s83, 65536\n"
        "buffer_load_dwordx4 v7, s[8:11], s83 offen lds\n"
        "s_getpc_b64 s[28:29]\n"
        "98:\n"
        "s_mov_b32 s30, 99f-98b\n"
        "s_mov_b32 s31, 0x20000\n"
        "s_lshl3_add_u32 s64, s50, s48\n"
        "s_lshl_b32 s64, s64, 8\n"
        "v_lshl_add_u32 v122, v1, 7, s64\n"
        "s_mov_b32 m0, 162816\n"
        "s_mov_b64 exec, 3\n"
        "buffer_load_dword v122, s[28:31], 0 offen lds\n"
        "s_mov_b64 exec, -1\n"
        "s_add_u32 m0, s56, 14592\n"
        "s_mul_i32 s7, s72, 229376\n"
        "buffer_load_dwordx4 v4, s[16:19], s7 offen offset:1792 lds\n"
        "s_add_u32 m0, s56, 39168\n"
        "s_add_u32 s73, s57, 1\n"
        "s_and_b32 s73, s73, 7\n"
        "s_mul_i32 s7, s73, 229376\n"
        "buffer_load_dwordx4 v4, s[16:19], s7 offen offset:1792 lds\n"
        "s_mov_b32 s14, 1572864\n"
        "s_mov_b32 s15, 0x20000\n"
        "s_mov_b32 s22, 16252928\n"
        "s_mov_b32 s23, 0x20000\n"
        "s_mov_b32 s26, 8388608\n"
        "s_mov_b32 s27, 0x20000\n"
        "s_mov_b32 s34, 2097152\n"
        "s_mov_b32 s35, 0x20000\n"
        "s_mov_b32 s38, 4096\n"
        "s_mov_b32 s39, 0x20000\n"
        "s_mov_b64 s[40:41], s[20:21]\n"
        "s_mov_b32 s43, 0x20000\n"
        "s_mov_b32 s59, 0x20000\n"
        "s_mov_b32 s62, 0\n"
        "buffer_load_dword v3, off, s[20:23], s62 sc1\n"
        "s_lshl3_add_u32 s62, s6, s48\n"
        "s_lshl_b32 s64, s62, 8\n"
        "s_add_u32 s64, s64, 8064\n"
        "s_lshl_b32 s62, s62, 7\n"
        "s_add_u32 s62, s62, 786432\n"
        "v_lshl_add_u32 v122, v1, 7, s64\n"
        "v_writelane_b32 v122, s62, 0\n"
        "s_mov_b32 m0, 162816\n"
        "s_mov_b64 exec, 1\n"
        "buffer_load_dword v122, s[20:23], 0 offen sc1 lds\n"
        "s_mov_b64 exec, -1\n"
        "v_mul_u32_u24 v5, 0xc00, v121\n"
        "v_add_u32 v5, v5, v120\n"
        "s_lshl_b32 s64, s51, 5\n"
        "s_lshl_b32 s65, s52, 1\n"
        "v_lshrrev_b32 v8, 4, v1\n"
        "v_bfe_u32 v126, v1, 1, 3\n"
        "v_xor_b32 v8, v8, v126\n"
        "v_lshlrev_b32 v8, 4, v8\n"
        "v_and_b32 v127, 15, v1\n"
        "v_add_u32 v127, s64, v127\n"
        "v_lshl_add_u32 v8, v127, 7, v8\n"
        "v_xor_b32 v9, 64, v8\n"
        "v_bfe_u32 v127, v1, 2, 2\n"
        "v_lshrrev_b32 v126, 4, v1\n"
        "v_lshl_add_u32 v127, v126, 3, v127\n"
        "v_and_b32 v10, 3, v1\n"
        "v_lshlrev_b32 v10, 3, v10\n"
        "v_bfe_u32 v125, v1, 3, 2\n"
        "v_xor_b32 v125, s65, v125\n"
        "v_lshl_add_u32 v10, v125, 5, v10\n"
        "v_lshl_add_u32 v10, v127, 7, v10\n"
        "v_xor_b32 v11, 32, v10\n"
        "v_and_b32 v12, 3, v1\n"
        "v_lshlrev_b32 v12, 3, v12\n"
        "v_bfe_u32 v125, v1, 4, 1\n"
        "v_xor_b32 v125, s52, v125\n"
        "v_lshl_add_u32 v12, v125, 5, v12\n"
        "v_lshl_add_u32 v12, v127, 6, v12\n"
        "v_and_b32 v120, 2, v1\n"
        "v_cmp_eq_u32 vcc, 0, v120\n"
        "v_mov_b32 v121, 0x3f803f80\n"
        "s_nop 0\n"
        "v_cndmask_b32 v28, 0, v121, vcc\n"
        "v_mov_b32 v29, v28\n"
        "v_mov_b32 v30, v28\n"
        "v_mov_b32 v31, v28\n"
        "v_and_b32 v120, 1, v1\n"
        "v_mul_u32_u24 v120, 0xaaaa, v120\n"
        "v_or_b32 v32, 0x4444, v120\n"
        "v_mov_b32 v80, 0\n"
        "v_mov_b32 v81, 0\n"
        "v_mov_b32 v82, 0\n"
        "v_mov_b32 v83, 0\n"
        "v_mov_b32 v84, 0\n"
        "v_mov_b32 v85, 0\n"
        "v_mov_b32 v86, 0\n"
        "v_mov_b32 v87, 0\n"
        "v_mov_b32 v56, 0\n"
        "v_mov_b32 v57, 0\n"
        "v_mov_b32 v58, 0\n"
        "v_mov_b32 v59, 0\n"
        "s_lshl_b32 s64, s51, 4\n"
        "s_lshl_b32 s65, s54, 6\n"
        "s_add_u32 s65, s65, 2048\n"
        "v_lshrrev_b32 v6, 2, v1\n"
        "v_add_u32 v6, s64, v6\n"
        "v_mul_u32_u24 v6, 0xc00, v6\n"
        "v_and_b32 v125, 1, v1\n"
        "v_lshl_add_u32 v127, v125, 4, s65\n"
        "v_bfe_u32 v126, v1, 1, 1\n"
        "v_lshrrev_b32 v125, 5, v1\n"
        "v_xor_b32 v126, v126, v125\n"
        "v_lshl_add_u32 v127, v126, 5, v127\n"
        "v_add_u32 v6, v6, v127\n"
        "v_lshlrev_b32 v2, 4, v1\n"
        "v_and_b32 v13, 15, v1\n"
        "v_lshlrev_b32 v13, 4, v13\n"
        "v_lshrrev_b32 v126, 5, v1\n"
        "v_lshl_add_u32 v13, v126, 8, v13\n"
        "v_bfe_u32 v127, v1, 4, 1\n"
        "v_lshl_add_u32 v13, v127, 9, v13\n"
        "s_lshl_b32 s64, s51, 5\n"
        "v_and_b32 v120, 3, v1\n"
        "v_bfe_u32 v127, v1, 3, 1\n"
        "v_lshlrev_b32 v127, 2, v127\n"
        "v_bfe_u32 v126, v1, 2, 1\n"
        "v_lshl_or_b32 v127, v126, 3, v127\n"
        "v_or_b32 v120, v120, v127\n"
        "v_lshrrev_b32 v121, 4, v1\n"
        "v_lshlrev_b32 v121, 2, v121\n"
        "v_sub_u32 v120, v120, v121\n"
        "v_mov_b32 v122, 0x3f80\n"
        "v_mov_b32 v123, 1.0\n"
        "v_cmp_eq_u32 vcc, 0, v120\n"
        "s_nop 1\n"
        "v_cndmask_b32 v26, 0, v122, vcc\n"
        "v_cmp_eq_u32 vcc, 1, v120\n"
        "s_nop 1\n"
        "v_cndmask_b32 v26, v26, v123, vcc\n"
        "v_cmp_eq_u32 vcc, 2, v120\n"
        "s_nop 1\n"
        "v_cndmask_b32 v27, 0, v122, vcc\n"
        "v_cmp_eq_u32 vcc, 3, v120\n"
        "s_nop 1\n"
        "v_cndmask_b32 v27, v27, v123, vcc\n"
        "v_and_b32 v120, 15, v1\n"
        "v_sub_u32 v120, v120, v121\n"
        "v_mov_b32 v122, 0x3b000000\n"
        "v_cmp_eq_u32 vcc, 0, v120\n"
        "s_nop 1\n"
        "v_cndmask_b32 v72, 0, v122, vcc\n"
        "v_cmp_eq_u32 vcc, 1, v120\n"
        "s_nop 1\n"
        "v_cndmask_b32 v73, 0, v122, vcc\n"
        "v_cmp_eq_u32 vcc, 2, v120\n"
        "s_nop 1\n"
        "v_cndmask_b32 v74, 0, v122, vcc\n"
        "v_cmp_eq_u32 vcc, 3, v120\n"
        "s_nop 1\n"
        "v_cndmask_b32 v75, 0, v122, vcc\n"
        "s_lshl_b32 s65, s52, 2\n"
        "v_lshrrev_b32 v34, 4, v1\n"
        "v_add_u32 v34, s65, v34\n"
        "v_lshlrev_b32 v34, 2, v34\n"
        "v_add_u32 v34, 0x24000, v34\n"
        "v_and_b32 v127, 15, v1\n"
        "v_add_u32 v127, s64, v127\n"
        "v_lshl_add_u32 v34, v127, 5, v34\n"
        "v_and_b32 v35, 1, v0\n"
        "v_lshlrev_b32 v35, 12, v35\n"
        "v_add_u32 v35, 0x24000, v35\n"
        "v_bfe_u32 v127, v0, 1, 7\n"
        "v_lshl_add_u32 v35, v127, 5, v35\n"
        "v_lshrrev_b32 v36, 7, v0\n"
        "v_lshlrev_b32 v36, 3, v36\n"
        "v_add_u32 v36, 0x26000, v36\n"
        "v_and_b32 v127, 0x7f, v0\n"
        "v_lshl_add_u32 v36, v127, 5, v36\n"
        "v_and_b32 v37, 15, v1\n"
        "v_add_u32 v37, s64, v37\n"
        "v_lshlrev_b32 v37, 5, v37\n"
        "v_add_u32 v37, 0x26000, v37\n"
        "v_and_b32 v77, 15, v1\n"
        "v_add_u32 v77, s64, v77\n"
        "v_lshlrev_b32 v77, 3, v77\n"
        "v_add_u32 v77, 0x27400, v77\n"
        "s_lshl_b32 s62, s52, 7\n"
        "v_add_u32 v76, s62, v77\n"
        "v_bfe_u32 v120, v1, 2, 2\n"
        "v_lshrrev_b32 v121, 4, v1\n"
        "v_cmp_eq_u32 vcc, v120, v121\n"
        "v_mov_b32 v120, 162816\n"
        "s_nop 0\n"
        "v_cndmask_b32 v76, v120, v76, vcc\n"
        "s_lshl_b32 s63, s53, 15\n"
        "s_add_u32 s63, s63, 8192\n"
        "s_lshl_b32 s65, s54, 11\n"
        "s_add_u32 s62, s63, s65\n"
        "s_cmp_eq_u32 s52, 0\n"
        "s_cselect_b32 s62, s62, 2147483632\n"
        "v_lshl_add_u32 v14, v0, 4, s63\n"
        "v_and_b32 v127, 0xff, v0\n"
        "v_lshl_add_u32 v15, v127, 3, s62\n"
        "s_lshl2_add_u32 s55, s53, s51\n"
        "s_lshr_b32 s62, s55, 1\n"
        "s_and_b32 s63, s55, 1\n"
        "s_lshl2_add_u32 s64, s62, s63\n"
        "s_lshl_b32 s64, s64, 10\n"
        "s_add_u32 s64, s64, 1048576\n"
        "s_lshl1_add_u32 s65, s54, s52\n"
        "s_lshl_b32 s68, s65, 17\n"
        "s_add_u32 s68, s68, s64\n"
        "s_add_u32 s68, s68, 2048\n"
        "s_lshl_b32 s69, s65, 6\n"
        "s_add_u32 s69, s69, s55\n"
        "s_lshl_b32 s69, s69, 11\n"
        "s_add_u32 s69, s69, 5242880\n"
        "s_mul_i32 s70, s55, 44\n"
        "s_add_u32 s70, s70, s65\n"
        "s_lshl_b32 s70, s70, 11\n"
        "s_add_u32 s70, s70, 8945664\n"
        "s_and_b32 s65, s65, 15\n"
        "s_mul_i32 s62, s55, 90112\n"
        "s_add_u32 s62, s62, 8912896\n"
        "s_cmp_lt_u32 s54, 8\n"
        "s_cselect_b32 s62, s62, s64\n"
        "s_cselect_b32 s63, 10, 17\n"
        "s_cselect_b32 s71, 0x3eb8aa3b, 1.0\n"
        "s_lshl_b32 s63, 1, s63\n"
        "s_mul_i32 s65, s65, s63\n"
        "s_lshl1_add_u32 s66, s65, s62\n"
        "s_add_u32 s67, s66, s63\n"
        "s_lshr_b32 s62, s50, 3\n"
        "s_lshl2_add_u32 s84, s49, s62\n"
        "s_and_b32 s62, s50, 7\n"
        "s_lshl2_add_u32 s62, s62, s51\n"
        "s_lshl1_add_u32 s0, s62, s52\n"
        "s_and_b32 s62, s50, 3\n"
        "s_lshl2_add_u32 s5, s49, s62\n"
        "s_and_b32 s62, s50, 28\n"
        "s_cmp_lt_u32 s50, 28\n"
        "s_cselect_b32 s62, s62, 0\n"
        "s_cselect_b32 s81, 0, 384\n"
        "s_add_u32 s89, s62, s51\n"
        "s_mul_i32 s87, s52, 61440\n"
        "s_add_u32 s87, s87, 40960\n"
        "s_mul_i32 s62, s51, 3072\n"
        "s_add_u32 s80, s87, s62\n"
        "s_lshl_b32 s62, s52, 1\n"
        "s_xor_b32 s62, s62, s51\n"
        "s_lshl2_add_u32 s62, s5, s62\n"
        "s_lshl_b32 s62, s62, 5\n"
        "s_add_u32 s62, s62, s52\n"
        "s_lshl_b32 s62, s62, 11\n"
        "s_add_u32 s81, s81, s62\n"
        "s_lshl4_add_u32 s62, s53, s54\n"
        "s_lshl_b32 s82, s62, 7\n"
        "s_add_u32 s82, s82, 786432\n"
        "s_cmp_eq_u32 s48, 0\n"
        "s_cselect_b32 s82, s82, 2147483632\n"
        "s_lshl1_add_u32 s88, s62, s52\n"
        "s_lshl_b32 s88, s88, 7\n"
        "s_add_u32 s88, s88, 917504\n"
        "s_cmp_eq_u32 s51, 0\n"
        "s_cselect_b32 s88, s88, 2147483632\n"
        "s_lshr_b32 s58, s84, 1\n"
        "s_mul_i32 s63, s49, 0x110\n"
        "s_add_u32 s58, s58, s63\n"
        "s_lshr_b32 s63, s89, 2\n"
        "s_mul_i32 s63, s63, 0x22000\n"
        "s_add_u32 s58, s58, s63\n"
        "s_add_u32 s58, s58, 0x10800\n"
        "s_lshl1_add_u32 s64, s89, s52\n"
        "s_lshl_b32 s64, s64, 16\n"
        "s_add_u32 s64, s64, 5242880\n"
        "v_add_u32 v0, s64, v2\n"
        "s_lshl1_add_u32 s86, s5, s52\n"
        "s_mul_i32 s4, s86, 44\n"
        "s_add_u32 s4, s4, s89\n"
        "s_lshl_b32 s4, s4, 11\n"
        "s_add_u32 s4, s4, 8945664\n"
        "s_lshl_b32 s86, s86, 6\n"
        "s_mul_i32 s1, s0, 88\n"
        "s_add_u32 s1, s1, s84\n"
        "s_lshl_b32 s1, s1, 10\n"
        "s_add_u32 s1, s1, 8912896\n"
        "s_mul_i32 s62, s52, 180224\n"
        "s_add_u32 s0, s1, 90112\n"
        "s_sub_u32 s0, s0, s62\n"
        "s_lshl_b32 s84, s84, 17\n"
        "s_add_u32 s84, s84, 1048576\n"
        "s_lshl_b32 s62, s52, 6\n"
        "s_add_u32 s62, s62, s51\n"
        "s_lshl_b32 s62, s62, 10\n"
        "s_add_u32 s85, s62, s84\n"
        "s_sub_u32 s85, s85, 2048\n"
        "s_lshr_b32 s62, s5, 1\n"
        "s_lshl_b32 s62, s62, 5\n"
        "s_add_u32 s89, s62, s89\n"
        "s_lshl_b32 s89, s89, 7\n"
        "s_add_u32 s89, s89, 917504\n"
        "s_mov_b32 s60, 0xffff\n"
        "s_mov_b32 s61, 0xffff\n"
        "s_mov_b64 s[28:29], s[44:45]\n"
        "s_mov_b32 s30, 524288\n"
        "s_mov_b32 s31, 0x20000\n"
        "s_lshl_b32 s55, s48, 12\n"
        "s_lshr_b32 s62, s50, 2\n"
        "s_lshl3_add_u32 s44, s62, s48\n"
        "s_mul_i32 s44, s44, 22528\n"
        "s_add_u32 s44, s44, 14680064\n"
        "s_lshr_b32 s63, s62, 1\n"
        "s_lshr_b32 s64, s48, 1\n"
        "s_lshl2_add_u32 s63, s63, s64\n"
        "s_and_b32 s62, s62, 1\n"
        "s_lshl4_add_u32 s63, s63, s62\n"
        "s_and_b32 s64, s48, 1\n"
        "s_lshl1_add_u32 s63, s63, s64\n"
        "s_lshl_b32 s45, s63, 7\n"
        "s_add_u32 s45, s45, 921088\n"
        "s_lshl_b32 s63, s48, 12\n"
        "s_add_u32 s63, s63, 786432\n"
        "v_lshrrev_b32 v120, 1, v1\n"
        "v_min_u32 v120, 4, v120\n"
        "v_lshlrev_b32 v120, 2, v120\n"
        "v_bfe_u32 v120, s58, v120, 4\n"
        "v_and_b32 v126, 1, v1\n"
        "v_lshl_add_u32 v33, v126, 11, s63\n"
        "v_lshl_add_u32 v33, v120, 7, v33\n"
        "s_cmp_eq_u32 s52, 1\n"
        "s_cselect_b64 vcc, -1, 0\n"
        "s_add_u32 s74, s57, 2\n"
        "s_and_b32 s74, s74, 7\n"
        "s_add_u32 s75, s57, 3\n"
        "s_and_b32 s75, s75, 7\n"
        "s_add_u32 s76, s57, 4\n"
        "s_and_b32 s76, s76, 7\n"
        "s_add_u32 s77, s57, 5\n"
        "s_and_b32 s77, s77, 7\n"
        "s_add_u32 s78, s57, 6\n"
        "s_and_b32 s78, s78, 7\n"
        "s_add_u32 s79, s57, 7\n"
        "s_and_b32 s79, s79, 7\n"
        "s_add_u32 m0, s56, 24576\n"
        "s_lshl_b32 s83, s73, 7\n"
        "buffer_load_dwordx4 v7, s[8:11], s83 offen lds\n"
        "s_add_u32 m0, s56, 32768\n"
        "s_add_u32 s83, s83, 65536\n"
        "buffer_load_dwordx4 v7, s[8:11], s83 offen lds\n"
        "s_add_u32 m0, s56, 49152\n"
        "s_lshl_b32 s83, s74, 7\n"
        "buffer_load_dwordx4 v7, s[8:11], s83 offen lds\n"
        "s_add_u32 m0, s56, 57344\n"
        "s_add_u32 s83, s83, 65536\n"
        "buffer_load_dwordx4 v7, s[8:11], s83 offen lds\n"
        "s_add_u32 m0, s56, 63744\n"
        "s_mul_i32 s7, s74, 229376\n"
        "buffer_load_dwordx4 v4, s[16:19], s7 offen offset:1792 lds\n"
        "s_cbranch_vccnz 101f\n"
        "s_waitcnt vmcnt(8)\n"
        "s_barrier\n"
        "s_setprio 1\n"
        "ds_read_b128 v[128:131], v8 offset:0\n"
        "ds_read_b128 v[192:195], v8 offset:2048\n"
        "ds_read_b64_tr_b16 v[88:89], v10 offset:16384\n"
        "ds_read_b64_tr_b16 v[90:91], v10 offset:16896\n"
        "ds_read_b64_tr_b16 v[96:97], v11 offset:16384\n"
        "ds_read_b64_tr_b16 v[98:99], v11 offset:16896\n"
        "ds_read_b128 v[132:135], v9 offset:0\n"
        "ds_read_b128 v[196:199], v9 offset:2048\n"
        "ds_read_b64_tr_b16 v[92:93], v10 offset:20480\n"
        "ds_read_b64_tr_b16 v[94:95], v10 offset:20992\n"
        "ds_read_b64_tr_b16 v[100:101], v11 offset:20480\n"
        "ds_read_b64_tr_b16 v[102:103], v11 offset:20992\n"
        "s_add_u32 m0, s56, 73728\n"
        "s_lshl_b32 s83, s75, 7\n"
        "buffer_load_dwordx4 v7, s[8:11], s83 offen lds\n"
        "s_add_u32 m0, s56, 81920\n"
        "s_add_u32 s83, s83, 65536\n"
        "buffer_load_dwordx4 v7, s[8:11], s83 offen lds\n"
        "s_add_u32 m0, s56, 88320\n"
        "s_mul_i32 s7, s75, 229376\n"
        "buffer_load_dwordx4 v4, s[16:19], s7 offen offset:1792 lds\n"
        "s_waitcnt lgkmcnt(0)\n"
        "s_setprio 0\n"
        "v_mfma_f32_16x16x32_bf16 v[40:43], v[88:91], v[128:131], 0\n"
        "v_mfma_f32_16x16x32_bf16 v[44:47], v[88:91], v[192:195], 0\n"
        "v_mfma_f32_16x16x32_bf16 v[48:51], v[96:99], v[128:131], 0\n"
        "v_mfma_f32_16x16x32_bf16 v[52:55], v[96:99], v[192:195], 0\n"
        "v_mfma_f32_16x16x32_bf16 v[40:43], v[92:95], v[132:135], v[40:43]\n"
        "v_mfma_f32_16x16x32_bf16 v[44:47], v[92:95], v[196:199], v[44:47]\n"
        "v_mfma_f32_16x16x32_bf16 v[48:51], v[100:103], v[132:135], v[48:51]\n"
        "v_mfma_f32_16x16x32_bf16 v[52:55], v[100:103], v[196:199], v[52:55]\n"
        "v_mfma_f32_16x16x32_bf16 v[60:63], v[128:131], v[128:131], 0\n"
        "v_mfma_f32_16x16x32_bf16 v[60:63], v[132:135], v[132:135], v[60:63]\n"
        "v_smfmac_f32_16x16x64_bf16 v[56:59], v[28:31], v[128:135], v32\n"
        "v_smfmac_f32_16x16x64_bf16 v[80:83], v[28:31], v[88:95], v32\n"
        "v_smfmac_f32_16x16x64_bf16 v[84:87], v[28:31], v[96:103], v32\n"
        "s_waitcnt vmcnt(6)\n"
        "s_barrier\n"
        "s_setprio 1\n"
        "ds_read_b128 v[136:139], v8 offset:24576\n"
        "ds_read_b128 v[200:203], v8 offset:26624\n"
        "ds_read_b64_tr_b16 v[104:105], v10 offset:40960\n"
        "ds_read_b64_tr_b16 v[106:107], v10 offset:41472\n"
        "ds_read_b64_tr_b16 v[112:113], v11 offset:40960\n"
        "ds_read_b64_tr_b16 v[114:115], v11 offset:41472\n"
        "ds_read_b128 v[140:143], v9 offset:24576\n"
        "ds_read_b128 v[204:207], v9 offset:26624\n"
        "ds_read_b64_tr_b16 v[108:109], v10 offset:45056\n"
        "ds_read_b64_tr_b16 v[110:111], v10 offset:45568\n"
        "ds_read_b64_tr_b16 v[116:117], v11 offset:45056\n"
        "ds_read_b64_tr_b16 v[118:119], v11 offset:45568\n"
        "s_add_u32 m0, s56, 98304\n"
        "s_lshl_b32 s83, s76, 7\n"
        "buffer_load_dwordx4 v7, s[8:11], s83 offen lds\n"
        "s_add_u32 m0, s56, 106496\n"
        "s_add_u32 s83, s83, 65536\n"
        "buffer_load_dwordx4 v7, s[8:11], s83 offen lds\n"
        "s_add_u32 m0, s56, 112896\n"
        "s_mul_i32 s7, s76, 229376\n"
        "buffer_load_dwordx4 v4, s[16:19], s7 offen offset:1792 lds\n"
        "s_waitcnt lgkmcnt(0)\n"
        "s_setprio 0\n"
        "v_mfma_f32_16x16x32_bf16 v[40:43], v[104:107], v[136:139], v[40:43]\n"
        "v_mfma_f32_16x16x32_bf16 v[44:47], v[104:107], v[200:203], v[44:47]\n"
        "v_mfma_f32_16x16x32_bf16 v[48:51], v[112:115], v[136:139], v[48:51]\n"
        "v_mfma_f32_16x16x32_bf16 v[52:55], v[112:115], v[200:203], v[52:55]\n"
        "v_mfma_f32_16x16x32_bf16 v[40:43], v[108:111], v[140:143], v[40:43]\n"
        "v_mfma_f32_16x16x32_bf16 v[44:47], v[108:111], v[204:207], v[44:47]\n"
        "v_mfma_f32_16x16x32_bf16 v[48:51], v[116:119], v[140:143], v[48:51]\n"
        "v_mfma_f32_16x16x32_bf16 v[52:55], v[116:119], v[204:207], v[52:55]\n"
        "v_mfma_f32_16x16x32_bf16 v[60:63], v[136:139], v[136:139], v[60:63]\n"
        "v_mfma_f32_16x16x32_bf16 v[60:63], v[140:143], v[140:143], v[60:63]\n"
        "v_smfmac_f32_16x16x64_bf16 v[56:59], v[28:31], v[136:143], v32\n"
        "v_smfmac_f32_16x16x64_bf16 v[80:83], v[28:31], v[104:111], v32\n"
        "v_smfmac_f32_16x16x64_bf16 v[84:87], v[28:31], v[112:119], v32\n"
        "s_waitcnt vmcnt(3)\n"
        "s_barrier\n"
        "s_setprio 1\n"
        "v_add_u32 v10, 65536, v10\n"
        "v_add_u32 v11, 65536, v11\n"
        "ds_read_b128 v[144:147], v8 offset:49152\n"
        "ds_read_b128 v[208:211], v8 offset:51200\n"
        "ds_read_b64_tr_b16 v[88:89], v10 offset:0\n"
        "ds_read_b64_tr_b16 v[90:91], v10 offset:512\n"
        "ds_read_b64_tr_b16 v[96:97], v11 offset:0\n"
        "ds_read_b64_tr_b16 v[98:99], v11 offset:512\n"
        "ds_read_b128 v[148:151], v9 offset:49152\n"
        "ds_read_b128 v[212:215], v9 offset:51200\n"
        "ds_read_b64_tr_b16 v[92:93], v10 offset:4096\n"
        "ds_read_b64_tr_b16 v[94:95], v10 offset:4608\n"
        "ds_read_b64_tr_b16 v[100:101], v11 offset:4096\n"
        "ds_read_b64_tr_b16 v[102:103], v11 offset:4608\n"
        "s_add_u32 m0, s56, 122880\n"
        "s_lshl_b32 s83, s77, 7\n"
        "buffer_load_dwordx4 v7, s[8:11], s83 offen lds\n"
        "s_add_u32 m0, s56, 131072\n"
        "s_add_u32 s83, s83, 65536\n"
        "buffer_load_dwordx4 v7, s[8:11], s83 offen lds\n"
        "s_add_u32 m0, s56, 137472\n"
        "s_mul_i32 s7, s77, 229376\n"
        "buffer_load_dwordx4 v4, s[16:19], s7 offen offset:1792 lds\n"
        "s_waitcnt lgkmcnt(0)\n"
        "s_setprio 0\n"
        "v_mfma_f32_16x16x32_bf16 v[40:43], v[88:91], v[144:147], v[40:43]\n"
        "v_mfma_f32_16x16x32_bf16 v[44:47], v[88:91], v[208:211], v[44:47]\n"
        "v_mfma_f32_16x16x32_bf16 v[48:51], v[96:99], v[144:147], v[48:51]\n"
        "v_mfma_f32_16x16x32_bf16 v[52:55], v[96:99], v[208:211], v[52:55]\n"
        "v_mfma_f32_16x16x32_bf16 v[40:43], v[92:95], v[148:151], v[40:43]\n"
        "v_mfma_f32_16x16x32_bf16 v[44:47], v[92:95], v[212:215], v[44:47]\n"
        "v_mfma_f32_16x16x32_bf16 v[48:51], v[100:103], v[148:151], v[48:51]\n"
        "v_mfma_f32_16x16x32_bf16 v[52:55], v[100:103], v[212:215], v[52:55]\n"
        "v_mfma_f32_16x16x32_bf16 v[60:63], v[144:147], v[144:147], v[60:63]\n"
        "v_mfma_f32_16x16x32_bf16 v[60:63], v[148:151], v[148:151], v[60:63]\n"
        "v_smfmac_f32_16x16x64_bf16 v[56:59], v[28:31], v[144:151], v32\n"
        "v_smfmac_f32_16x16x64_bf16 v[80:83], v[28:31], v[88:95], v32\n"
        "v_smfmac_f32_16x16x64_bf16 v[84:87], v[28:31], v[96:103], v32\n"
        "s_setprio 1\n"
        "v_add_u32 v8, 73728, v8\n"
        "v_add_u32 v9, 73728, v9\n"
        "ds_read_b128 v[152:155], v8 offset:0\n"
        "ds_read_b128 v[216:219], v8 offset:2048\n"
        "ds_read_b64_tr_b16 v[104:105], v10 offset:24576\n"
        "ds_read_b64_tr_b16 v[106:107], v10 offset:25088\n"
        "ds_read_b64_tr_b16 v[112:113], v11 offset:24576\n"
        "ds_read_b64_tr_b16 v[114:115], v11 offset:25088\n"
        "ds_read_b128 v[156:159], v9 offset:0\n"
        "ds_read_b128 v[220:223], v9 offset:2048\n"
        "ds_read_b64_tr_b16 v[108:109], v10 offset:28672\n"
        "ds_read_b64_tr_b16 v[110:111], v10 offset:29184\n"
        "ds_read_b64_tr_b16 v[116:117], v11 offset:28672\n"
        "ds_read_b64_tr_b16 v[118:119], v11 offset:29184\n"
        "s_add_u32 m0, s56, 0\n"
        "s_lshl_b32 s83, s78, 7\n"
        "buffer_load_dwordx4 v7, s[8:11], s83 offen lds\n"
        "s_add_u32 m0, s56, 8192\n"
        "s_add_u32 s83, s83, 65536\n"
        "buffer_load_dwordx4 v7, s[8:11], s83 offen lds\n"
        "s_add_u32 m0, s56, 14592\n"
        "s_mul_i32 s7, s78, 229376\n"
        "buffer_load_dwordx4 v4, s[16:19], s7 offen offset:1792 lds\n"
        "s_waitcnt lgkmcnt(0)\n"
        "s_setprio 0\n"
        "v_mfma_f32_16x16x32_bf16 v[40:43], v[104:107], v[152:155], v[40:43]\n"
        "v_mfma_f32_16x16x32_bf16 v[44:47], v[104:107], v[216:219], v[44:47]\n"
        "v_mfma_f32_16x16x32_bf16 v[48:51], v[112:115], v[152:155], v[48:51]\n"
        "v_mfma_f32_16x16x32_bf16 v[52:55], v[112:115], v[216:219], v[52:55]\n"
        "v_mfma_f32_16x16x32_bf16 v[40:43], v[108:111], v[156:159], v[40:43]\n"
        "v_mfma_f32_16x16x32_bf16 v[44:47], v[108:111], v[220:223], v[44:47]\n"
        "v_mfma_f32_16x16x32_bf16 v[48:51], v[116:119], v[156:159], v[48:51]\n"
        "v_mfma_f32_16x16x32_bf16 v[52:55], v[116:119], v[220:223], v[52:55]\n"
        "v_mfma_f32_16x16x32_bf16 v[60:63], v[152:155], v[152:155], v[60:63]\n"
        "v_mfma_f32_16x16x32_bf16 v[60:63], v[156:159], v[156:159], v[60:63]\n"
        "v_smfmac_f32_16x16x64_bf16 v[56:59], v[28:31], v[152:159], v32\n"
        "v_smfmac_f32_16x16x64_bf16 v[80:83], v[28:31], v[104:111], v32\n"
        "v_smfmac_f32_16x16x64_bf16 v[84:87], v[28:31], v[112:119], v32\n"
        "s_waitcnt vmcnt(3)\n"
        "s_barrier\n"
        "s_setprio 1\n"
        "ds_read_b128 v[160:163], v8 offset:24576\n"
        "ds_read_b128 v[224:227], v8 offset:26624\n"
        "ds_read_b64_tr_b16 v[88:89], v10 offset:49152\n"
        "ds_read_b64_tr_b16 v[90:91], v10 offset:49664\n"
        "ds_read_b64_tr_b16 v[96:97], v11 offset:49152\n"
        "ds_read_b64_tr_b16 v[98:99], v11 offset:49664\n"
        "ds_read_b128 v[164:167], v9 offset:24576\n"
        "ds_read_b128 v[228:231], v9 offset:26624\n"
        "ds_read_b64_tr_b16 v[92:93], v10 offset:53248\n"
        "ds_read_b64_tr_b16 v[94:95], v10 offset:53760\n"
        "ds_read_b64_tr_b16 v[100:101], v11 offset:53248\n"
        "ds_read_b64_tr_b16 v[102:103], v11 offset:53760\n"
        "s_add_u32 m0, s56, 24576\n"
        "s_lshl_b32 s83, s79, 7\n"
        "buffer_load_dwordx4 v7, s[8:11], s83 offen lds\n"
        "s_add_u32 m0, s56, 32768\n"
        "s_add_u32 s83, s83, 65536\n"
        "buffer_load_dwordx4 v7, s[8:11], s83 offen lds\n"
        "s_add_u32 m0, s56, 39168\n"
        "s_mul_i32 s7, s79, 229376\n"
        "buffer_load_dwordx4 v4, s[16:19], s7 offen offset:1792 lds\n"
        "s_waitcnt lgkmcnt(0)\n"
        "s_setprio 0\n"
        "v_mfma_f32_16x16x32_bf16 v[40:43], v[88:91], v[160:163], v[40:43]\n"
        "v_mfma_f32_16x16x32_bf16 v[44:47], v[88:91], v[224:227], v[44:47]\n"
        "v_mfma_f32_16x16x32_bf16 v[48:51], v[96:99], v[160:163], v[48:51]\n"
        "v_mfma_f32_16x16x32_bf16 v[52:55], v[96:99], v[224:227], v[52:55]\n"
        "v_mfma_f32_16x16x32_bf16 v[40:43], v[92:95], v[164:167], v[40:43]\n"
        "v_mfma_f32_16x16x32_bf16 v[44:47], v[92:95], v[228:231], v[44:47]\n"
        "v_mfma_f32_16x16x32_bf16 v[48:51], v[100:103], v[164:167], v[48:51]\n"
        "v_mfma_f32_16x16x32_bf16 v[52:55], v[100:103], v[228:231], v[52:55]\n"
        "v_mfma_f32_16x16x32_bf16 v[60:63], v[160:163], v[160:163], v[60:63]\n"
        "v_mfma_f32_16x16x32_bf16 v[60:63], v[164:167], v[164:167], v[60:63]\n"
        "v_smfmac_f32_16x16x64_bf16 v[56:59], v[28:31], v[160:167], v32\n"
        "v_smfmac_f32_16x16x64_bf16 v[80:83], v[28:31], v[88:95], v32\n"
        "v_smfmac_f32_16x16x64_bf16 v[84:87], v[28:31], v[96:103], v32\n"
        "s_setprio 1\n"
        "v_add_u32 v10, 73728, v10\n"
        "v_add_u32 v11, 73728, v11\n"
        "ds_read_b128 v[168:171], v8 offset:49152\n"
        "ds_read_b128 v[232:235], v8 offset:51200\n"
        "ds_read_b64_tr_b16 v[104:105], v10 offset:0\n"
        "ds_read_b64_tr_b16 v[106:107], v10 offset:512\n"
        "ds_read_b64_tr_b16 v[112:113], v11 offset:0\n"
        "ds_read_b64_tr_b16 v[114:115], v11 offset:512\n"
        "ds_read_b128 v[172:175], v9 offset:49152\n"
        "ds_read_b128 v[236:239], v9 offset:51200\n"
        "ds_read_b64_tr_b16 v[108:109], v10 offset:4096\n"
        "ds_read_b64_tr_b16 v[110:111], v10 offset:4608\n"
        "ds_read_b64_tr_b16 v[116:117], v11 offset:4096\n"
        "ds_read_b64_tr_b16 v[118:119], v11 offset:4608\n"
        "s_add_u32 m0, s56, 49152\n"
        "s_mul_i32 s7, s72, 196608\n"
        "buffer_load_dwordx4 v5, s[12:15], s7 offen lds\n"
        "s_add_u32 m0, s56, 57344\n"
        "s_mul_i32 s7, s73, 196608\n"
        "buffer_load_dwordx4 v5, s[12:15], s7 offen lds\n"
        "s_waitcnt lgkmcnt(0)\n"
        "s_setprio 0\n"
        "v_mfma_f32_16x16x32_bf16 v[40:43], v[104:107], v[168:171], v[40:43]\n"
        "v_mfma_f32_16x16x32_bf16 v[44:47], v[104:107], v[232:235], v[44:47]\n"
        "v_mfma_f32_16x16x32_bf16 v[48:51], v[112:115], v[168:171], v[48:51]\n"
        "v_mfma_f32_16x16x32_bf16 v[52:55], v[112:115], v[232:235], v[52:55]\n"
        "v_mfma_f32_16x16x32_bf16 v[40:43], v[108:111], v[172:175], v[40:43]\n"
        "v_mfma_f32_16x16x32_bf16 v[44:47], v[108:111], v[236:239], v[44:47]\n"
        "v_mfma_f32_16x16x32_bf16 v[48:51], v[116:119], v[172:175], v[48:51]\n"
        "v_mfma_f32_16x16x32_bf16 v[52:55], v[116:119], v[236:239], v[52:55]\n"
        "v_mfma_f32_16x16x32_bf16 v[60:63], v[168:171], v[168:171], v[60:63]\n"
        "v_mfma_f32_16x16x32_bf16 v[60:63], v[172:175], v[172:175], v[60:63]\n"
        "v_smfmac_f32_16x16x64_bf16 v[56:59], v[28:31], v[168:175], v32\n"
        "v_smfmac_f32_16x16x64_bf16 v[80:83], v[28:31], v[104:111], v32\n"
        "v_smfmac_f32_16x16x64_bf16 v[84:87], v[28:31], v[112:119], v32\n"
        "s_waitcnt vmcnt(2)\n"
        "s_barrier\n"
        "s_setprio 1\n"
        "v_subrev_u32 v10, 122880, v10\n"
        "v_subrev_u32 v11, 122880, v11\n"
        "v_subrev_u32 v8, 73728, v8\n"
        "v_subrev_u32 v9, 73728, v9\n"
        "ds_read_b128 v[176:179], v8 offset:0\n"
        "ds_read_b128 v[240:243], v8 offset:2048\n"
        "ds_read_b64_tr_b16 v[88:89], v10 offset:0\n"
        "ds_read_b64_tr_b16 v[90:91], v10 offset:512\n"
        "ds_read_b64_tr_b16 v[96:97], v11 offset:0\n"
        "ds_read_b64_tr_b16 v[98:99], v11 offset:512\n"
        "ds_read_b128 v[180:183], v9 offset:0\n"
        "ds_read_b128 v[244:247], v9 offset:2048\n"
        "ds_read_b64_tr_b16 v[92:93], v10 offset:4096\n"
        "ds_read_b64_tr_b16 v[94:95], v10 offset:4608\n"
        "ds_read_b64_tr_b16 v[100:101], v11 offset:4096\n"
        "ds_read_b64_tr_b16 v[102:103], v11 offset:4608\n"
        "s_add_u32 m0, s56, 114688\n"
        "s_cmp_eq_u32 s52, 0\n"
        "s_cselect_b32 s7, s72, s73\n"
        "s_mul_i32 s7, s7, 196608\n"
        "buffer_load_dwordx4 v6, s[12:15], s7 offen lds\n"
        "s_add_u32 m0, s56, 65536\n"
        "s_mul_i32 s7, s74, 196608\n"
        "buffer_load_dwordx4 v5, s[12:15], s7 offen lds\n"
        "s_waitcnt lgkmcnt(0)\n"
        "s_setprio 0\n"
        "v_mfma_f32_16x16x32_bf16 v[40:43], v[88:91], v[176:179], v[40:43]\n"
        "v_mfma_f32_16x16x32_bf16 v[44:47], v[88:91], v[240:243], v[44:47]\n"
        "v_mfma_f32_16x16x32_bf16 v[48:51], v[96:99], v[176:179], v[48:51]\n"
        "v_mfma_f32_16x16x32_bf16 v[52:55], v[96:99], v[240:243], v[52:55]\n"
        "v_mfma_f32_16x16x32_bf16 v[40:43], v[92:95], v[180:183], v[40:43]\n"
        "v_mfma_f32_16x16x32_bf16 v[44:47], v[92:95], v[244:247], v[44:47]\n"
        "v_mfma_f32_16x16x32_bf16 v[48:51], v[100:103], v[180:183], v[48:51]\n"
        "v_mfma_f32_16x16x32_bf16 v[52:55], v[100:103], v[244:247], v[52:55]\n"
        "v_mfma_f32_16x16x32_bf16 v[60:63], v[176:179], v[176:179], v[60:63]\n"
        "v_mfma_f32_16x16x32_bf16 v[60:63], v[180:183], v[180:183], v[60:63]\n"
        "v_smfmac_f32_16x16x64_bf16 v[56:59], v[28:31], v[176:183], v32\n"
        "v_smfmac_f32_16x16x64_bf16 v[80:83], v[28:31], v[88:95], v32\n"
        "v_smfmac_f32_16x16x64_bf16 v[84:87], v[28:31], v[96:103], v32\n"
        "s_setprio 1\n"
        "ds_read_b128 v[184:187], v8 offset:24576\n"
        "ds_read_b128 v[248:251], v8 offset:26624\n"
        "ds_read_b64_tr_b16 v[104:105], v10 offset:24576\n"
        "ds_read_b64_tr_b16 v[106:107], v10 offset:25088\n"
        "ds_read_b64_tr_b16 v[112:113], v11 offset:24576\n"
        "ds_read_b64_tr_b16 v[114:115], v11 offset:25088\n"
        "ds_read_b128 v[188:191], v9 offset:24576\n"
        "ds_read_b128 v[252:255], v9 offset:26624\n"
        "ds_read_b64_tr_b16 v[108:109], v10 offset:28672\n"
        "ds_read_b64_tr_b16 v[110:111], v10 offset:29184\n"
        "ds_read_b64_tr_b16 v[116:117], v11 offset:28672\n"
        "ds_read_b64_tr_b16 v[118:119], v11 offset:29184\n"
        "s_waitcnt lgkmcnt(0)\n"
        "s_setprio 0\n"
        "v_mfma_f32_16x16x32_bf16 v[60:63], v[184:187], v[184:187], v[60:63]\n"
        "v_mfma_f32_16x16x32_bf16 v[60:63], v[188:191], v[188:191], v[60:63]\n"
        "v_smfmac_f32_16x16x64_bf16 v[56:59], v[28:31], v[184:191], v32\n"
        "v_smfmac_f32_16x16x64_bf16 v[80:83], v[28:31], v[104:111], v32\n"
        "v_smfmac_f32_16x16x64_bf16 v[84:87], v[28:31], v[112:119], v32\n"
        "v_mfma_f32_16x16x32_bf16 v[40:43], v[104:107], v[184:187], v[40:43]\n"
        "v_mfma_f32_16x16x32_bf16 v[44:47], v[104:107], v[248:251], v[44:47]\n"
        "v_mfma_f32_16x16x32_bf16 v[48:51], v[112:115], v[184:187], v[48:51]\n"
        "v_mfma_f32_16x16x32_bf16 v[52:55], v[112:115], v[248:251], v[52:55]\n"
        "v_mfma_f32_16x16x32_bf16 v[40:43], v[108:111], v[188:191], v[40:43]\n"
        "v_mfma_f32_16x16x32_bf16 v[44:47], v[108:111], v[252:255], v[44:47]\n"
        "v_mfma_f32_16x16x32_bf16 v[48:51], v[116:119], v[188:191], v[48:51]\n"
        "v_mfma_f32_16x16x32_bf16 v[52:55], v[116:119], v[252:255], v[52:55]\n"
        "v_mul_f32 v59, v60, v72\n"
        "v_fmac_f32 v59, v61, v73\n"
        "v_fmac_f32 v59, v62, v74\n"
        "v_fmac_f32 v59, v63, v75\n"
        "v_add_f32 v58, v56, v57\n"
        "ds_write_b64 v76, v[58:59]\n"
        "s_waitcnt lgkmcnt(0)\n"
        "s_barrier\n"
        "ds_read_b64 v[120:121], v77 offset:0\n"
        "ds_read_b64 v[122:123], v77 offset:128\n"
        "v_add_u32 v3, 1, v3\n"
        "s_waitcnt lgkmcnt(0)\n"
        "v_mul_f32 v18, 0xba000000, v120\n"
        "v_mul_f32 v19, 0xba000000, v122\n"
        "v_mul_f32 v120, 0x3b000000, v120\n"
        "v_mul_f32 v122, 0x3b000000, v122\n"
        "v_add_f32 v82, v80, v81\n"
        "v_add_f32 v86, v84, v85\n"
        "v_mov_b32 v80, 0\n"
        "v_mov_b32 v81, 0\n"
        "v_mov_b32 v84, 0\n"
        "v_mov_b32 v85, 0\n"
        "v_mfma_f32_16x16x4_f32 v[40:43], v82, v18, v[40:43]\n"
        "v_mfma_f32_16x16x4_f32 v[44:47], v82, v19, v[44:47]\n"
        "v_mfma_f32_16x16x4_f32 v[48:51], v86, v18, v[48:51]\n"
        "v_mfma_f32_16x16x4_f32 v[52:55], v86, v19, v[52:55]\n"
        "v_fma_f32 v24, -v120, v120, v121\n"
        "v_fma_f32 v25, -v122, v122, v123\n"
        "v_max_f32 v24, 0, v24\n"
        "v_max_f32 v25, 0, v25\n"
        "v_add_f32 v24, 0x3727c5ac, v24\n"
        "v_add_f32 v25, 0x3727c5ac, v25\n"
        "v_rsq_f32 v16, v24\n"
        "v_rsq_f32 v17, v25\n"
        "v_mul_f32 v20, 0xc0135761, v16\n"
        "v_mul_f32 v21, 0xc0135761, v17\n"
        "v_pk_mul_f32 v[22:23], v[16:17], v[16:17]\n"
        "v_pk_mul_f32 v[24:25], v[24:25], v[16:17]\n"
        "v_pk_mul_f32 v[22:23], v[22:23], v[20:21]\n"
        "v_mul_f32 v38, s71, v16\n"
        "v_mul_f32 v39, s71, v17\n"
        "v_mul_f32 v22, 0x3d372713, v22\n"
        "v_mul_f32 v23, 0x3d372713, v23\n"
        "v_pk_mul_f32 v[120:121], v[40:41], v[40:41]\n"
        "v_pk_mul_f32 v[122:123], v[42:43], v[42:43]\n"
        "v_pk_mul_f32 v[124:125], v[44:45], v[44:45]\n"
        "v_pk_mul_f32 v[126:127], v[46:47], v[46:47]\n"
        "v_pk_fma_f32 v[120:121], v[22:23], v[120:121], v[20:21] op_sel:[0,0,0] op_sel_hi:[0,1,0]\n"
        "v_pk_fma_f32 v[122:123], v[22:23], v[122:123], v[20:21] op_sel:[0,0,0] op_sel_hi:[0,1,0]\n"
        "v_pk_fma_f32 v[124:125], v[22:23], v[124:125], v[20:21] op_sel:[1,0,1] op_sel_hi:[1,1,1]\n"
        "v_pk_fma_f32 v[126:127], v[22:23], v[126:127], v[20:21] op_sel:[1,0,1] op_sel_hi:[1,1,1]\n"
        "v_pk_mul_f32 v[120:121], v[120:121], v[40:41]\n"
        "v_pk_mul_f32 v[122:123], v[122:123], v[42:43]\n"
        "v_pk_mul_f32 v[124:125], v[124:125], v[44:45]\n"
        "v_pk_mul_f32 v[126:127], v[126:127], v[46:47]\n"
        "v_exp_f32 v120, v120\n"
        "v_exp_f32 v121, v121\n"
        "v_exp_f32 v122, v122\n"
        "v_exp_f32 v123, v123\n"
        "v_exp_f32 v124, v124\n"
        "v_exp_f32 v125, v125\n"
        "v_exp_f32 v126, v126\n"
        "v_exp_f32 v127, v127\n"
        "v_pk_fma_f32 v[120:121], v[24:25], v[120:121], v[24:25] op_sel:[0,0,0] op_sel_hi:[0,1,0]\n"
        "v_pk_fma_f32 v[122:123], v[24:25], v[122:123], v[24:25] op_sel:[0,0,0] op_sel_hi:[0,1,0]\n"
        "v_pk_fma_f32 v[124:125], v[24:25], v[124:125], v[24:25] op_sel:[1,0,1] op_sel_hi:[1,1,1]\n"
        "v_pk_fma_f32 v[126:127], v[24:25], v[126:127], v[24:25] op_sel:[1,0,1] op_sel_hi:[1,1,1]\n"
        "v_rcp_f32 v120, v120\n"
        "v_rcp_f32 v121, v121\n"
        "v_rcp_f32 v122, v122\n"
        "v_rcp_f32 v123, v123\n"
        "v_rcp_f32 v124, v124\n"
        "v_rcp_f32 v125, v125\n"
        "v_rcp_f32 v126, v126\n"
        "v_rcp_f32 v127, v127\n"
        "v_pk_mul_f32 v[40:41], v[40:41], v[120:121]\n"
        "v_pk_mul_f32 v[42:43], v[42:43], v[122:123]\n"
        "v_pk_mul_f32 v[44:45], v[44:45], v[124:125]\n"
        "v_pk_mul_f32 v[46:47], v[46:47], v[126:127]\n"
        "v_pk_mul_f32 v[120:121], v[48:49], v[48:49]\n"
        "v_pk_mul_f32 v[122:123], v[50:51], v[50:51]\n"
        "v_pk_mul_f32 v[124:125], v[52:53], v[52:53]\n"
        "v_pk_mul_f32 v[126:127], v[54:55], v[54:55]\n"
        "v_pk_fma_f32 v[120:121], v[22:23], v[120:121], v[20:21] op_sel:[0,0,0] op_sel_hi:[0,1,0]\n"
        "v_pk_fma_f32 v[122:123], v[22:23], v[122:123], v[20:21] op_sel:[0,0,0] op_sel_hi:[0,1,0]\n"
        "v_pk_fma_f32 v[124:125], v[22:23], v[124:125], v[20:21] op_sel:[1,0,1] op_sel_hi:[1,1,1]\n"
        "v_pk_fma_f32 v[126:127], v[22:23], v[126:127], v[20:21] op_sel:[1,0,1] op_sel_hi:[1,1,1]\n"
        "v_pk_mul_f32 v[120:121], v[120:121], v[48:49]\n"
        "v_pk_mul_f32 v[122:123], v[122:123], v[50:51]\n"
        "v_pk_mul_f32 v[124:125], v[124:125], v[52:53]\n"
        "v_pk_mul_f32 v[126:127], v[126:127], v[54:55]\n"
        "v_exp_f32 v120, v120\n"
        "v_exp_f32 v121, v121\n"
        "v_exp_f32 v122, v122\n"
        "v_exp_f32 v123, v123\n"
        "v_exp_f32 v124, v124\n"
        "v_exp_f32 v125, v125\n"
        "v_exp_f32 v126, v126\n"
        "v_exp_f32 v127, v127\n"
        "v_pk_fma_f32 v[120:121], v[24:25], v[120:121], v[24:25] op_sel:[0,0,0] op_sel_hi:[0,1,0]\n"
        "v_pk_fma_f32 v[122:123], v[24:25], v[122:123], v[24:25] op_sel:[0,0,0] op_sel_hi:[0,1,0]\n"
        "v_pk_fma_f32 v[124:125], v[24:25], v[124:125], v[24:25] op_sel:[1,0,1] op_sel_hi:[1,1,1]\n"
        "v_pk_fma_f32 v[126:127], v[24:25], v[126:127], v[24:25] op_sel:[1,0,1] op_sel_hi:[1,1,1]\n"
        "v_rcp_f32 v120, v120\n"
        "v_rcp_f32 v121, v121\n"
        "v_rcp_f32 v122, v122\n"
        "v_rcp_f32 v123, v123\n"
        "v_rcp_f32 v124, v124\n"
        "v_rcp_f32 v125, v125\n"
        "v_rcp_f32 v126, v126\n"
        "v_rcp_f32 v127, v127\n"
        "v_pk_mul_f32 v[48:49], v[48:49], v[120:121]\n"
        "v_pk_mul_f32 v[50:51], v[50:51], v[122:123]\n"
        "v_pk_mul_f32 v[52:53], v[52:53], v[124:125]\n"
        "v_pk_mul_f32 v[54:55], v[54:55], v[126:127]\n"
        "v_pk_add_f32 v[72:73], v[40:41], v[42:43]\n"
        "v_pk_mul_f32 v[76:77], v[40:41], v[40:41]\n"
        "v_pk_add_f32 v[72:73], v[72:73], v[48:49]\n"
        "v_pk_fma_f32 v[76:77], v[42:43], v[42:43], v[76:77]\n"
        "v_pk_add_f32 v[72:73], v[72:73], v[50:51]\n"
        "v_pk_fma_f32 v[76:77], v[48:49], v[48:49], v[76:77]\n"
        "v_pk_fma_f32 v[76:77], v[50:51], v[50:51], v[76:77]\n"
        "v_pk_add_f32 v[74:75], v[44:45], v[46:47]\n"
        "v_pk_mul_f32 v[78:79], v[44:45], v[44:45]\n"
        "v_pk_add_f32 v[74:75], v[74:75], v[52:53]\n"
        "v_pk_fma_f32 v[78:79], v[46:47], v[46:47], v[78:79]\n"
        "v_pk_add_f32 v[74:75], v[74:75], v[54:55]\n"
        "v_pk_fma_f32 v[78:79], v[52:53], v[52:53], v[78:79]\n"
        "v_pk_fma_f32 v[78:79], v[54:55], v[54:55], v[78:79]\n"
        "v_add_f32 v72, v72, v73\n"
        "v_add_f32 v74, v74, v75\n"
        "v_add_f32 v76, v76, v77\n"
        "v_add_f32 v78, v78, v79\n"
        "ds_write_b32 v34, v72 offset:0\n"
        "ds_write_b32 v34, v74 offset:512\n"
        "ds_write_b32 v34, v76 offset:4096\n"
        "ds_write_b32 v34, v78 offset:4608\n"
        "s_add_u32 m0, s56, 73728\n"
        "s_mul_i32 s7, s75, 196608\n"
        "buffer_load_dwordx4 v5, s[12:15], s7 offen lds\n"
        "s_add_u32 m0, s56, 81920\n"
        "s_mul_i32 s7, s76, 196608\n"
        "buffer_load_dwordx4 v5, s[12:15], s7 offen lds\n"
        "s_add_u32 m0, s56, 90112\n"
        "s_mul_i32 s7, s77, 196608\n"
        "buffer_load_dwordx4 v5, s[12:15], s7 offen lds\n"
        "s_add_u32 m0, s56, 98304\n"
        "s_mul_i32 s7, s78, 196608\n"
        "buffer_load_dwordx4 v5, s[12:15], s7 offen lds\n"
        "s_add_u32 m0, s56, 106496\n"
        "s_mul_i32 s7, s79, 196608\n"
        "buffer_load_dwordx4 v5, s[12:15], s7 offen lds\n"
        "s_add_u32 m0, s56, 122880\n"
        "s_cmp_eq_u32 s52, 0\n"
        "s_cselect_b32 s7, s74, s75\n"
        "s_mul_i32 s7, s7, 196608\n"
        "buffer_load_dwordx4 v6, s[12:15], s7 offen lds\n"
        "s_add_u32 m0, s56, 131072\n"
        "s_cmp_eq_u32 s52, 0\n"
        "s_cselect_b32 s7, s76, s77\n"
        "s_mul_i32 s7, s7, 196608\n"
        "buffer_load_dwordx4 v6, s[12:15], s7 offen lds\n"
        "s_add_u32 m0, s56, 139264\n"
        "s_cmp_eq_u32 s52, 0\n"
        "s_cselect_b32 s7, s78, s79\n"
        "s_mul_i32 s7, s7, 196608\n"
        "buffer_load_dwordx4 v6, s[12:15], s7 offen lds\n"
        "s_branch 102f\n"
        "101:\n"
        "s_waitcnt vmcnt(8)\n"
        "s_barrier\n"
        "s_setprio 1\n"
        "ds_read_b128 v[128:131], v8 offset:0\n"
        "ds_read_b128 v[192:195], v8 offset:2048\n"
        "ds_read_b64_tr_b16 v[88:89], v10 offset:16384\n"
        "ds_read_b64_tr_b16 v[90:91], v10 offset:16896\n"
        "ds_read_b64_tr_b16 v[96:97], v11 offset:16384\n"
        "ds_read_b64_tr_b16 v[98:99], v11 offset:16896\n"
        "ds_read_b128 v[132:135], v9 offset:0\n"
        "ds_read_b128 v[196:199], v9 offset:2048\n"
        "ds_read_b64_tr_b16 v[92:93], v10 offset:20480\n"
        "ds_read_b64_tr_b16 v[94:95], v10 offset:20992\n"
        "ds_read_b64_tr_b16 v[100:101], v11 offset:20480\n"
        "ds_read_b64_tr_b16 v[102:103], v11 offset:20992\n"
        "s_add_u32 m0, s56, 73728\n"
        "s_lshl_b32 s83, s75, 7\n"
        "buffer_load_dwordx4 v7, s[8:11], s83 offen lds\n"
        "s_add_u32 m0, s56, 81920\n"
        "s_add_u32 s83, s83, 65536\n"
        "buffer_load_dwordx4 v7, s[8:11], s83 offen lds\n"
        "s_add_u32 m0, s56, 88320\n"
        "s_mul_i32 s7, s75, 229376\n"
        "buffer_load_dwordx4 v4, s[16:19], s7 offen offset:1792 lds\n"
        "s_waitcnt vmcnt(6) lgkmcnt(0)\n"
        "s_barrier\n"
        "s_setprio 0\n"
        "v_mfma_f32_16x16x32_bf16 v[40:43], v[88:91], v[128:131], 0\n"
        "v_mfma_f32_16x16x32_bf16 v[44:47], v[88:91], v[192:195], 0\n"
        "v_mfma_f32_16x16x32_bf16 v[48:51], v[96:99], v[128:131], 0\n"
        "v_mfma_f32_16x16x32_bf16 v[52:55], v[96:99], v[192:195], 0\n"
        "v_mfma_f32_16x16x32_bf16 v[40:43], v[92:95], v[132:135], v[40:43]\n"
        "v_mfma_f32_16x16x32_bf16 v[44:47], v[92:95], v[196:199], v[44:47]\n"
        "v_mfma_f32_16x16x32_bf16 v[48:51], v[100:103], v[132:135], v[48:51]\n"
        "v_mfma_f32_16x16x32_bf16 v[52:55], v[100:103], v[196:199], v[52:55]\n"
        "v_mfma_f32_16x16x32_bf16 v[60:63], v[192:195], v[192:195], 0\n"
        "v_mfma_f32_16x16x32_bf16 v[60:63], v[196:199], v[196:199], v[60:63]\n"
        "v_smfmac_f32_16x16x64_bf16 v[56:59], v[28:31], v[192:199], v32\n"
        "v_smfmac_f32_16x16x64_bf16 v[80:83], v[28:31], v[88:95], v32\n"
        "v_smfmac_f32_16x16x64_bf16 v[84:87], v[28:31], v[96:103], v32\n"
        "s_setprio 1\n"
        "ds_read_b128 v[136:139], v8 offset:24576\n"
        "ds_read_b128 v[200:203], v8 offset:26624\n"
        "ds_read_b64_tr_b16 v[104:105], v10 offset:40960\n"
        "ds_read_b64_tr_b16 v[106:107], v10 offset:41472\n"
        "ds_read_b64_tr_b16 v[112:113], v11 offset:40960\n"
        "ds_read_b64_tr_b16 v[114:115], v11 offset:41472\n"
        "ds_read_b128 v[140:143], v9 offset:24576\n"
        "ds_read_b128 v[204:207], v9 offset:26624\n"
        "ds_read_b64_tr_b16 v[108:109], v10 offset:45056\n"
        "ds_read_b64_tr_b16 v[110:111], v10 offset:45568\n"
        "ds_read_b64_tr_b16 v[116:117], v11 offset:45056\n"
        "ds_read_b64_tr_b16 v[118:119], v11 offset:45568\n"
        "s_add_u32 m0, s56, 98304\n"
        "s_lshl_b32 s83, s76, 7\n"
        "buffer_load_dwordx4 v7, s[8:11], s83 offen lds\n"
        "s_add_u32 m0, s56, 106496\n"
        "s_add_u32 s83, s83, 65536\n"
        "buffer_load_dwordx4 v7, s[8:11], s83 offen lds\n"
        "s_add_u32 m0, s56, 112896\n"
        "s_mul_i32 s7, s76, 229376\n"
        "buffer_load_dwordx4 v4, s[16:19], s7 offen offset:1792 lds\n"
        "s_waitcnt vmcnt(3) lgkmcnt(0)\n"
        "s_barrier\n"
        "s_setprio 0\n"
        "v_mfma_f32_16x16x32_bf16 v[40:43], v[104:107], v[136:139], v[40:43]\n"
        "v_mfma_f32_16x16x32_bf16 v[44:47], v[104:107], v[200:203], v[44:47]\n"
        "v_mfma_f32_16x16x32_bf16 v[48:51], v[112:115], v[136:139], v[48:51]\n"
        "v_mfma_f32_16x16x32_bf16 v[52:55], v[112:115], v[200:203], v[52:55]\n"
        "v_mfma_f32_16x16x32_bf16 v[40:43], v[108:111], v[140:143], v[40:43]\n"
        "v_mfma_f32_16x16x32_bf16 v[44:47], v[108:111], v[204:207], v[44:47]\n"
        "v_mfma_f32_16x16x32_bf16 v[48:51], v[116:119], v[140:143], v[48:51]\n"
        "v_mfma_f32_16x16x32_bf16 v[52:55], v[116:119], v[204:207], v[52:55]\n"
        "v_mfma_f32_16x16x32_bf16 v[60:63], v[200:203], v[200:203], v[60:63]\n"
        "v_mfma_f32_16x16x32_bf16 v[60:63], v[204:207], v[204:207], v[60:63]\n"
        "v_smfmac_f32_16x16x64_bf16 v[56:59], v[28:31], v[200:207], v32\n"
        "v_smfmac_f32_16x16x64_bf16 v[80:83], v[28:31], v[104:111], v32\n"
        "v_smfmac_f32_16x16x64_bf16 v[84:87], v[28:31], v[112:119], v32\n"
        "s_setprio 1\n"
        "v_add_u32 v10, 65536, v10\n"
        "v_add_u32 v11, 65536, v11\n"
        "ds_read_b128 v[144:147], v8 offset:49152\n"
        "ds_read_b128 v[208:211], v8 offset:51200\n"
        "ds_read_b64_tr_b16 v[88:89], v10 offset:0\n"
        "ds_read_b64_tr_b16 v[90:91], v10 offset:512\n"
        "ds_read_b64_tr_b16 v[96:97], v11 offset:0\n"
        "ds_read_b64_tr_b16 v[98:99], v11 offset:512\n"
        "ds_read_b128 v[148:151], v9 offset:49152\n"
        "ds_read_b128 v[212:215], v9 offset:51200\n"
        "ds_read_b64_tr_b16 v[92:93], v10 offset:4096\n"
        "ds_read_b64_tr_b16 v[94:95], v10 offset:4608\n"
        "ds_read_b64_tr_b16 v[100:101], v11 offset:4096\n"
        "ds_read_b64_tr_b16 v[102:103], v11 offset:4608\n"
        "s_add_u32 m0, s56, 122880\n"
        "s_lshl_b32 s83, s77, 7\n"
        "buffer_load_dwordx4 v7, s[8:11], s83 offen lds\n"
        "s_add_u32 m0, s56, 131072\n"
        "s_add_u32 s83, s83, 65536\n"
        "buffer_load_dwordx4 v7, s[8:11], s83 offen lds\n"
        "s_add_u32 m0, s56, 137472\n"
        "s_mul_i32 s7, s77, 229376\n"
        "buffer_load_dwordx4 v4, s[16:19], s7 offen offset:1792 lds\n"
        "s_waitcnt lgkmcnt(0)\n"
        "s_setprio 0\n"
        "v_mfma_f32_16x16x32_bf16 v[40:43], v[88:91], v[144:147], v[40:43]\n"
        "v_mfma_f32_16x16x32_bf16 v[44:47], v[88:91], v[208:211], v[44:47]\n"
        "v_mfma_f32_16x16x32_bf16 v[48:51], v[96:99], v[144:147], v[48:51]\n"
        "v_mfma_f32_16x16x32_bf16 v[52:55], v[96:99], v[208:211], v[52:55]\n"
        "v_mfma_f32_16x16x32_bf16 v[40:43], v[92:95], v[148:151], v[40:43]\n"
        "v_mfma_f32_16x16x32_bf16 v[44:47], v[92:95], v[212:215], v[44:47]\n"
        "v_mfma_f32_16x16x32_bf16 v[48:51], v[100:103], v[148:151], v[48:51]\n"
        "v_mfma_f32_16x16x32_bf16 v[52:55], v[100:103], v[212:215], v[52:55]\n"
        "v_mfma_f32_16x16x32_bf16 v[60:63], v[208:211], v[208:211], v[60:63]\n"
        "v_mfma_f32_16x16x32_bf16 v[60:63], v[212:215], v[212:215], v[60:63]\n"
        "v_smfmac_f32_16x16x64_bf16 v[56:59], v[28:31], v[208:215], v32\n"
        "v_smfmac_f32_16x16x64_bf16 v[80:83], v[28:31], v[88:95], v32\n"
        "v_smfmac_f32_16x16x64_bf16 v[84:87], v[28:31], v[96:103], v32\n"
        "s_setprio 1\n"
        "v_add_u32 v8, 73728, v8\n"
        "v_add_u32 v9, 73728, v9\n"
        "ds_read_b128 v[152:155], v8 offset:0\n"
        "ds_read_b128 v[216:219], v8 offset:2048\n"
        "ds_read_b64_tr_b16 v[104:105], v10 offset:24576\n"
        "ds_read_b64_tr_b16 v[106:107], v10 offset:25088\n"
        "ds_read_b64_tr_b16 v[112:113], v11 offset:24576\n"
        "ds_read_b64_tr_b16 v[114:115], v11 offset:25088\n"
        "ds_read_b128 v[156:159], v9 offset:0\n"
        "ds_read_b128 v[220:223], v9 offset:2048\n"
        "ds_read_b64_tr_b16 v[108:109], v10 offset:28672\n"
        "ds_read_b64_tr_b16 v[110:111], v10 offset:29184\n"
        "ds_read_b64_tr_b16 v[116:117], v11 offset:28672\n"
        "ds_read_b64_tr_b16 v[118:119], v11 offset:29184\n"
        "s_add_u32 m0, s56, 0\n"
        "s_lshl_b32 s83, s78, 7\n"
        "buffer_load_dwordx4 v7, s[8:11], s83 offen lds\n"
        "s_add_u32 m0, s56, 8192\n"
        "s_add_u32 s83, s83, 65536\n"
        "buffer_load_dwordx4 v7, s[8:11], s83 offen lds\n"
        "s_add_u32 m0, s56, 14592\n"
        "s_mul_i32 s7, s78, 229376\n"
        "buffer_load_dwordx4 v4, s[16:19], s7 offen offset:1792 lds\n"
        "s_waitcnt vmcnt(3) lgkmcnt(0)\n"
        "s_barrier\n"
        "s_setprio 0\n"
        "v_mfma_f32_16x16x32_bf16 v[40:43], v[104:107], v[152:155], v[40:43]\n"
        "v_mfma_f32_16x16x32_bf16 v[44:47], v[104:107], v[216:219], v[44:47]\n"
        "v_mfma_f32_16x16x32_bf16 v[48:51], v[112:115], v[152:155], v[48:51]\n"
        "v_mfma_f32_16x16x32_bf16 v[52:55], v[112:115], v[216:219], v[52:55]\n"
        "v_mfma_f32_16x16x32_bf16 v[40:43], v[108:111], v[156:159], v[40:43]\n"
        "v_mfma_f32_16x16x32_bf16 v[44:47], v[108:111], v[220:223], v[44:47]\n"
        "v_mfma_f32_16x16x32_bf16 v[48:51], v[116:119], v[156:159], v[48:51]\n"
        "v_mfma_f32_16x16x32_bf16 v[52:55], v[116:119], v[220:223], v[52:55]\n"
        "v_mfma_f32_16x16x32_bf16 v[60:63], v[216:219], v[216:219], v[60:63]\n"
        "v_mfma_f32_16x16x32_bf16 v[60:63], v[220:223], v[220:223], v[60:63]\n"
        "v_smfmac_f32_16x16x64_bf16 v[56:59], v[28:31], v[216:223], v32\n"
        "v_smfmac_f32_16x16x64_bf16 v[80:83], v[28:31], v[104:111], v32\n"
        "v_smfmac_f32_16x16x64_bf16 v[84:87], v[28:31], v[112:119], v32\n"
        "s_setprio 1\n"
        "ds_read_b128 v[160:163], v8 offset:24576\n"
        "ds_read_b128 v[224:227], v8 offset:26624\n"
        "ds_read_b64_tr_b16 v[88:89], v10 offset:49152\n"
        "ds_read_b64_tr_b16 v[90:91], v10 offset:49664\n"
        "ds_read_b64_tr_b16 v[96:97], v11 offset:49152\n"
        "ds_read_b64_tr_b16 v[98:99], v11 offset:49664\n"
        "ds_read_b128 v[164:167], v9 offset:24576\n"
        "ds_read_b128 v[228:231], v9 offset:26624\n"
        "ds_read_b64_tr_b16 v[92:93], v10 offset:53248\n"
        "ds_read_b64_tr_b16 v[94:95], v10 offset:53760\n"
        "ds_read_b64_tr_b16 v[100:101], v11 offset:53248\n"
        "ds_read_b64_tr_b16 v[102:103], v11 offset:53760\n"
        "s_add_u32 m0, s56, 24576\n"
        "s_lshl_b32 s83, s79, 7\n"
        "buffer_load_dwordx4 v7, s[8:11], s83 offen lds\n"
        "s_add_u32 m0, s56, 32768\n"
        "s_add_u32 s83, s83, 65536\n"
        "buffer_load_dwordx4 v7, s[8:11], s83 offen lds\n"
        "s_add_u32 m0, s56, 39168\n"
        "s_mul_i32 s7, s79, 229376\n"
        "buffer_load_dwordx4 v4, s[16:19], s7 offen offset:1792 lds\n"
        "s_waitcnt lgkmcnt(0)\n"
        "s_setprio 0\n"
        "v_mfma_f32_16x16x32_bf16 v[40:43], v[88:91], v[160:163], v[40:43]\n"
        "v_mfma_f32_16x16x32_bf16 v[44:47], v[88:91], v[224:227], v[44:47]\n"
        "v_mfma_f32_16x16x32_bf16 v[48:51], v[96:99], v[160:163], v[48:51]\n"
        "v_mfma_f32_16x16x32_bf16 v[52:55], v[96:99], v[224:227], v[52:55]\n"
        "v_mfma_f32_16x16x32_bf16 v[40:43], v[92:95], v[164:167], v[40:43]\n"
        "v_mfma_f32_16x16x32_bf16 v[44:47], v[92:95], v[228:231], v[44:47]\n"
        "v_mfma_f32_16x16x32_bf16 v[48:51], v[100:103], v[164:167], v[48:51]\n"
        "v_mfma_f32_16x16x32_bf16 v[52:55], v[100:103], v[228:231], v[52:55]\n"
        "v_mfma_f32_16x16x32_bf16 v[60:63], v[224:227], v[224:227], v[60:63]\n"
        "v_mfma_f32_16x16x32_bf16 v[60:63], v[228:231], v[228:231], v[60:63]\n"
        "v_smfmac_f32_16x16x64_bf16 v[56:59], v[28:31], v[224:231], v32\n"
        "v_smfmac_f32_16x16x64_bf16 v[80:83], v[28:31], v[88:95], v32\n"
        "v_smfmac_f32_16x16x64_bf16 v[84:87], v[28:31], v[96:103], v32\n"
        "s_setprio 1\n"
        "v_add_u32 v10, 73728, v10\n"
        "v_add_u32 v11, 73728, v11\n"
        "ds_read_b128 v[168:171], v8 offset:49152\n"
        "ds_read_b128 v[232:235], v8 offset:51200\n"
        "ds_read_b64_tr_b16 v[104:105], v10 offset:0\n"
        "ds_read_b64_tr_b16 v[106:107], v10 offset:512\n"
        "ds_read_b64_tr_b16 v[112:113], v11 offset:0\n"
        "ds_read_b64_tr_b16 v[114:115], v11 offset:512\n"
        "ds_read_b128 v[172:175], v9 offset:49152\n"
        "ds_read_b128 v[236:239], v9 offset:51200\n"
        "ds_read_b64_tr_b16 v[108:109], v10 offset:4096\n"
        "ds_read_b64_tr_b16 v[110:111], v10 offset:4608\n"
        "ds_read_b64_tr_b16 v[116:117], v11 offset:4096\n"
        "ds_read_b64_tr_b16 v[118:119], v11 offset:4608\n"
        "s_add_u32 m0, s56, 49152\n"
        "s_mul_i32 s7, s72, 196608\n"
        "buffer_load_dwordx4 v5, s[12:15], s7 offen lds\n"
        "s_add_u32 m0, s56, 57344\n"
        "s_mul_i32 s7, s73, 196608\n"
        "buffer_load_dwordx4 v5, s[12:15], s7 offen lds\n"
        "s_waitcnt vmcnt(2) lgkmcnt(0)\n"
        "s_barrier\n"
        "s_setprio 0\n"
        "v_mfma_f32_16x16x32_bf16 v[40:43], v[104:107], v[168:171], v[40:43]\n"
        "v_mfma_f32_16x16x32_bf16 v[44:47], v[104:107], v[232:235], v[44:47]\n"
        "v_mfma_f32_16x16x32_bf16 v[48:51], v[112:115], v[168:171], v[48:51]\n"
        "v_mfma_f32_16x16x32_bf16 v[52:55], v[112:115], v[232:235], v[52:55]\n"
        "v_mfma_f32_16x16x32_bf16 v[40:43], v[108:111], v[172:175], v[40:43]\n"
        "v_mfma_f32_16x16x32_bf16 v[44:47], v[108:111], v[236:239], v[44:47]\n"
        "v_mfma_f32_16x16x32_bf16 v[48:51], v[116:119], v[172:175], v[48:51]\n"
        "v_mfma_f32_16x16x32_bf16 v[52:55], v[116:119], v[236:239], v[52:55]\n"
        "v_mfma_f32_16x16x32_bf16 v[60:63], v[232:235], v[232:235], v[60:63]\n"
        "v_mfma_f32_16x16x32_bf16 v[60:63], v[236:239], v[236:239], v[60:63]\n"
        "v_smfmac_f32_16x16x64_bf16 v[56:59], v[28:31], v[232:239], v32\n"
        "v_smfmac_f32_16x16x64_bf16 v[80:83], v[28:31], v[104:111], v32\n"
        "v_smfmac_f32_16x16x64_bf16 v[84:87], v[28:31], v[112:119], v32\n"
        "s_setprio 1\n"
        "v_subrev_u32 v10, 122880, v10\n"
        "v_subrev_u32 v11, 122880, v11\n"
        "v_subrev_u32 v8, 73728, v8\n"
        "v_subrev_u32 v9, 73728, v9\n"
        "ds_read_b128 v[176:179], v8 offset:0\n"
        "ds_read_b128 v[240:243], v8 offset:2048\n"
        "ds_read_b64_tr_b16 v[88:89], v10 offset:0\n"
        "ds_read_b64_tr_b16 v[90:91], v10 offset:512\n"
        "ds_read_b64_tr_b16 v[96:97], v11 offset:0\n"
        "ds_read_b64_tr_b16 v[98:99], v11 offset:512\n"
        "ds_read_b128 v[180:183], v9 offset:0\n"
        "ds_read_b128 v[244:247], v9 offset:2048\n"
        "ds_read_b64_tr_b16 v[92:93], v10 offset:4096\n"
        "ds_read_b64_tr_b16 v[94:95], v10 offset:4608\n"
        "ds_read_b64_tr_b16 v[100:101], v11 offset:4096\n"
        "ds_read_b64_tr_b16 v[102:103], v11 offset:4608\n"
        "s_add_u32 m0, s56, 114688\n"
        "s_cmp_eq_u32 s52, 0\n"
        "s_cselect_b32 s7, s72, s73\n"
        "s_mul_i32 s7, s7, 196608\n"
        "buffer_load_dwordx4 v6, s[12:15], s7 offen lds\n"
        "s_add_u32 m0, s56, 65536\n"
        "s_mul_i32 s7, s74, 196608\n"
        "buffer_load_dwordx4 v5, s[12:15], s7 offen lds\n"
        "s_waitcnt lgkmcnt(0)\n"
        "s_setprio 0\n"
        "v_mfma_f32_16x16x32_bf16 v[40:43], v[88:91], v[176:179], v[40:43]\n"
        "v_mfma_f32_16x16x32_bf16 v[44:47], v[88:91], v[240:243], v[44:47]\n"
        "v_mfma_f32_16x16x32_bf16 v[48:51], v[96:99], v[176:179], v[48:51]\n"
        "v_mfma_f32_16x16x32_bf16 v[52:55], v[96:99], v[240:243], v[52:55]\n"
        "v_mfma_f32_16x16x32_bf16 v[40:43], v[92:95], v[180:183], v[40:43]\n"
        "v_mfma_f32_16x16x32_bf16 v[44:47], v[92:95], v[244:247], v[44:47]\n"
        "v_mfma_f32_16x16x32_bf16 v[48:51], v[100:103], v[180:183], v[48:51]\n"
        "v_mfma_f32_16x16x32_bf16 v[52:55], v[100:103], v[244:247], v[52:55]\n"
        "v_mfma_f32_16x16x32_bf16 v[60:63], v[240:243], v[240:243], v[60:63]\n"
        "v_mfma_f32_16x16x32_bf16 v[60:63], v[244:247], v[244:247], v[60:63]\n"
        "v_smfmac_f32_16x16x64_bf16 v[56:59], v[28:31], v[240:247], v32\n"
        "v_smfmac_f32_16x16x64_bf16 v[80:83], v[28:31], v[88:95], v32\n"
        "v_smfmac_f32_16x16x64_bf16 v[84:87], v[28:31], v[96:103], v32\n"
        "s_setprio 1\n"
        "ds_read_b128 v[184:187], v8 offset:24576\n"
        "ds_read_b128 v[248:251], v8 offset:26624\n"
        "ds_read_b64_tr_b16 v[104:105], v10 offset:24576\n"
        "ds_read_b64_tr_b16 v[106:107], v10 offset:25088\n"
        "ds_read_b64_tr_b16 v[112:113], v11 offset:24576\n"
        "ds_read_b64_tr_b16 v[114:115], v11 offset:25088\n"
        "ds_read_b128 v[188:191], v9 offset:24576\n"
        "ds_read_b128 v[252:255], v9 offset:26624\n"
        "ds_read_b64_tr_b16 v[108:109], v10 offset:28672\n"
        "ds_read_b64_tr_b16 v[110:111], v10 offset:29184\n"
        "ds_read_b64_tr_b16 v[116:117], v11 offset:28672\n"
        "ds_read_b64_tr_b16 v[118:119], v11 offset:29184\n"
        "s_waitcnt lgkmcnt(0)\n"
        "s_setprio 0\n"
        "v_mfma_f32_16x16x32_bf16 v[60:63], v[248:251], v[248:251], v[60:63]\n"
        "v_mfma_f32_16x16x32_bf16 v[60:63], v[252:255], v[252:255], v[60:63]\n"
        "v_smfmac_f32_16x16x64_bf16 v[56:59], v[28:31], v[248:255], v32\n"
        "v_smfmac_f32_16x16x64_bf16 v[80:83], v[28:31], v[104:111], v32\n"
        "v_smfmac_f32_16x16x64_bf16 v[84:87], v[28:31], v[112:119], v32\n"
        "v_mfma_f32_16x16x32_bf16 v[40:43], v[104:107], v[184:187], v[40:43]\n"
        "v_mfma_f32_16x16x32_bf16 v[44:47], v[104:107], v[248:251], v[44:47]\n"
        "s_nop 3\n"
        "v_mul_f32 v59, v60, v72\n"
        "v_fmac_f32 v59, v61, v73\n"
        "v_fmac_f32 v59, v62, v74\n"
        "v_fmac_f32 v59, v63, v75\n"
        "v_add_f32 v58, v56, v57\n"
        "ds_write_b64 v76, v[58:59]\n"
        "s_waitcnt lgkmcnt(0)\n"
        "s_barrier\n"
        "ds_read_b64 v[120:121], v77 offset:0\n"
        "ds_read_b64 v[122:123], v77 offset:128\n"
        "v_mfma_f32_16x16x32_bf16 v[48:51], v[112:115], v[184:187], v[48:51]\n"
        "v_mfma_f32_16x16x32_bf16 v[52:55], v[112:115], v[248:251], v[52:55]\n"
        "v_mfma_f32_16x16x32_bf16 v[40:43], v[108:111], v[188:191], v[40:43]\n"
        "v_mfma_f32_16x16x32_bf16 v[44:47], v[108:111], v[252:255], v[44:47]\n"
        "v_mfma_f32_16x16x32_bf16 v[48:51], v[116:119], v[188:191], v[48:51]\n"
        "v_mfma_f32_16x16x32_bf16 v[52:55], v[116:119], v[252:255], v[52:55]\n"
        "s_add_u32 m0, s56, 73728\n"
        "s_mul_i32 s7, s75, 196608\n"
        "buffer_load_dwordx4 v5, s[12:15], s7 offen lds\n"
        "s_add_u32 m0, s56, 81920\n"
        "s_mul_i32 s7, s76, 196608\n"
        "buffer_load_dwordx4 v5, s[12:15], s7 offen lds\n"
        "s_add_u32 m0, s56, 90112\n"
        "s_mul_i32 s7, s77, 196608\n"
        "buffer_load_dwordx4 v5, s[12:15], s7 offen lds\n"
        "s_add_u32 m0, s56, 98304\n"
        "s_mul_i32 s7, s78, 196608\n"
        "buffer_load_dwordx4 v5, s[12:15], s7 offen lds\n"
        "s_add_u32 m0, s56, 106496\n"
        "s_mul_i32 s7, s79, 196608\n"
        "buffer_load_dwordx4 v5, s[12:15], s7 offen lds\n"
        "s_add_u32 m0, s56, 122880\n"
        "s_cmp_eq_u32 s52, 0\n"
        "s_cselect_b32 s7, s74, s75\n"
        "s_mul_i32 s7, s7, 196608\n"
        "buffer_load_dwordx4 v6, s[12:15], s7 offen lds\n"
        "s_add_u32 m0, s56, 131072\n"
        "s_cmp_eq_u32 s52, 0\n"
        "s_cselect_b32 s7, s76, s77\n"
        "s_mul_i32 s7, s7, 196608\n"
        "buffer_load_dwordx4 v6, s[12:15], s7 offen lds\n"
        "s_add_u32 m0, s56, 139264\n"
        "s_cmp_eq_u32 s52, 0\n"
        "s_cselect_b32 s7, s78, s79\n"
        "s_mul_i32 s7, s7, 196608\n"
        "buffer_load_dwordx4 v6, s[12:15], s7 offen lds\n"
        "v_add_u32 v3, 1, v3\n"
        "s_waitcnt lgkmcnt(0)\n"
        "v_mul_f32 v18, 0xba000000, v120\n"
        "v_mul_f32 v19, 0xba000000, v122\n"
        "v_mul_f32 v120, 0x3b000000, v120\n"
        "v_mul_f32 v122, 0x3b000000, v122\n"
        "v_add_f32 v82, v80, v81\n"
        "v_add_f32 v86, v84, v85\n"
        "v_mov_b32 v80, 0\n"
        "v_mov_b32 v81, 0\n"
        "v_mov_b32 v84, 0\n"
        "v_mov_b32 v85, 0\n"
        "v_mfma_f32_16x16x4_f32 v[40:43], v82, v18, v[40:43]\n"
        "v_mfma_f32_16x16x4_f32 v[44:47], v82, v19, v[44:47]\n"
        "v_mfma_f32_16x16x4_f32 v[48:51], v86, v18, v[48:51]\n"
        "v_mfma_f32_16x16x4_f32 v[52:55], v86, v19, v[52:55]\n"
        "v_fma_f32 v24, -v120, v120, v121\n"
        "v_fma_f32 v25, -v122, v122, v123\n"
        "v_max_f32 v24, 0, v24\n"
        "v_max_f32 v25, 0, v25\n"
        "v_add_f32 v24, 0x3727c5ac, v24\n"
        "v_add_f32 v25, 0x3727c5ac, v25\n"
        "v_rsq_f32 v16, v24\n"
        "v_rsq_f32 v17, v25\n"
        "v_mul_f32 v20, 0xc0135761, v16\n"
        "v_mul_f32 v21, 0xc0135761, v17\n"
        "v_pk_mul_f32 v[22:23], v[16:17], v[16:17]\n"
        "v_pk_mul_f32 v[24:25], v[24:25], v[16:17]\n"
        "v_pk_mul_f32 v[22:23], v[22:23], v[20:21]\n"
        "v_mul_f32 v38, s71, v16\n"
        "v_mul_f32 v39, s71, v17\n"
        "v_mul_f32 v22, 0x3d372713, v22\n"
        "v_mul_f32 v23, 0x3d372713, v23\n"
        "v_pk_mul_f32 v[120:121], v[40:41], v[40:41]\n"
        "v_pk_mul_f32 v[122:123], v[42:43], v[42:43]\n"
        "v_pk_mul_f32 v[124:125], v[44:45], v[44:45]\n"
        "v_pk_mul_f32 v[126:127], v[46:47], v[46:47]\n"
        "v_pk_fma_f32 v[120:121], v[22:23], v[120:121], v[20:21] op_sel:[0,0,0] op_sel_hi:[0,1,0]\n"
        "v_pk_fma_f32 v[122:123], v[22:23], v[122:123], v[20:21] op_sel:[0,0,0] op_sel_hi:[0,1,0]\n"
        "v_pk_fma_f32 v[124:125], v[22:23], v[124:125], v[20:21] op_sel:[1,0,1] op_sel_hi:[1,1,1]\n"
        "v_pk_fma_f32 v[126:127], v[22:23], v[126:127], v[20:21] op_sel:[1,0,1] op_sel_hi:[1,1,1]\n"
        "v_pk_mul_f32 v[120:121], v[120:121], v[40:41]\n"
        "v_pk_mul_f32 v[122:123], v[122:123], v[42:43]\n"
        "v_pk_mul_f32 v[124:125], v[124:125], v[44:45]\n"
        "v_pk_mul_f32 v[126:127], v[126:127], v[46:47]\n"
        "v_exp_f32 v120, v120\n"
        "v_exp_f32 v121, v121\n"
        "v_exp_f32 v122, v122\n"
        "v_exp_f32 v123, v123\n"
        "v_exp_f32 v124, v124\n"
        "v_exp_f32 v125, v125\n"
        "v_exp_f32 v126, v126\n"
        "v_exp_f32 v127, v127\n"
        "v_pk_fma_f32 v[120:121], v[24:25], v[120:121], v[24:25] op_sel:[0,0,0] op_sel_hi:[0,1,0]\n"
        "v_pk_fma_f32 v[122:123], v[24:25], v[122:123], v[24:25] op_sel:[0,0,0] op_sel_hi:[0,1,0]\n"
        "v_pk_fma_f32 v[124:125], v[24:25], v[124:125], v[24:25] op_sel:[1,0,1] op_sel_hi:[1,1,1]\n"
        "v_pk_fma_f32 v[126:127], v[24:25], v[126:127], v[24:25] op_sel:[1,0,1] op_sel_hi:[1,1,1]\n"
        "v_rcp_f32 v120, v120\n"
        "v_rcp_f32 v121, v121\n"
        "v_rcp_f32 v122, v122\n"
        "v_rcp_f32 v123, v123\n"
        "v_rcp_f32 v124, v124\n"
        "v_rcp_f32 v125, v125\n"
        "v_rcp_f32 v126, v126\n"
        "v_rcp_f32 v127, v127\n"
        "v_pk_mul_f32 v[40:41], v[40:41], v[120:121]\n"
        "v_pk_mul_f32 v[42:43], v[42:43], v[122:123]\n"
        "v_pk_mul_f32 v[44:45], v[44:45], v[124:125]\n"
        "v_pk_mul_f32 v[46:47], v[46:47], v[126:127]\n"
        "v_pk_mul_f32 v[120:121], v[48:49], v[48:49]\n"
        "v_pk_mul_f32 v[122:123], v[50:51], v[50:51]\n"
        "v_pk_mul_f32 v[124:125], v[52:53], v[52:53]\n"
        "v_pk_mul_f32 v[126:127], v[54:55], v[54:55]\n"
        "v_pk_fma_f32 v[120:121], v[22:23], v[120:121], v[20:21] op_sel:[0,0,0] op_sel_hi:[0,1,0]\n"
        "v_pk_fma_f32 v[122:123], v[22:23], v[122:123], v[20:21] op_sel:[0,0,0] op_sel_hi:[0,1,0]\n"
        "v_pk_fma_f32 v[124:125], v[22:23], v[124:125], v[20:21] op_sel:[1,0,1] op_sel_hi:[1,1,1]\n"
        "v_pk_fma_f32 v[126:127], v[22:23], v[126:127], v[20:21] op_sel:[1,0,1] op_sel_hi:[1,1,1]\n"
        "v_pk_mul_f32 v[120:121], v[120:121], v[48:49]\n"
        "v_pk_mul_f32 v[122:123], v[122:123], v[50:51]\n"
        "v_pk_mul_f32 v[124:125], v[124:125], v[52:53]\n"
        "v_pk_mul_f32 v[126:127], v[126:127], v[54:55]\n"
        "v_exp_f32 v120, v120\n"
        "v_exp_f32 v121, v121\n"
        "v_exp_f32 v122, v122\n"
        "v_exp_f32 v123, v123\n"
        "v_exp_f32 v124, v124\n"
        "v_exp_f32 v125, v125\n"
        "v_exp_f32 v126, v126\n"
        "v_exp_f32 v127, v127\n"
        "v_pk_fma_f32 v[120:121], v[24:25], v[120:121], v[24:25] op_sel:[0,0,0] op_sel_hi:[0,1,0]\n"
        "v_pk_fma_f32 v[122:123], v[24:25], v[122:123], v[24:25] op_sel:[0,0,0] op_sel_hi:[0,1,0]\n"
        "v_pk_fma_f32 v[124:125], v[24:25], v[124:125], v[24:25] op_sel:[1,0,1] op_sel_hi:[1,1,1]\n"
        "v_pk_fma_f32 v[126:127], v[24:25], v[126:127], v[24:25] op_sel:[1,0,1] op_sel_hi:[1,1,1]\n"
        "v_rcp_f32 v120, v120\n"
        "v_rcp_f32 v121, v121\n"
        "v_rcp_f32 v122, v122\n"
        "v_rcp_f32 v123, v123\n"
        "v_rcp_f32 v124, v124\n"
        "v_rcp_f32 v125, v125\n"
        "v_rcp_f32 v126, v126\n"
        "v_rcp_f32 v127, v127\n"
        "v_pk_mul_f32 v[48:49], v[48:49], v[120:121]\n"
        "v_pk_mul_f32 v[50:51], v[50:51], v[122:123]\n"
        "v_pk_mul_f32 v[52:53], v[52:53], v[124:125]\n"
        "v_pk_mul_f32 v[54:55], v[54:55], v[126:127]\n"
        "v_pk_add_f32 v[72:73], v[40:41], v[42:43]\n"
        "v_pk_mul_f32 v[76:77], v[40:41], v[40:41]\n"
        "v_pk_add_f32 v[72:73], v[72:73], v[48:49]\n"
        "v_pk_fma_f32 v[76:77], v[42:43], v[42:43], v[76:77]\n"
        "v_pk_add_f32 v[72:73], v[72:73], v[50:51]\n"
        "v_pk_fma_f32 v[76:77], v[48:49], v[48:49], v[76:77]\n"
        "v_pk_fma_f32 v[76:77], v[50:51], v[50:51], v[76:77]\n"
        "v_pk_add_f32 v[74:75], v[44:45], v[46:47]\n"
        "v_pk_mul_f32 v[78:79], v[44:45], v[44:45]\n"
        "v_pk_add_f32 v[74:75], v[74:75], v[52:53]\n"
        "v_pk_fma_f32 v[78:79], v[46:47], v[46:47], v[78:79]\n"
        "v_pk_add_f32 v[74:75], v[74:75], v[54:55]\n"
        "v_pk_fma_f32 v[78:79], v[52:53], v[52:53], v[78:79]\n"
        "v_pk_fma_f32 v[78:79], v[54:55], v[54:55], v[78:79]\n"
        "v_add_f32 v72, v72, v73\n"
        "v_add_f32 v74, v74, v75\n"
        "v_add_f32 v76, v76, v77\n"
        "v_add_f32 v78, v78, v79\n"
        "ds_write_b32 v34, v72 offset:0\n"
        "ds_write_b32 v34, v74 offset:512\n"
        "ds_write_b32 v34, v76 offset:4096\n"
        "ds_write_b32 v34, v78 offset:4608\n"
        "102:\n"
        "s_waitcnt vmcnt(3) lgkmcnt(0)\n"
        "s_barrier\n"
        "ds_read_b128 v[120:123], v35 offset:0\n"
        "ds_read_b128 v[124:127], v35 offset:16\n"
        "ds_read_b64_tr_b16 v[88:89], v10 offset:32768\n"
        "ds_read_b64_tr_b16 v[90:91], v10 offset:33280\n"
        "ds_read_b64_tr_b16 v[96:97], v11 offset:32768\n"
        "ds_read_b64_tr_b16 v[98:99], v11 offset:33280\n"
        "ds_read_b64_tr_b16 v[92:93], v10 offset:36864\n"
        "ds_read_b64_tr_b16 v[94:95], v10 offset:37376\n"
        "ds_read_b64_tr_b16 v[100:101], v11 offset:36864\n"
        "ds_read_b64_tr_b16 v[102:103], v11 offset:37376\n"
        "s_waitcnt lgkmcnt(8)\n"
        "v_add_f32 v120, v120, v122\n"
        "v_add_f32 v121, v121, v123\n"
        "v_add_f32 v124, v124, v126\n"
        "v_add_f32 v125, v125, v127\n"
        "v_add_f32 v120, v120, v124\n"
        "v_add_f32 v121, v121, v125\n"
        "v_add_f32 v120, v120, v121\n"
        "v_mov_b32 v121, v3\n"
        "buffer_store_dwordx2 v[120:121], v15, s[20:23], 0 offen sc1\n"
        "s_waitcnt lgkmcnt(4)\n"
        "s_setprio 3\n"
        "v_mfma_f32_16x16x32_bf16 v[56:59], v[88:91], v[128:131], 0\n"
        "ds_read_b64_tr_b16 v[104:105], v10 offset:40960\n"
        "s_mov_b32 s57, 229376\n"
        "s_add_u32 m0, s56, 0\n"
        "s_mul_i32 s7, s72, s57\n"
        "buffer_load_dwordx4 v4, s[16:19], s7 offen lds\n"
        "v_mfma_f32_16x16x32_bf16 v[60:63], v[88:91], v[192:195], 0\n"
        "ds_read_b64_tr_b16 v[106:107], v10 offset:41472\n"
        "v_mfma_f32_16x16x32_bf16 v[64:67], v[96:99], v[128:131], 0\n"
        "ds_read_b64_tr_b16 v[112:113], v11 offset:40960\n"
        "v_mfma_f32_16x16x32_bf16 v[68:71], v[96:99], v[192:195], 0\n"
        "ds_read_b64_tr_b16 v[114:115], v11 offset:41472\n"
        "s_waitcnt lgkmcnt(4)\n"
        "v_mfma_f32_16x16x32_bf16 v[56:59], v[92:95], v[132:135], v[56:59]\n"
        "ds_read_b64_tr_b16 v[108:109], v10 offset:45056\n"
        "v_mfma_f32_16x16x32_bf16 v[60:63], v[92:95], v[196:199], v[60:63]\n"
        "ds_read_b64_tr_b16 v[110:111], v10 offset:45568\n"
        "v_mfma_f32_16x16x32_bf16 v[64:67], v[100:103], v[132:135], v[64:67]\n"
        "ds_read_b64_tr_b16 v[116:117], v11 offset:45056\n"
        "v_mfma_f32_16x16x32_bf16 v[68:71], v[100:103], v[196:199], v[68:71]\n"
        "ds_read_b64_tr_b16 v[118:119], v11 offset:45568\n"
        "v_smfmac_f32_16x16x64_bf16 v[80:83], v[28:31], v[88:95], v32\n"
        "v_smfmac_f32_16x16x64_bf16 v[84:87], v[28:31], v[96:103], v32\n"
        "s_waitcnt lgkmcnt(4)\n"
        "v_mfma_f32_16x16x32_bf16 v[56:59], v[104:107], v[136:139], v[56:59]\n"
        "ds_read_b64_tr_b16 v[88:89], v10 offset:49152\n"
        "s_add_u32 m0, s56, 8192\n"
        "s_mul_i32 s7, s73, s57\n"
        "buffer_load_dwordx4 v4, s[16:19], s7 offen lds\n"
        "v_mfma_f32_16x16x32_bf16 v[60:63], v[104:107], v[200:203], v[60:63]\n"
        "ds_read_b64_tr_b16 v[90:91], v10 offset:49664\n"
        "v_mfma_f32_16x16x32_bf16 v[64:67], v[112:115], v[136:139], v[64:67]\n"
        "ds_read_b64_tr_b16 v[96:97], v11 offset:49152\n"
        "v_mfma_f32_16x16x32_bf16 v[68:71], v[112:115], v[200:203], v[68:71]\n"
        "ds_read_b64_tr_b16 v[98:99], v11 offset:49664\n"
        "s_waitcnt lgkmcnt(4)\n"
        "v_mfma_f32_16x16x32_bf16 v[56:59], v[108:111], v[140:143], v[56:59]\n"
        "ds_read_b64_tr_b16 v[92:93], v10 offset:53248\n"
        "v_mfma_f32_16x16x32_bf16 v[60:63], v[108:111], v[204:207], v[60:63]\n"
        "ds_read_b64_tr_b16 v[94:95], v10 offset:53760\n"
        "v_mfma_f32_16x16x32_bf16 v[64:67], v[116:119], v[140:143], v[64:67]\n"
        "ds_read_b64_tr_b16 v[100:101], v11 offset:53248\n"
        "v_mfma_f32_16x16x32_bf16 v[68:71], v[116:119], v[204:207], v[68:71]\n"
        "ds_read_b64_tr_b16 v[102:103], v11 offset:53760\n"
        "v_smfmac_f32_16x16x64_bf16 v[80:83], v[28:31], v[104:111], v32\n"
        "v_smfmac_f32_16x16x64_bf16 v[84:87], v[28:31], v[112:119], v32\n"
        "s_waitcnt lgkmcnt(4)\n"
        "v_mfma_f32_16x16x32_bf16 v[56:59], v[88:91], v[144:147], v[56:59]\n"
        "ds_read_b64_tr_b16 v[104:105], v10 offset:57344\n"
        "s_add_u32 m0, s56, 16384\n"
        "s_mul_i32 s7, s74, s57\n"
        "buffer_load_dwordx4 v4, s[16:19], s7 offen lds\n"
        "v_mfma_f32_16x16x32_bf16 v[60:63], v[88:91], v[208:211], v[60:63]\n"
        "ds_read_b64_tr_b16 v[106:107], v10 offset:57856\n"
        "v_mfma_f32_16x16x32_bf16 v[64:67], v[96:99], v[144:147], v[64:67]\n"
        "ds_read_b64_tr_b16 v[112:113], v11 offset:57344\n"
        "v_mfma_f32_16x16x32_bf16 v[68:71], v[96:99], v[208:211], v[68:71]\n"
        "ds_read_b64_tr_b16 v[114:115], v11 offset:57856\n"
        "s_waitcnt lgkmcnt(4)\n"
        "v_mfma_f32_16x16x32_bf16 v[56:59], v[92:95], v[148:151], v[56:59]\n"
        "ds_read_b64_tr_b16 v[108:109], v10 offset:61440\n"
        "v_mfma_f32_16x16x32_bf16 v[60:63], v[92:95], v[212:215], v[60:63]\n"
        "ds_read_b64_tr_b16 v[110:111], v10 offset:61952\n"
        "v_mfma_f32_16x16x32_bf16 v[64:67], v[100:103], v[148:151], v[64:67]\n"
        "ds_read_b64_tr_b16 v[116:117], v11 offset:61440\n"
        "v_mfma_f32_16x16x32_bf16 v[68:71], v[100:103], v[212:215], v[68:71]\n"
        "ds_read_b64_tr_b16 v[118:119], v11 offset:61952\n"
        "v_smfmac_f32_16x16x64_bf16 v[80:83], v[28:31], v[88:95], v32\n"
        "v_smfmac_f32_16x16x64_bf16 v[84:87], v[28:31], v[96:103], v32\n"
        "s_waitcnt lgkmcnt(4)\n"
        "v_add_u32 v10, 65536, v10\n"
        "v_add_u32 v11, 65536, v11\n"
        "s_setprio 2\n"
        "v_mfma_f32_16x16x32_bf16 v[56:59], v[104:107], v[152:155], v[56:59]\n"
        "ds_read_b64_tr_b16 v[88:89], v10 offset:0\n"
        "s_add_u32 m0, s56, 24576\n"
        "s_mul_i32 s7, s75, s57\n"
        "buffer_load_dwordx4 v4, s[16:19], s7 offen lds\n"
        "v_mfma_f32_16x16x32_bf16 v[60:63], v[104:107], v[216:219], v[60:63]\n"
        "ds_read_b64_tr_b16 v[90:91], v10 offset:512\n"
        "v_mfma_f32_16x16x32_bf16 v[64:67], v[112:115], v[152:155], v[64:67]\n"
        "ds_read_b64_tr_b16 v[96:97], v11 offset:0\n"
        "v_mfma_f32_16x16x32_bf16 v[68:71], v[112:115], v[216:219], v[68:71]\n"
        "ds_read_b64_tr_b16 v[98:99], v11 offset:512\n"
        "s_waitcnt lgkmcnt(4)\n"
        "v_mfma_f32_16x16x32_bf16 v[56:59], v[108:111], v[156:159], v[56:59]\n"
        "ds_read_b64_tr_b16 v[92:93], v10 offset:4096\n"
        "v_mfma_f32_16x16x32_bf16 v[60:63], v[108:111], v[220:223], v[60:63]\n"
        "ds_read_b64_tr_b16 v[94:95], v10 offset:4608\n"
        "v_mfma_f32_16x16x32_bf16 v[64:67], v[116:119], v[156:159], v[64:67]\n"
        "ds_read_b64_tr_b16 v[100:101], v11 offset:4096\n"
        "v_mfma_f32_16x16x32_bf16 v[68:71], v[116:119], v[220:223], v[68:71]\n"
        "ds_read_b64_tr_b16 v[102:103], v11 offset:4608\n"
        "v_smfmac_f32_16x16x64_bf16 v[80:83], v[28:31], v[104:111], v32\n"
        "v_smfmac_f32_16x16x64_bf16 v[84:87], v[28:31], v[112:119], v32\n"
        "s_waitcnt lgkmcnt(4)\n"
        "v_mfma_f32_16x16x32_bf16 v[56:59], v[88:91], v[160:163], v[56:59]\n"
        "ds_read_b64_tr_b16 v[104:105], v10 offset:8192\n"
        "s_mov_b32 s64, 0\n"
        "buffer_load_dwordx4 v[72:75], v14, s[20:23], s64 offen sc1\n"
        "v_mfma_f32_16x16x32_bf16 v[60:63], v[88:91], v[224:227], v[60:63]\n"
        "ds_read_b64_tr_b16 v[106:107], v10 offset:8704\n"
        "v_mfma_f32_16x16x32_bf16 v[64:67], v[96:99], v[160:163], v[64:67]\n"
        "ds_read_b64_tr_b16 v[112:113], v11 offset:8192\n"
        "v_mfma_f32_16x16x32_bf16 v[68:71], v[96:99], v[224:227], v[68:71]\n"
        "ds_read_b64_tr_b16 v[114:115], v11 offset:8704\n"
        "s_waitcnt lgkmcnt(4)\n"
        "v_mfma_f32_16x16x32_bf16 v[56:59], v[92:95], v[164:167], v[56:59]\n"
        "ds_read_b64_tr_b16 v[108:109], v10 offset:12288\n"
        "v_mfma_f32_16x16x32_bf16 v[60:63], v[92:95], v[228:231], v[60:63]\n"
        "ds_read_b64_tr_b16 v[110:111], v10 offset:12800\n"
        "s_mov_b32 s64, 8192\n"
        "buffer_load_dwordx4 v[76:79], v14, s[20:23], s64 offen sc1\n"
        "v_mfma_f32_16x16x32_bf16 v[64:67], v[100:103], v[164:167], v[64:67]\n"
        "ds_read_b64_tr_b16 v[116:117], v11 offset:12288\n"
        "v_mfma_f32_16x16x32_bf16 v[68:71], v[100:103], v[228:231], v[68:71]\n"
        "ds_read_b64_tr_b16 v[118:119], v11 offset:12800\n"
        "v_smfmac_f32_16x16x64_bf16 v[80:83], v[28:31], v[88:95], v32\n"
        "v_smfmac_f32_16x16x64_bf16 v[84:87], v[28:31], v[96:103], v32\n"
        "s_waitcnt lgkmcnt(4)\n"
        "s_setprio 1\n"
        "v_mfma_f32_16x16x32_bf16 v[56:59], v[104:107], v[168:171], v[56:59]\n"
        "ds_read_b64_tr_b16 v[88:89], v10 offset:16384\n"
        "s_mov_b32 s64, 16384\n"
        "buffer_load_dwordx4 v[120:123], v14, s[20:23], s64 offen sc1\n"
        "v_mfma_f32_16x16x32_bf16 v[60:63], v[104:107], v[232:235], v[60:63]\n"
        "ds_read_b64_tr_b16 v[90:91], v10 offset:16896\n"
        "v_mfma_f32_16x16x32_bf16 v[64:67], v[112:115], v[168:171], v[64:67]\n"
        "ds_read_b64_tr_b16 v[96:97], v11 offset:16384\n"
        "v_mfma_f32_16x16x32_bf16 v[68:71], v[112:115], v[232:235], v[68:71]\n"
        "ds_read_b64_tr_b16 v[98:99], v11 offset:16896\n"
        "s_waitcnt lgkmcnt(4)\n"
        "v_mfma_f32_16x16x32_bf16 v[56:59], v[108:111], v[172:175], v[56:59]\n"
        "ds_read_b64_tr_b16 v[92:93], v10 offset:20480\n"
        "v_mfma_f32_16x16x32_bf16 v[60:63], v[108:111], v[236:239], v[60:63]\n"
        "ds_read_b64_tr_b16 v[94:95], v10 offset:20992\n"
        "s_mov_b32 s64, 24576\n"
        "buffer_load_dwordx4 v[124:127], v14, s[20:23], s64 offen sc1\n"
        "v_mfma_f32_16x16x32_bf16 v[64:67], v[116:119], v[172:175], v[64:67]\n"
        "ds_read_b64_tr_b16 v[100:101], v11 offset:20480\n"
        "v_mfma_f32_16x16x32_bf16 v[68:71], v[116:119], v[236:239], v[68:71]\n"
        "ds_read_b64_tr_b16 v[102:103], v11 offset:20992\n"
        "v_smfmac_f32_16x16x64_bf16 v[80:83], v[28:31], v[104:111], v32\n"
        "v_smfmac_f32_16x16x64_bf16 v[84:87], v[28:31], v[112:119], v32\n"
        "s_waitcnt lgkmcnt(4)\n"
        "v_mfma_f32_16x16x32_bf16 v[56:59], v[88:91], v[176:179], v[56:59]\n"
        "ds_read_b64_tr_b16 v[104:105], v10 offset:24576\n"
        "s_add_u32 m0, s56, 32768\n"
        "s_mul_i32 s7, s76, s57\n"
        "buffer_load_dwordx4 v4, s[16:19], s7 offen lds\n"
        "v_mfma_f32_16x16x32_bf16 v[60:63], v[88:91], v[240:243], v[60:63]\n"
        "ds_read_b64_tr_b16 v[106:107], v10 offset:25088\n"
        "v_mfma_f32_16x16x32_bf16 v[64:67], v[96:99], v[176:179], v[64:67]\n"
        "ds_read_b64_tr_b16 v[112:113], v11 offset:24576\n"
        "v_mfma_f32_16x16x32_bf16 v[68:71], v[96:99], v[240:243], v[68:71]\n"
        "ds_read_b64_tr_b16 v[114:115], v11 offset:25088\n"
        "s_waitcnt lgkmcnt(4)\n"
        "v_mfma_f32_16x16x32_bf16 v[56:59], v[92:95], v[180:183], v[56:59]\n"
        "ds_read_b64_tr_b16 v[108:109], v10 offset:28672\n"
        "v_mfma_f32_16x16x32_bf16 v[60:63], v[92:95], v[244:247], v[60:63]\n"
        "ds_read_b64_tr_b16 v[110:111], v10 offset:29184\n"
        "v_mfma_f32_16x16x32_bf16 v[64:67], v[100:103], v[180:183], v[64:67]\n"
        "ds_read_b64_tr_b16 v[116:117], v11 offset:28672\n"
        "v_mfma_f32_16x16x32_bf16 v[68:71], v[100:103], v[244:247], v[68:71]\n"
        "ds_read_b64_tr_b16 v[118:119], v11 offset:29184\n"
        "v_smfmac_f32_16x16x64_bf16 v[80:83], v[28:31], v[88:95], v32\n"
        "v_smfmac_f32_16x16x64_bf16 v[84:87], v[28:31], v[96:103], v32\n"
        "s_waitcnt lgkmcnt(4)\n"
        "s_setprio 0\n"
        "v_mfma_f32_16x16x32_bf16 v[56:59], v[104:107], v[184:187], v[56:59]\n"
        "s_add_u32 m0, s56, 40960\n"
        "s_mul_i32 s7, s77, s57\n"
        "buffer_load_dwordx4 v4, s[16:19], s7 offen lds\n"
        "v_mfma_f32_16x16x32_bf16 v[60:63], v[104:107], v[248:251], v[60:63]\n"
        "v_mfma_f32_16x16x32_bf16 v[64:67], v[112:115], v[184:187], v[64:67]\n"
        "v_mfma_f32_16x16x32_bf16 v[68:71], v[112:115], v[248:251], v[68:71]\n"
        "s_waitcnt lgkmcnt(0)\n"
        "v_smfmac_f32_16x16x64_bf16 v[80:83], v[28:31], v[104:111], v32\n"
        "s_waitcnt vmcnt(2)\n"
        "v_xor_b32 v15, v73, v3\n"
        "v_bitop3_b32 v15, v75, v3, v15 bitop3:0xbe\n"
        "v_smfmac_f32_16x16x64_bf16 v[84:87], v[28:31], v[112:119], v32\n"
        "v_bitop3_b32 v15, v77, v3, v15 bitop3:0xbe\n"
        "v_bitop3_b32 v15, v79, v3, v15 bitop3:0xbe\n"
        "v_bitop3_b32 v15, v121, v3, v15 bitop3:0xbe\n"
        "v_mfma_f32_16x16x32_bf16 v[56:59], v[108:111], v[188:191], v[56:59]\n"
        "v_bitop3_b32 v15, v123, v3, v15 bitop3:0xbe\n"
        "v_bitop3_b32 v15, v125, v3, v15 bitop3:0xbe\n"
        "v_bitop3_b32 v15, v127, v3, v15 bitop3:0xbe\n"
        "v_mfma_f32_16x16x32_bf16 v[60:63], v[108:111], v[252:255], v[60:63]\n"
        "v_cmp_ne_u32 vcc, 0, v15\n"
        "s_cbranch_vccnz 103f\n"
        "104:\n"
        "v_add_f32 v72, v72, v76\n"
        "v_mfma_f32_16x16x32_bf16 v[64:67], v[116:119], v[188:191], v[64:67]\n"
        "v_add_f32 v73, v74, v78\n"
        "v_add_f32 v72, v72, v120\n"
        "v_add_f32 v73, v73, v122\n"
        "v_mfma_f32_16x16x32_bf16 v[68:71], v[116:119], v[252:255], v[68:71]\n"
        "v_add_f32 v72, v72, v124\n"
        "v_add_f32 v73, v73, v126\n"
        "ds_write_b64 v36, v[72:73]\n"
        "v_add_f32 v82, v80, v81\n"
        "v_add_f32 v86, v84, v85\n"
        "v_mov_b32 v80, 0\n"
        "v_mov_b32 v81, 0\n"
        "v_mov_b32 v84, 0\n"
        "v_mov_b32 v85, 0\n"
        "v_mfma_f32_16x16x4_f32 v[56:59], v82, v18, v[56:59]\n"
        "v_mfma_f32_16x16x4_f32 v[60:63], v82, v19, v[60:63]\n"
        "v_mfma_f32_16x16x4_f32 v[64:67], v86, v18, v[64:67]\n"
        "v_mfma_f32_16x16x4_f32 v[68:71], v86, v19, v[68:71]\n"
        "s_waitcnt lgkmcnt(0)\n"
        "s_barrier\n"
        "v_add_u32 v12, 114688, v12\n"
        "ds_read_b64_tr_b16 v[88:89], v12 offset:0\n"
        "ds_read_b64_tr_b16 v[90:91], v12 offset:256\n"
        "ds_read_b64_tr_b16 v[92:93], v12 offset:2048\n"
        "ds_read_b64_tr_b16 v[94:95], v12 offset:2304\n"
        "ds_read_b128 v[104:107], v37 offset:0\n"
        "ds_read_b128 v[108:111], v37 offset:16\n"
        "ds_read_b128 v[112:115], v37 offset:512\n"
        "ds_read_b128 v[116:119], v37 offset:528\n"
        "v_mul_f32 v56, v38, v56\n"
        "v_mul_f32 v57, v38, v57\n"
        "v_mul_f32 v58, v38, v58\n"
        "v_mul_f32 v59, v38, v59\n"
        "v_mul_f32 v60, v39, v60\n"
        "v_mul_f32 v61, v39, v61\n"
        "v_mul_f32 v62, v39, v62\n"
        "v_mul_f32 v63, v39, v63\n"
        "v_cvt_pk_bf16_f32 v120, v56, v57\n"
        "v_cvt_pk_bf16_f32 v121, v58, v59\n"
        "v_cvt_pk_bf16_f32 v122, v60, v61\n"
        "v_cvt_pk_bf16_f32 v123, v62, v63\n"
        "s_nop 0\n"
        "v_permlane16_swap_b32 v120, v122\n"
        "v_permlane16_swap_b32 v121, v123\n"
        "buffer_store_dwordx4 v[120:123], v2, s[20:23], s66 offen sc1\n"
        "s_waitcnt lgkmcnt(6)\n"
        "s_setprio 3\n"
        "v_mfma_f32_16x16x32_bf16 v[72:75], v[88:91], v[128:131], 0\n"
        "ds_read_b64_tr_b16 v[96:97], v12 offset:4096\n"
        "v_mul_f32 v64, v38, v64\n"
        "v_mul_f32 v65, v38, v65\n"
        "v_mul_f32 v66, v38, v66\n"
        "v_mfma_f32_16x16x32_bf16 v[76:79], v[88:91], v[192:195], 0\n"
        "ds_read_b64_tr_b16 v[98:99], v12 offset:4352\n"
        "v_mul_f32 v67, v38, v67\n"
        "v_mul_f32 v68, v39, v68\n"
        "s_waitcnt lgkmcnt(6)\n"
        "v_mfma_f32_16x16x32_bf16 v[72:75], v[92:95], v[132:135], v[72:75]\n"
        "ds_read_b64_tr_b16 v[100:101], v12 offset:6144\n"
        "v_mul_f32 v69, v39, v69\n"
        "v_mul_f32 v70, v39, v70\n"
        "v_mul_f32 v71, v39, v71\n"
        "v_mfma_f32_16x16x32_bf16 v[76:79], v[92:95], v[196:199], v[76:79]\n"
        "ds_read_b64_tr_b16 v[102:103], v12 offset:6400\n"
        "v_cvt_pk_bf16_f32 v124, v64, v65\n"
        "v_cvt_pk_bf16_f32 v125, v66, v67\n"
        "v_smfmac_f32_16x16x64_bf16 v[80:83], v[28:31], v[88:95], v32\n"
        "v_cvt_pk_bf16_f32 v126, v68, v69\n"
        "v_cvt_pk_bf16_f32 v127, v70, v71\n"
        "s_waitcnt lgkmcnt(2)\n"
        "v_mfma_f32_16x16x32_bf16 v[72:75], v[96:99], v[136:139], v[72:75]\n"
        "ds_read_b64_tr_b16 v[88:89], v12 offset:8192\n"
        "v_permlane16_swap_b32 v124, v126\n"
        "v_permlane16_swap_b32 v125, v127\n"
        "buffer_store_dwordx4 v[124:127], v2, s[20:23], s67 offen sc1\n"
        "v_mfma_f32_16x16x32_bf16 v[76:79], v[96:99], v[200:203], v[76:79]\n"
        "ds_read_b64_tr_b16 v[90:91], v12 offset:8448\n"
        "v_add_f32 v104, v104, v106\n"
        "v_add_f32 v105, v105, v107\n"
        "s_waitcnt lgkmcnt(2)\n"
        "v_mfma_f32_16x16x32_bf16 v[72:75], v[100:103], v[140:143], v[72:75]\n"
        "ds_read_b64_tr_b16 v[92:93], v12 offset:10240\n"
        "v_add_f32 v108, v108, v110\n"
        "v_add_f32 v109, v109, v111\n"
        "v_add_f32 v112, v112, v114\n"
        "v_mfma_f32_16x16x32_bf16 v[76:79], v[100:103], v[204:207], v[76:79]\n"
        "ds_read_b64_tr_b16 v[94:95], v12 offset:10496\n"
        "v_add_f32 v113, v113, v115\n"
        "v_add_f32 v116, v116, v118\n"
        "v_smfmac_f32_16x16x64_bf16 v[80:83], v[28:31], v[96:103], v32\n"
        "v_add_f32 v117, v117, v119\n"
        "v_add_f32 v104, v104, v108\n"
        "s_waitcnt lgkmcnt(2)\n"
        "v_mfma_f32_16x16x32_bf16 v[72:75], v[88:91], v[144:147], v[72:75]\n"
        "ds_read_b64_tr_b16 v[96:97], v12 offset:12288\n"
        "v_add_f32 v105, v105, v109\n"
        "v_add_f32 v112, v112, v116\n"
        "v_add_f32 v113, v113, v117\n"
        "v_mfma_f32_16x16x32_bf16 v[76:79], v[88:91], v[208:211], v[76:79]\n"
        "ds_read_b64_tr_b16 v[98:99], v12 offset:12544\n"
        "v_mul_f32 v104, 0x3a924925, v104\n"
        "v_mul_f32 v105, 0x3a924925, v105\n"
        "s_waitcnt lgkmcnt(2)\n"
        "v_mfma_f32_16x16x32_bf16 v[72:75], v[92:95], v[148:151], v[72:75]\n"
        "ds_read_b64_tr_b16 v[100:101], v12 offset:14336\n"
        "v_mul_f32 v112, 0x3a924925, v112\n"
        "v_mul_f32 v113, 0x3a924925, v113\n"
        "v_fma_f32 v34, -v104, v104, v105\n"
        "v_mfma_f32_16x16x32_bf16 v[76:79], v[92:95], v[212:215], v[76:79]\n"
        "ds_read_b64_tr_b16 v[102:103], v12 offset:14592\n"
        "v_fma_f32 v35, -v112, v112, v113\n"
        "v_max_f32 v34, 0, v34\n"
        "v_smfmac_f32_16x16x64_bf16 v[80:83], v[28:31], v[88:95], v32\n"
        "v_max_f32 v35, 0, v35\n"
        "v_add_f32 v34, 0x3727c5ac, v34\n"
        "s_add_u32 m0, s56, 49152\n"
        "s_mul_i32 s7, s78, s57\n"
        "buffer_load_dwordx4 v4, s[16:19], s7 offen lds\n"
        "s_waitcnt lgkmcnt(2)\n"
        "s_setprio 2\n"
        "v_mfma_f32_16x16x32_bf16 v[72:75], v[96:99], v[152:155], v[72:75]\n"
        "ds_read_b64_tr_b16 v[88:89], v12 offset:16384\n"
        "v_add_f32 v35, 0x3727c5ac, v35\n"
        "v_rsq_f32 v34, v34\n"
        "v_mfma_f32_16x16x32_bf16 v[76:79], v[96:99], v[216:219], v[76:79]\n"
        "ds_read_b64_tr_b16 v[90:91], v12 offset:16640\n"
        "v_rsq_f32 v35, v35\n"
        "s_waitcnt lgkmcnt(2)\n"
        "v_mfma_f32_16x16x32_bf16 v[72:75], v[100:103], v[156:159], v[72:75]\n"
        "ds_read_b64_tr_b16 v[92:93], v12 offset:18432\n"
        "v_mul_f32_e64 v8, -v104, v34\n"
        "v_mul_f32_e64 v9, -v112, v35\n"
        "v_fma_f32 v40, v34, v40, v8\n"
        "v_mfma_f32_16x16x32_bf16 v[76:79], v[100:103], v[220:223], v[76:79]\n"
        "ds_read_b64_tr_b16 v[94:95], v12 offset:18688\n"
        "v_fma_f32 v41, v34, v41, v8\n"
        "v_fma_f32 v42, v34, v42, v8\n"
        "v_smfmac_f32_16x16x64_bf16 v[80:83], v[28:31], v[96:103], v32\n"
        "v_fma_f32 v43, v34, v43, v8\n"
        "v_fma_f32 v48, v34, v48, v8\n"
        "s_add_u32 m0, s56, 57344\n"
        "s_mul_i32 s7, s79, s57\n"
        "buffer_load_dwordx4 v4, s[16:19], s7 offen lds\n"
        "s_waitcnt lgkmcnt(2)\n"
        "v_mfma_f32_16x16x32_bf16 v[72:75], v[88:91], v[160:163], v[72:75]\n"
        "ds_read_b64_tr_b16 v[96:97], v12 offset:20480\n"
        "v_fma_f32 v49, v34, v49, v8\n"
        "v_fma_f32 v50, v34, v50, v8\n"
        "v_fma_f32 v51, v34, v51, v8\n"
        "v_mfma_f32_16x16x32_bf16 v[76:79], v[88:91], v[224:227], v[76:79]\n"
        "ds_read_b64_tr_b16 v[98:99], v12 offset:20736\n"
        "v_cvt_pk_bf16_f32 v104, v40, v41\n"
        "v_cvt_pk_bf16_f32 v105, v42, v43\n"
        "s_waitcnt lgkmcnt(2)\n"
        "v_mfma_f32_16x16x32_bf16 v[72:75], v[92:95], v[164:167], v[72:75]\n"
        "ds_read_b64_tr_b16 v[100:101], v12 offset:22528\n"
        "v_cvt_pk_bf16_f32 v106, v48, v49\n"
        "v_cvt_pk_bf16_f32 v107, v50, v51\n"
        "v_mfma_f32_16x16x16_bf16 v[40:43], v[104:105], v[26:27], 0\n"
        "v_mfma_f32_16x16x32_bf16 v[76:79], v[92:95], v[228:231], v[76:79]\n"
        "ds_read_b64_tr_b16 v[102:103], v12 offset:22784\n"
        "v_mfma_f32_16x16x16_bf16 v[48:51], v[106:107], v[26:27], 0\n"
        "v_fma_f32 v44, v35, v44, v9\n"
        "v_smfmac_f32_16x16x64_bf16 v[80:83], v[28:31], v[88:95], v32\n"
        "v_fma_f32 v45, v35, v45, v9\n"
        "v_fma_f32 v46, v35, v46, v9\n"
        "s_waitcnt lgkmcnt(2)\n"
        "s_setprio 1\n"
        "v_mfma_f32_16x16x32_bf16 v[72:75], v[96:99], v[168:171], v[72:75]\n"
        "ds_read_b64_tr_b16 v[88:89], v12 offset:24576\n"
        "v_fma_f32 v47, v35, v47, v9\n"
        "v_fma_f32 v52, v35, v52, v9\n"
        "v_fma_f32 v53, v35, v53, v9\n"
        "v_mfma_f32_16x16x32_bf16 v[76:79], v[96:99], v[232:235], v[76:79]\n"
        "ds_read_b64_tr_b16 v[90:91], v12 offset:24832\n"
        "v_fma_f32 v54, v35, v54, v9\n"
        "v_fma_f32 v55, v35, v55, v9\n"
        "s_waitcnt lgkmcnt(2)\n"
        "v_mfma_f32_16x16x32_bf16 v[72:75], v[100:103], v[172:175], v[72:75]\n"
        "ds_read_b64_tr_b16 v[92:93], v12 offset:26624\n"
        "v_cvt_pk_bf16_f32 v108, v44, v45\n"
        "v_cvt_pk_bf16_f32 v109, v46, v47\n"
        "v_cvt_pk_bf16_f32 v110, v52, v53\n"
        "v_mfma_f32_16x16x32_bf16 v[76:79], v[100:103], v[236:239], v[76:79]\n"
        "ds_read_b64_tr_b16 v[94:95], v12 offset:26880\n"
        "v_cvt_pk_bf16_f32 v111, v54, v55\n"
        "v_mfma_f32_16x16x16_bf16 v[44:47], v[108:109], v[26:27], 0\n"
        "v_smfmac_f32_16x16x64_bf16 v[80:83], v[28:31], v[96:103], v32\n"
        "v_mfma_f32_16x16x16_bf16 v[52:55], v[110:111], v[26:27], 0\n"
        "v_cvt_pk_bf16_f32 v104, v40, v41\n"
        "s_waitcnt lgkmcnt(2)\n"
        "v_mfma_f32_16x16x32_bf16 v[72:75], v[88:91], v[176:179], v[72:75]\n"
        "ds_read_b64_tr_b16 v[96:97], v12 offset:28672\n"
        "v_cvt_pk_bf16_f32 v105, v42, v43\n"
        "v_cvt_pk_bf16_f32 v106, v48, v49\n"
        "v_cvt_pk_bf16_f32 v107, v50, v51\n"
        "v_mfma_f32_16x16x32_bf16 v[76:79], v[88:91], v[240:243], v[76:79]\n"
        "ds_read_b64_tr_b16 v[98:99], v12 offset:28928\n"
        "v_permlane16_swap_b32 v104, v106\n"
        "v_permlane16_swap_b32 v105, v107\n"
        "s_waitcnt lgkmcnt(2)\n"
        "v_mfma_f32_16x16x32_bf16 v[72:75], v[92:95], v[180:183], v[72:75]\n"
        "ds_read_b64_tr_b16 v[100:101], v12 offset:30720\n"
        "buffer_store_dwordx4 v[104:107], v2, s[40:43], s69 offen offset:0 sc1\n"
        "v_cvt_pk_bf16_f32 v108, v44, v45\n"
        "v_cvt_pk_bf16_f32 v109, v46, v47\n"
        "v_mfma_f32_16x16x32_bf16 v[76:79], v[92:95], v[244:247], v[76:79]\n"
        "ds_read_b64_tr_b16 v[102:103], v12 offset:30976\n"
        "v_cvt_pk_bf16_f32 v110, v52, v53\n"
        "v_cvt_pk_bf16_f32 v111, v54, v55\n"
        "s_nop 0\n"
        "v_permlane16_swap_b32 v108, v110\n"
        "v_smfmac_f32_16x16x64_bf16 v[80:83], v[28:31], v[88:95], v32\n"
        "v_permlane16_swap_b32 v109, v111\n"
        "buffer_store_dwordx4 v[108:111], v2, s[40:43], s69 offen offset:1024 sc1\n"
        "s_waitcnt lgkmcnt(2)\n"
        "v_subrev_u32 v10, 81920, v10\n"
        "v_subrev_u32 v11, 81920, v11\n"
        "s_setprio 0\n"
        "v_mfma_f32_16x16x32_bf16 v[72:75], v[96:99], v[184:187], v[72:75]\n"
        "ds_read_b64_tr_b16 v[104:105], v10 offset:0\n"
        "v_mfma_f32_16x16x32_bf16 v[76:79], v[96:99], v[248:251], v[76:79]\n"
        "ds_read_b64_tr_b16 v[106:107], v10 offset:512\n"
        "s_waitcnt lgkmcnt(2)\n"
        "v_smfmac_f32_16x16x64_bf16 v[80:83], v[28:31], v[96:103], v32\n"
        "ds_read_b64_tr_b16 v[112:113], v11 offset:0\n"
        "v_mfma_f32_16x16x32_bf16 v[72:75], v[100:103], v[188:191], v[72:75]\n"
        "ds_read_b64_tr_b16 v[114:115], v11 offset:512\n"
        "v_mfma_f32_16x16x32_bf16 v[76:79], v[100:103], v[252:255], v[76:79]\n"
        "ds_read_b64_tr_b16 v[108:109], v10 offset:4096\n"
        "ds_read_b64_tr_b16 v[110:111], v10 offset:4608\n"
        "ds_read_b64_tr_b16 v[116:117], v11 offset:4096\n"
        "ds_read_b64_tr_b16 v[118:119], v11 offset:4608\n"
        "v_add_f32 v82, v80, v81\n"
        "v_mov_b32 v80, 0\n"
        "v_mov_b32 v81, 0\n"
        "v_mfma_f32_16x16x4_f32 v[72:75], v82, v18, v[72:75]\n"
        "v_mfma_f32_16x16x4_f32 v[76:79], v82, v19, v[76:79]\n"
        "s_waitcnt vmcnt(2)\n"
        "s_barrier\n"
        "s_waitcnt lgkmcnt(4)\n"
        "s_setprio 3\n"
        "v_mfma_f32_16x16x32_bf16 v[56:59], v[104:107], v[128:131], 0\n"
        "ds_read_b64_tr_b16 v[88:89], v10 offset:8192\n"
        "s_nop 2\n"
        "v_mul_f32 v72, v16, v72\n"
        "v_mul_f32 v73, v16, v73\n"
        "v_mul_f32 v74, v16, v74\n"
        "v_mfma_f32_16x16x32_bf16 v[60:63], v[104:107], v[192:195], 0\n"
        "ds_read_b64_tr_b16 v[90:91], v10 offset:8704\n"
        "v_mul_f32 v75, v16, v75\n"
        "v_mul_f32 v76, v17, v76\n"
        "v_mfma_f32_16x16x32_bf16 v[64:67], v[112:115], v[128:131], 0\n"
        "ds_read_b64_tr_b16 v[96:97], v11 offset:8192\n"
        "v_mul_f32 v77, v17, v77\n"
        "v_mul_f32 v78, v17, v78\n"
        "v_mfma_f32_16x16x32_bf16 v[68:71], v[112:115], v[192:195], 0\n"
        "ds_read_b64_tr_b16 v[98:99], v11 offset:8704\n"
        "v_mul_f32 v79, v17, v79\n"
        "v_cvt_pk_bf16_f32 v120, v72, v73\n"
        "s_waitcnt lgkmcnt(4)\n"
        "v_mfma_f32_16x16x32_bf16 v[56:59], v[108:111], v[132:135], v[56:59]\n"
        "ds_read_b64_tr_b16 v[92:93], v10 offset:12288\n"
        "v_cvt_pk_bf16_f32 v121, v74, v75\n"
        "v_cvt_pk_bf16_f32 v122, v76, v77\n"
        "v_mfma_f32_16x16x32_bf16 v[60:63], v[108:111], v[196:199], v[60:63]\n"
        "ds_read_b64_tr_b16 v[94:95], v10 offset:12800\n"
        "v_cvt_pk_bf16_f32 v123, v78, v79\n"
        "v_mfma_f32_16x16x16_bf16 v[72:75], v[120:121], v[26:27], 0\n"
        "v_mfma_f32_16x16x32_bf16 v[64:67], v[116:119], v[132:135], v[64:67]\n"
        "ds_read_b64_tr_b16 v[100:101], v11 offset:12288\n"
        "v_mfma_f32_16x16x16_bf16 v[76:79], v[122:123], v[26:27], 0\n"
        "s_nop 4\n"
        "v_cvt_pk_bf16_f32 v120, v72, v73\n"
        "v_mfma_f32_16x16x32_bf16 v[68:71], v[116:119], v[196:199], v[68:71]\n"
        "ds_read_b64_tr_b16 v[102:103], v11 offset:12800\n"
        "v_cvt_pk_bf16_f32 v121, v74, v75\n"
        "v_cvt_pk_bf16_f32 v122, v76, v77\n"
        "v_smfmac_f32_16x16x64_bf16 v[80:83], v[28:31], v[104:111], v32\n"
        "v_cvt_pk_bf16_f32 v123, v78, v79\n"
        "v_permlane16_swap_b32 v120, v122\n"
        "v_smfmac_f32_16x16x64_bf16 v[84:87], v[28:31], v[112:119], v32\n"
        "v_permlane16_swap_b32 v121, v123\n"
        "buffer_store_dwordx4 v[120:123], v13, s[20:23], s68 offen sc1\n"
        "s_waitcnt lgkmcnt(4)\n"
        "v_mfma_f32_16x16x32_bf16 v[56:59], v[88:91], v[136:139], v[56:59]\n"
        "ds_read_b64_tr_b16 v[104:105], v10 offset:16384\n"
        "s_mov_b32 s56, s80\n"
        "s_cmp_ge_u32 s50, 28\n"
        "s_cselect_b64 vcc, -1, 0\n"
        "v_and_b32 v54, 7, v1\n"
        "v_mfma_f32_16x16x32_bf16 v[60:63], v[88:91], v[200:203], v[60:63]\n"
        "ds_read_b64_tr_b16 v[106:107], v10 offset:16896\n"
        "v_lshrrev_b32 v53, 4, v1\n"
        "v_mfma_f32_16x16x32_bf16 v[64:67], v[96:99], v[136:139], v[64:67]\n"
        "ds_read_b64_tr_b16 v[112:113], v11 offset:16384\n"
        "v_xor_b32 v54, v54, v53\n"
        "v_mfma_f32_16x16x32_bf16 v[68:71], v[96:99], v[200:203], v[68:71]\n"
        "ds_read_b64_tr_b16 v[114:115], v11 offset:16896\n"
        "v_lshl_add_u32 v12, v54, 4, s81\n"
        "s_waitcnt lgkmcnt(4)\n"
        "v_mfma_f32_16x16x32_bf16 v[56:59], v[92:95], v[140:143], v[56:59]\n"
        "ds_read_b64_tr_b16 v[108:109], v10 offset:20480\n"
        "v_lshrrev_b32 v55, 3, v1\n"
        "v_mfma_f32_16x16x32_bf16 v[60:63], v[92:95], v[204:207], v[60:63]\n"
        "ds_read_b64_tr_b16 v[110:111], v10 offset:20992\n"
        "v_lshl_add_u32 v12, v55, 12, v12\n"
        "v_mfma_f32_16x16x32_bf16 v[64:67], v[100:103], v[140:143], v[64:67]\n"
        "ds_read_b64_tr_b16 v[116:117], v11 offset:20480\n"
        "v_xor_b32 v13, 64, v12\n"
        "v_add_u32 v13, 31744, v13\n"
        "v_mfma_f32_16x16x32_bf16 v[68:71], v[100:103], v[204:207], v[68:71]\n"
        "ds_read_b64_tr_b16 v[118:119], v11 offset:20992\n"
        "v_mov_b32 v40, 2147483632\n"
        "v_smfmac_f32_16x16x64_bf16 v[80:83], v[28:31], v[88:95], v32\n"
        "v_writelane_b32 v40, s82, 0\n"
        "v_smfmac_f32_16x16x64_bf16 v[84:87], v[28:31], v[96:103], v32\n"
        "s_add_u32 m0, s56, 24576\n"
        "s_movk_i32 s7, 0\n"
        "s_cbranch_vccnz 106f\n"
        "buffer_load_dwordx4 v12, s[24:27], s7 offen offset:0 nt lds\n"
        "107:\n"
        "s_waitcnt lgkmcnt(4)\n"
        "v_mfma_f32_16x16x32_bf16 v[56:59], v[104:107], v[144:147], v[56:59]\n"
        "ds_read_b64_tr_b16 v[88:89], v10 offset:24576\n"
        "v_add_u32 v15, s85, v2\n"
        "v_mfma_f32_16x16x32_bf16 v[60:63], v[104:107], v[208:211], v[60:63]\n"
        "ds_read_b64_tr_b16 v[90:91], v10 offset:25088\n"
        "v_and_b32 v55, 31, v1\n"
        "v_mfma_f32_16x16x32_bf16 v[64:67], v[112:115], v[144:147], v[64:67]\n"
        "ds_read_b64_tr_b16 v[96:97], v11 offset:24576\n"
        "v_lshl_add_u32 v26, v55, 1, s86\n"
        "v_mfma_f32_16x16x32_bf16 v[68:71], v[112:115], v[208:211], v[68:71]\n"
        "ds_read_b64_tr_b16 v[98:99], v11 offset:25088\n"
        "s_waitcnt lgkmcnt(4)\n"
        "v_mfma_f32_16x16x32_bf16 v[56:59], v[108:111], v[148:151], v[56:59]\n"
        "ds_read_b64_tr_b16 v[92:93], v10 offset:28672\n"
        "s_cbranch_vccnz 108f\n"
        "buffer_load_dwordx4 v13, s[24:27], s7 offen offset:1024 nt lds\n"
        "109:\n"
        "buffer_load_ushort v26, v26, s[36:39], 0 offen\n"
        "v_mfma_f32_16x16x32_bf16 v[60:63], v[108:111], v[212:215], v[60:63]\n"
        "ds_read_b64_tr_b16 v[94:95], v10 offset:29184\n"
        "v_bfe_u32 v4, v1, 4, 1\n"
        "v_mfma_f32_16x16x32_bf16 v[64:67], v[116:119], v[148:151], v[64:67]\n"
        "ds_read_b64_tr_b16 v[100:101], v11 offset:28672\n"
        "v_mul_u32_u24 v4, 0xc00, v4\n"
        "v_mfma_f32_16x16x32_bf16 v[68:71], v[116:119], v[212:215], v[68:71]\n"
        "ds_read_b64_tr_b16 v[102:103], v11 offset:29184\n"
        "v_smfmac_f32_16x16x64_bf16 v[80:83], v[28:31], v[104:111], v32\n"
        "v_lshrrev_b32 v53, 5, v1\n"
        "v_smfmac_f32_16x16x64_bf16 v[84:87], v[28:31], v[112:119], v32\n"
        "s_add_u32 m0, s56, 36864\n"
        "s_movk_i32 s7, 128\n"
        "s_cbranch_vccnz 110f\n"
        "buffer_load_dwordx4 v12, s[24:27], s7 offen offset:0 nt lds\n"
        "111:\n"
        "s_waitcnt lgkmcnt(4)\n"
        "s_setprio 2\n"
        "v_mfma_f32_16x16x32_bf16 v[56:59], v[88:91], v[152:155], v[56:59]\n"
        "ds_read_b64_tr_b16 v[104:105], v10 offset:32768\n"
        "v_bfe_u32 v52, v1, 1, 3\n"
        "v_mfma_f32_16x16x32_bf16 v[60:63], v[88:91], v[216:219], v[60:63]\n"
        "ds_read_b64_tr_b16 v[106:107], v10 offset:33280\n"
        "v_xor_b32 v53, v53, v52\n"
        "v_mfma_f32_16x16x32_bf16 v[64:67], v[96:99], v[152:155], v[64:67]\n"
        "ds_read_b64_tr_b16 v[112:113], v11 offset:32768\n"
        "v_lshl_add_u32 v55, v53, 4, s87\n"
        "v_mfma_f32_16x16x32_bf16 v[68:71], v[96:99], v[216:219], v[68:71]\n"
        "ds_read_b64_tr_b16 v[114:115], v11 offset:33280\n"
        "s_cbranch_vccnz 112f\n"
        "buffer_load_dwordx4 v13, s[24:27], s7 offen offset:1024 nt lds\n"
        "113:\n"
        "s_waitcnt lgkmcnt(4)\n"
        "v_mfma_f32_16x16x32_bf16 v[56:59], v[92:95], v[156:159], v[56:59]\n"
        "ds_read_b64_tr_b16 v[108:109], v10 offset:36864\n"
        "v_and_b32 v54, 15, v1\n"
        "v_mfma_f32_16x16x32_bf16 v[60:63], v[92:95], v[220:223], v[60:63]\n"
        "ds_read_b64_tr_b16 v[110:111], v10 offset:37376\n"
        "v_lshl_add_u32 v55, v54, 7, v55\n"
        "v_mfma_f32_16x16x32_bf16 v[64:67], v[100:103], v[156:159], v[64:67]\n"
        "ds_read_b64_tr_b16 v[116:117], v11 offset:36864\n"
        "v_add_u32 v4, v4, v55\n"
        "v_mfma_f32_16x16x32_bf16 v[68:71], v[100:103], v[220:223], v[68:71]\n"
        "ds_read_b64_tr_b16 v[118:119], v11 offset:37376\n"
        "v_smfmac_f32_16x16x64_bf16 v[80:83], v[28:31], v[88:95], v32\n"
        "v_xor_b32 v5, 32, v4\n"
        "v_smfmac_f32_16x16x64_bf16 v[84:87], v[28:31], v[96:103], v32\n"
        "s_add_u32 m0, s56, 49152\n"
        "s_movk_i32 s7, 256\n"
        "s_cbranch_vccnz 114f\n"
        "buffer_load_dwordx4 v12, s[24:27], s7 offen offset:0 nt lds\n"
        "115:\n"
        "s_waitcnt lgkmcnt(4)\n"
        "v_mfma_f32_16x16x32_bf16 v[56:59], v[104:107], v[160:163], v[56:59]\n"
        "ds_read_b64_tr_b16 v[88:89], v10 offset:40960\n"
        "v_xor_b32 v6, 64, v4\n"
        "v_mfma_f32_16x16x32_bf16 v[60:63], v[104:107], v[224:227], v[60:63]\n"
        "ds_read_b64_tr_b16 v[90:91], v10 offset:41472\n"
        "v_xor_b32 v7, 96, v4\n"
        "v_mfma_f32_16x16x32_bf16 v[64:67], v[112:115], v[160:163], v[64:67]\n"
        "ds_read_b64_tr_b16 v[96:97], v11 offset:40960\n"
        "v_add_u32 v8, s87, v2\n"
        "v_mfma_f32_16x16x32_bf16 v[68:71], v[112:115], v[224:227], v[68:71]\n"
        "ds_read_b64_tr_b16 v[98:99], v11 offset:41472\n"
        "s_cbranch_vccnz 116f\n"
        "buffer_load_dwordx4 v13, s[24:27], s7 offen offset:1024 nt lds\n"
        "117:\n"
        "v_lshrrev_b32 v54, 5, v1\n"
        "s_waitcnt lgkmcnt(4)\n"
        "v_mfma_f32_16x16x32_bf16 v[56:59], v[108:111], v[164:167], v[56:59]\n"
        "ds_read_b64_tr_b16 v[92:93], v10 offset:45056\n"
        "v_mfma_f32_16x16x32_bf16 v[60:63], v[108:111], v[228:231], v[60:63]\n"
        "ds_read_b64_tr_b16 v[94:95], v10 offset:45568\n"
        "v_lshl_add_u32 v9, v54, 8, s87\n"
        "v_mfma_f32_16x16x32_bf16 v[64:67], v[116:119], v[164:167], v[64:67]\n"
        "ds_read_b64_tr_b16 v[100:101], v11 offset:45056\n"
        "v_and_b32 v55, 15, v1\n"
        "v_mfma_f32_16x16x32_bf16 v[68:71], v[116:119], v[228:231], v[68:71]\n"
        "ds_read_b64_tr_b16 v[102:103], v11 offset:45568\n"
        "v_lshl_add_u32 v9, v55, 4, v9\n"
        "v_smfmac_f32_16x16x64_bf16 v[80:83], v[28:31], v[104:111], v32\n"
        "s_waitcnt vmcnt(7)\n"
        "s_barrier\n"
        "buffer_store_dword v3, v40, s[20:23], 0 offen sc1\n"
        "v_smfmac_f32_16x16x64_bf16 v[84:87], v[28:31], v[112:119], v32\n"
        "s_waitcnt lgkmcnt(4)\n"
        "s_setprio 1\n"
        "v_mfma_f32_16x16x32_bf16 v[56:59], v[88:91], v[168:171], v[56:59]\n"
        "ds_read_b64_tr_b16 v[104:105], v10 offset:49152\n"
        "s_lshl_b32 s64, s52, 8\n"
        "v_mfma_f32_16x16x32_bf16 v[60:63], v[88:91], v[232:235], v[60:63]\n"
        "ds_read_b64_tr_b16 v[106:107], v10 offset:49664\n"
        "s_add_u32 s64, s64, 32768\n"
        "v_mfma_f32_16x16x32_bf16 v[64:67], v[96:99], v[168:171], v[64:67]\n"
        "ds_read_b64_tr_b16 v[112:113], v11 offset:49152\n"
        "v_lshl_add_u32 v14, v1, 2, s64\n"
        "v_mfma_f32_16x16x32_bf16 v[68:71], v[96:99], v[232:235], v[68:71]\n"
        "ds_read_b64_tr_b16 v[114:115], v11 offset:49664\n"
        "s_waitcnt lgkmcnt(4)\n"
        "v_mfma_f32_16x16x32_bf16 v[56:59], v[92:95], v[172:175], v[56:59]\n"
        "ds_read_b64_tr_b16 v[108:109], v10 offset:53248\n"
        "v_mov_b32 v27, 2147483632\n"
        "v_mfma_f32_16x16x32_bf16 v[60:63], v[92:95], v[236:239], v[60:63]\n"
        "ds_read_b64_tr_b16 v[110:111], v10 offset:53760\n"
        "v_mfma_f32_16x16x32_bf16 v[64:67], v[100:103], v[172:175], v[64:67]\n"
        "ds_read_b64_tr_b16 v[116:117], v11 offset:53248\n"
        "v_writelane_b32 v27, s88, 0\n"
        "v_mfma_f32_16x16x32_bf16 v[68:71], v[100:103], v[236:239], v[68:71]\n"
        "ds_read_b64_tr_b16 v[118:119], v11 offset:53760\n"
        "v_smfmac_f32_16x16x64_bf16 v[80:83], v[28:31], v[88:95], v32\n"
        "s_mov_b64 s[12:13], s[46:47]\n"
        "v_smfmac_f32_16x16x64_bf16 v[84:87], v[28:31], v[96:103], v32\n"
        "s_mov_b32 s14, 917504\n"
        "s_waitcnt lgkmcnt(4)\n"
        "v_mfma_f32_16x16x32_bf16 v[56:59], v[104:107], v[176:179], v[56:59]\n"
        "ds_read_b64_tr_b16 v[88:89], v10 offset:57344\n"
        "v_and_b32 v46, 31, v1\n"
        "v_mfma_f32_16x16x32_bf16 v[60:63], v[104:107], v[240:243], v[60:63]\n"
        "ds_read_b64_tr_b16 v[90:91], v10 offset:57856\n"
        "v_cmp_eq_u32 vcc, 16, v46\n"
        "v_mfma_f32_16x16x32_bf16 v[64:67], v[112:115], v[176:179], v[64:67]\n"
        "ds_read_b64_tr_b16 v[96:97], v11 offset:57344\n"
        "v_mov_b32 v47, 0x3f803f80\n"
        "v_mfma_f32_16x16x32_bf16 v[68:71], v[112:115], v[240:243], v[68:71]\n"
        "ds_read_b64_tr_b16 v[98:99], v11 offset:57856\n"
        "s_waitcnt lgkmcnt(4)\n"
        "v_mfma_f32_16x16x32_bf16 v[56:59], v[108:111], v[180:183], v[56:59]\n"
        "ds_read_b64_tr_b16 v[92:93], v10 offset:61440\n"
        "v_cndmask_b32 v140, 0, v47, vcc\n"
        "v_mfma_f32_16x16x32_bf16 v[60:63], v[108:111], v[244:247], v[60:63]\n"
        "ds_read_b64_tr_b16 v[94:95], v10 offset:61952\n"
        "v_mov_b32 v141, v140\n"
        "v_mfma_f32_16x16x32_bf16 v[64:67], v[116:119], v[180:183], v[64:67]\n"
        "ds_read_b64_tr_b16 v[100:101], v11 offset:61440\n"
        "v_mfma_f32_16x16x32_bf16 v[68:71], v[116:119], v[244:247], v[68:71]\n"
        "ds_read_b64_tr_b16 v[102:103], v11 offset:61952\n"
        "v_mov_b32 v142, v140\n"
        "v_smfmac_f32_16x16x64_bf16 v[80:83], v[28:31], v[104:111], v32\n"
        "v_mov_b32 v143, v140\n"
        "v_smfmac_f32_16x16x64_bf16 v[84:87], v[28:31], v[112:119], v32\n"
        "s_waitcnt lgkmcnt(4)\n"
        "s_setprio 0\n"
        "v_mfma_f32_16x16x32_bf16 v[56:59], v[88:91], v[184:187], v[56:59]\n"
        "v_mov_b32 v144, v140\n"
        "v_mfma_f32_16x16x32_bf16 v[60:63], v[88:91], v[248:251], v[60:63]\n"
        "v_mov_b32 v145, v140\n"
        "v_mfma_f32_16x16x32_bf16 v[64:67], v[96:99], v[184:187], v[64:67]\n"
        "v_mov_b32 v146, v140\n"
        "v_mfma_f32_16x16x32_bf16 v[68:71], v[96:99], v[248:251], v[68:71]\n"
        "v_mov_b32 v147, v140\n"
        "s_waitcnt lgkmcnt(0)\n"
        "v_smfmac_f32_16x16x64_bf16 v[80:83], v[28:31], v[88:95], v32\n"
        "v_smfmac_f32_16x16x64_bf16 v[84:87], v[28:31], v[96:103], v32\n"
        "v_mov_b32 v164, 0\n"
        "v_mfma_f32_16x16x32_bf16 v[56:59], v[92:95], v[188:191], v[56:59]\n"
        "v_mov_b32 v165, 0\n"
        "v_mfma_f32_16x16x32_bf16 v[60:63], v[92:95], v[252:255], v[60:63]\n"
        "v_mov_b32 v166, 0\n"
        "v_mfma_f32_16x16x32_bf16 v[64:67], v[100:103], v[188:191], v[64:67]\n"
        "v_mov_b32 v167, 0\n"
        "v_mfma_f32_16x16x32_bf16 v[68:71], v[100:103], v[252:255], v[68:71]\n"
        "v_add_f32 v82, v80, v81\n"
        "v_add_f32 v86, v84, v85\n"
        "v_mov_b32 v80, 0\n"
        "v_mov_b32 v81, 0\n"
        "v_mov_b32 v84, 0\n"
        "v_mov_b32 v85, 0\n"
        "v_mfma_f32_16x16x4_f32 v[56:59], v82, v18, v[56:59]\n"
        "v_mfma_f32_16x16x4_f32 v[60:63], v82, v19, v[60:63]\n"
        "v_mfma_f32_16x16x4_f32 v[64:67], v86, v18, v[64:67]\n"
        "v_mfma_f32_16x16x4_f32 v[68:71], v86, v19, v[68:71]\n"
        "buffer_load_dword v43, v33, s[20:23], 0 offen sc1\n"
        "s_nop 5\n"
        "v_pk_mul_f32 v[120:121], v[56:57], v[56:57]\n"
        "v_pk_mul_f32 v[122:123], v[58:59], v[58:59]\n"
        "v_pk_mul_f32 v[124:125], v[60:61], v[60:61]\n"
        "v_pk_mul_f32 v[126:127], v[62:63], v[62:63]\n"
        "v_pk_fma_f32 v[120:121], v[22:23], v[120:121], v[20:21] op_sel:[0,0,0] op_sel_hi:[0,1,0]\n"
        "v_pk_fma_f32 v[122:123], v[22:23], v[122:123], v[20:21] op_sel:[0,0,0] op_sel_hi:[0,1,0]\n"
        "v_pk_fma_f32 v[124:125], v[22:23], v[124:125], v[20:21] op_sel:[1,0,1] op_sel_hi:[1,1,1]\n"
        "v_pk_fma_f32 v[126:127], v[22:23], v[126:127], v[20:21] op_sel:[1,0,1] op_sel_hi:[1,1,1]\n"
        "v_pk_mul_f32 v[120:121], v[120:121], v[56:57]\n"
        "v_pk_mul_f32 v[122:123], v[122:123], v[58:59]\n"
        "v_pk_mul_f32 v[124:125], v[124:125], v[60:61]\n"
        "v_pk_mul_f32 v[126:127], v[126:127], v[62:63]\n"
        "v_exp_f32 v120, v120\n"
        "v_exp_f32 v121, v121\n"
        "v_exp_f32 v122, v122\n"
        "v_exp_f32 v123, v123\n"
        "v_exp_f32 v124, v124\n"
        "v_exp_f32 v125, v125\n"
        "v_exp_f32 v126, v126\n"
        "v_exp_f32 v127, v127\n"
        "v_pk_fma_f32 v[120:121], v[24:25], v[120:121], v[24:25] op_sel:[0,0,0] op_sel_hi:[0,1,0]\n"
        "v_pk_fma_f32 v[122:123], v[24:25], v[122:123], v[24:25] op_sel:[0,0,0] op_sel_hi:[0,1,0]\n"
        "v_pk_fma_f32 v[124:125], v[24:25], v[124:125], v[24:25] op_sel:[1,0,1] op_sel_hi:[1,1,1]\n"
        "v_pk_fma_f32 v[126:127], v[24:25], v[126:127], v[24:25] op_sel:[1,0,1] op_sel_hi:[1,1,1]\n"
        "v_rcp_f32 v120, v120\n"
        "v_rcp_f32 v121, v121\n"
        "v_rcp_f32 v122, v122\n"
        "v_rcp_f32 v123, v123\n"
        "v_rcp_f32 v124, v124\n"
        "v_rcp_f32 v125, v125\n"
        "v_rcp_f32 v126, v126\n"
        "v_rcp_f32 v127, v127\n"
        "v_pk_mul_f32 v[56:57], v[56:57], v[120:121]\n"
        "v_pk_mul_f32 v[58:59], v[58:59], v[122:123]\n"
        "v_pk_mul_f32 v[60:61], v[60:61], v[124:125]\n"
        "v_pk_mul_f32 v[62:63], v[62:63], v[126:127]\n"
        "v_pk_mul_f32 v[120:121], v[64:65], v[64:65]\n"
        "v_pk_mul_f32 v[122:123], v[66:67], v[66:67]\n"
        "v_pk_mul_f32 v[124:125], v[68:69], v[68:69]\n"
        "v_pk_mul_f32 v[126:127], v[70:71], v[70:71]\n"
        "v_pk_fma_f32 v[120:121], v[22:23], v[120:121], v[20:21] op_sel:[0,0,0] op_sel_hi:[0,1,0]\n"
        "v_pk_fma_f32 v[122:123], v[22:23], v[122:123], v[20:21] op_sel:[0,0,0] op_sel_hi:[0,1,0]\n"
        "s_waitcnt vmcnt(0)\n"
        "v_cmp_ne_u32 vcc, v43, v3\n"
        "s_cbranch_vccnz 118f\n"
        "119:\n"
        "s_barrier\n"
        "buffer_load_dwordx4 v[128:131], v2, s[20:23], s1 offen sc1\n"
        "buffer_load_dwordx4 v[132:135], v2, s[20:23], s0 offen sc1\n"
        "buffer_load_dwordx4 v[180:183], v2, s[20:23], s84 offen sc1\n"
        "s_cmp_lt_u32 s50, 28\n"
        "s_cbranch_scc0 120f\n"
        "s_add_u32 m0, s56, 24576\n"
        "s_mov_b32 s63, 0\n"
        "buffer_load_dwordx4 v15, s[20:23], s63 offen offset:2048 sc1 lds\n"
        "v_pk_fma_f32 v[124:125], v[22:23], v[124:125], v[20:21] op_sel:[1,0,1] op_sel_hi:[1,1,1]\n"
        "s_add_u32 m0, s56, 36864\n"
        "s_mov_b32 s63, 4096\n"
        "buffer_load_dwordx4 v15, s[20:23], s63 offen offset:2048 sc1 lds\n"
        "v_pk_fma_f32 v[126:127], v[22:23], v[126:127], v[20:21] op_sel:[1,0,1] op_sel_hi:[1,1,1]\n"
        "v_pk_mul_f32 v[120:121], v[120:121], v[64:65]\n"
        "v_pk_mul_f32 v[122:123], v[122:123], v[66:67]\n"
        "v_pk_mul_f32 v[124:125], v[124:125], v[68:69]\n"
        "v_pk_mul_f32 v[126:127], v[126:127], v[70:71]\n"
        "v_exp_f32 v120, v120\n"
        "v_exp_f32 v121, v121\n"
        "v_exp_f32 v122, v122\n"
        "v_exp_f32 v123, v123\n"
        "v_exp_f32 v124, v124\n"
        "s_add_u32 m0, s56, 49152\n"
        "s_mov_b32 s63, 8192\n"
        "buffer_load_dwordx4 v15, s[20:23], s63 offen offset:2048 sc1 lds\n"
        "v_exp_f32 v125, v125\n"
        "v_exp_f32 v126, v126\n"
        "v_exp_f32 v127, v127\n"
        "v_pk_fma_f32 v[120:121], v[24:25], v[120:121], v[24:25] op_sel:[0,0,0] op_sel_hi:[0,1,0]\n"
        "v_pk_fma_f32 v[122:123], v[24:25], v[122:123], v[24:25] op_sel:[0,0,0] op_sel_hi:[0,1,0]\n"
        "v_pk_fma_f32 v[124:125], v[24:25], v[124:125], v[24:25] op_sel:[1,0,1] op_sel_hi:[1,1,1]\n"
        "v_pk_fma_f32 v[126:127], v[24:25], v[126:127], v[24:25] op_sel:[1,0,1] op_sel_hi:[1,1,1]\n"
        "v_rcp_f32 v120, v120\n"
        "v_rcp_f32 v121, v121\n"
        "v_rcp_f32 v122, v122\n"
        "v_rcp_f32 v123, v123\n"
        "s_mov_b32 s62, 0\n"
        "buffer_load_dwordx4 v[188:191], v0, s[40:43], s62 offen offset:0 sc1\n"
        "v_rcp_f32 v124, v124\n"
        "v_rcp_f32 v125, v125\n"
        "v_rcp_f32 v126, v126\n"
        "v_rcp_f32 v127, v127\n"
        "v_pk_mul_f32 v[64:65], v[64:65], v[120:121]\n"
        "v_pk_mul_f32 v[66:67], v[66:67], v[122:123]\n"
        "v_pk_mul_f32 v[68:69], v[68:69], v[124:125]\n"
        "v_pk_mul_f32 v[70:71], v[70:71], v[126:127]\n"
        "v_cvt_pk_bf16_f32 v16, v56, v57\n"
        "v_cvt_pk_bf16_f32 v17, v58, v59\n"
        "buffer_load_dwordx4 v[192:195], v0, s[40:43], s62 offen offset:1024 sc1\n"
        "v_cvt_pk_bf16_f32 v18, v60, v61\n"
        "v_cvt_pk_bf16_f32 v19, v62, v63\n"
        "v_cvt_pk_bf16_f32 v28, v64, v65\n"
        "v_cvt_pk_bf16_f32 v29, v66, v67\n"
        "v_cvt_pk_bf16_f32 v30, v68, v69\n"
        "v_cvt_pk_bf16_f32 v31, v70, v71\n"
        "v_permlane16_swap_b32 v16, v18\n"
        "v_permlane16_swap_b32 v17, v19\n"
        "v_permlane16_swap_b32 v28, v30\n"
        "v_permlane16_swap_b32 v29, v31\n"
        "ds_write_b32 v14, v164\n"
        "s_waitcnt vmcnt(5)\n"
        "v_mfma_f32_32x32x16_bf16 v[32:47], v[180:183], v[128:131], 0\n"
        "v_mfma_f32_32x32x16_bf16 v[48:63], v[180:183], v[132:135], 0\n"
        "v_mov_b32 v20, 0xdddd\n"
        "v_mov_b32 v11, 1\n"
        "v_xor_b32 v25, 256, v14\n"
        "v_cmp_gt_u32 vcc, 32, v1\n"
        "v_lshlrev_b32 v26, 16, v26\n"
        "s_nop 0\n"
        "v_cndmask_b32_e64 v21, 0, 1.0, vcc\n"
        "v_mov_b32 v252, v140\n"
        "v_mov_b32 v253, v140\n"
        "v_mov_b32 v254, v140\n"
        "v_mov_b32 v255, v140\n"
        "v_max3_f32 v64, v32, v33, v34\n"
        "v_max3_f32 v64, v64, v35, v36\n"
        "v_max3_f32 v64, v64, v37, v38\n"
        "v_max3_f32 v64, v64, v39, v40\n"
        "v_max3_f32 v64, v64, v41, v42\n"
        "v_max3_f32 v64, v64, v43, v44\n"
        "v_max3_f32 v64, v64, v45, v46\n"
        "v_max_f32 v64, v64, v47\n"
        "v_max3_f32 v80, v48, v49, v50\n"
        "v_max3_f32 v80, v80, v51, v52\n"
        "v_max3_f32 v80, v80, v53, v54\n"
        "v_max3_f32 v80, v80, v55, v56\n"
        "v_max3_f32 v80, v80, v57, v58\n"
        "v_max3_f32 v80, v80, v59, v60\n"
        "v_max3_f32 v80, v80, v61, v62\n"
        "v_max_f32 v80, v80, v63\n"
        "v_mov_b32 v65, v64\n"
        "v_mov_b32 v81, v80\n"
        "s_nop 0\n"
        "v_permlane32_swap_b32 v64, v65\n"
        "v_permlane32_swap_b32 v80, v81\n"
        "v_max_f32 v64, v64, v65\n"
        "v_max_f32 v80, v80, v81\n"
        "v_sub_f32 v22, 0xc2c80000, v64\n"
        "v_sub_f32 v23, 0xc2c80000, v80\n"
        "s_nop 0\n"
        "v_mfma_f32_32x32x2_f32 v[64:79], v21, v22, 0\n"
        "v_mfma_f32_32x32x2_f32 v[80:95], v21, v23, 0\n"
        "s_waitcnt vmcnt(4) lgkmcnt(0)\n"
        "s_barrier\n"
        "ds_read_b128 v[136:139], v8 offset:26624\n"
        "s_mov_b64 exec, s[60:61]\n"
        "ds_read_b128 v[140:143], v9 offset:32768\n"
        "s_mov_b64 exec, -1\n"
        "ds_read_b128 v[180:183], v4 offset:24576\n"
        "ds_read_b128 v[184:187], v4 offset:30720\n"
        "s_waitcnt lgkmcnt(3)\n"
        "s_nop 7\n"
        "v_mfma_f32_32x32x16_bf16 v[32:47], v[136:139], v[128:131], v[64:79]\n"
        "s_setprio 2\n"
        "s_nop 10\n"
        "v_exp_f32 v32, v32\n"
        "v_exp_f32 v33, v33\n"
        "s_mov_b64 exec, s[60:61]\n"
        "ds_read_b128 v[144:147], v9 offset:33280\n"
        "ds_read_b128 v[252:255], v9 offset:35840\n"
        "s_mov_b64 exec, -1\n"
        "v_mfma_f32_32x32x16_bf16 v[96:111], v[164:167], v[164:167], 0\n"
        "v_exp_f32 v34, v34\n"
        "v_exp_f32 v35, v35\n"
        "v_exp_f32 v36, v36\n"
        "v_exp_f32 v37, v37\n"
        "v_mfma_f32_32x32x16_bf16 v[48:63], v[136:139], v[132:135], v[80:95]\n"
        "ds_read_b128 v[136:139], v8 offset:29696\n"
        "buffer_load_dwordx4 v[196:199], v0, s[40:43], s62 offen offset:2048 sc1\n"
        "v_exp_f32 v38, v38\n"
        "v_exp_f32 v39, v39\n"
        "v_exp_f32 v40, v40\n"
        "v_exp_f32 v41, v41\n"
        "v_exp_f32 v42, v42\n"
        "v_exp_f32 v43, v43\n"
        "s_waitcnt vmcnt(3) lgkmcnt(1)\n"
        "v_smfmac_f32_32x32x32_bf16 v[96:111], v[140:143], v[32:39], v20\n"
        "ds_add_u32 v14, v11\n"
        "buffer_load_dwordx4 v[200:203], v0, s[40:43], s62 offen offset:3072 sc1\n"
        "v_mfma_f32_32x32x16_bf16 v[112:127], v[164:167], v[164:167], 0\n"
        "v_exp_f32 v44, v44\n"
        "v_exp_f32 v45, v45\n"
        "v_exp_f32 v46, v46\n"
        "v_exp_f32 v47, v47\n"
        "v_exp_f32 v48, v48\n"
        "v_exp_f32 v49, v49\n"
        "v_smfmac_f32_32x32x32_bf16 v[96:111], v[144:147], v[40:47], v20\n"
        "ds_read_b32 v10, v14\n"
        "ds_read_b32 v24, v25\n"
        "v_exp_f32 v50, v50\n"
        "v_exp_f32 v51, v51\n"
        "v_exp_f32 v52, v52\n"
        "v_exp_f32 v53, v53\n"
        "s_waitcnt lgkmcnt(2)\n"
        "v_mfma_f32_32x32x16_bf16 v[32:47], v[136:139], v[128:131], v[64:79]\n"
        "s_mov_b32 s62, 4096\n"
        "buffer_load_dwordx4 v[204:207], v0, s[40:43], s62 offen offset:0 sc1\n"
        "v_mfma_f32_32x32x2_f32 v[148:163], v21, v26, 0\n"
        "v_exp_f32 v54, v54\n"
        "v_exp_f32 v55, v55\n"
        "v_exp_f32 v56, v56\n"
        "v_exp_f32 v57, v57\n"
        "v_exp_f32 v58, v58\n"
        "v_exp_f32 v59, v59\n"
        "s_waitcnt lgkmcnt(0)\n"
        "v_cmp_gt_u32 vcc, 4, v10\n"
        "v_smfmac_f32_32x32x32_bf16 v[112:127], v[140:143], v[48:55], v20\n"
        "s_cbranch_vccnz 122f\n"
        "123:\n"
        "v_cmp_gt_u32 vcc, 4, v24\n"
        "s_setprio 3\n"
        "s_cbranch_vccz 124f\n"
        "s_setprio 2\n"
        "124:\n"
        "v_exp_f32 v60, v60\n"
        "v_exp_f32 v61, v61\n"
        "v_exp_f32 v62, v62\n"
        "v_exp_f32 v63, v63\n"
        "v_exp_f32 v32, v32\n"
        "v_exp_f32 v33, v33\n"
        "v_smfmac_f32_32x32x32_bf16 v[112:127], v[144:147], v[56:63], v20\n"
        "s_mov_b64 exec, s[60:61]\n"
        "ds_read_b128 v[144:147], v9 offset:36352\n"
        "ds_read_b128 v[140:143], v9 offset:45056\n"
        "s_mov_b64 exec, -1\n"
        "v_exp_f32 v34, v34\n"
        "v_exp_f32 v35, v35\n"
        "v_mfma_f32_32x32x16_bf16 v[48:63], v[136:139], v[132:135], v[80:95]\n"
        "ds_read_b128 v[136:139], v8 offset:38912\n"
        "buffer_load_dwordx4 v[208:211], v0, s[40:43], s62 offen offset:1024 sc1\n"
        "v_exp_f32 v36, v36\n"
        "v_exp_f32 v37, v37\n"
        "v_exp_f32 v38, v38\n"
        "s_waitcnt vmcnt(5) lgkmcnt(1)\n"
        "v_mfma_f32_32x32x16_bf16 v[148:163], v[188:191], v[180:183], v[148:163]\n"
        "ds_read_b128 v[180:183], v5 offset:24576\n"
        "buffer_load_dwordx4 v[212:215], v0, s[40:43], s62 offen offset:2048 sc1\n"
        "v_exp_f32 v39, v39\n"
        "v_exp_f32 v40, v40\n"
        "v_exp_f32 v41, v41\n"
        "v_smfmac_f32_32x32x32_bf16 v[96:111], v[252:255], v[32:39], v20\n"
        "buffer_load_dwordx4 v[216:219], v0, s[40:43], s62 offen offset:3072 sc1\n"
        "v_exp_f32 v42, v42\n"
        "v_exp_f32 v43, v43\n"
        "v_mfma_f32_32x32x16_bf16 v[164:179], v[188:191], v[184:187], 0\n"
        "ds_read_b128 v[184:187], v5 offset:30720\n"
        "v_exp_f32 v44, v44\n"
        "v_exp_f32 v45, v45\n"
        "v_exp_f32 v46, v46\n"
        "s_waitcnt vmcnt(6) lgkmcnt(1)\n"
        "v_mfma_f32_32x32x16_bf16 v[148:163], v[192:195], v[180:183], v[148:163]\n"
        "ds_read_b128 v[180:183], v6 offset:24576\n"
        "v_exp_f32 v47, v47\n"
        "v_exp_f32 v48, v48\n"
        "v_exp_f32 v49, v49\n"
        "v_smfmac_f32_32x32x32_bf16 v[96:111], v[144:147], v[40:47], v20\n"
        "s_add_u32 m0, s56, 0\n"
        "s_mov_b32 s63, 12288\n"
        "buffer_load_dwordx4 v15, s[20:23], s63 offen offset:2048 sc1 lds\n"
        "v_exp_f32 v50, v50\n"
        "v_exp_f32 v51, v51\n"
        "v_exp_f32 v52, v52\n"
        "v_mfma_f32_32x32x16_bf16 v[32:47], v[136:139], v[128:131], v[64:79]\n"
        "s_movk_i32 s7, 384\n"
        "buffer_load_dwordx4 v12, s[24:27], s7 offen offset:0 lds\n"
        "v_exp_f32 v53, v53\n"
        "v_exp_f32 v54, v54\n"
        "v_exp_f32 v55, v55\n"
        "s_waitcnt lgkmcnt(0)\n"
        "v_mfma_f32_32x32x16_bf16 v[164:179], v[192:195], v[184:187], v[164:179]\n"
        "ds_read_b128 v[184:187], v6 offset:30720\n"
        "buffer_load_dwordx4 v13, s[24:27], s7 offen offset:1024 lds\n"
        "v_exp_f32 v56, v56\n"
        "v_exp_f32 v57, v57\n"
        "v_exp_f32 v58, v58\n"
        "v_smfmac_f32_32x32x32_bf16 v[112:127], v[252:255], v[48:55], v20\n"
        "v_exp_f32 v59, v59\n"
        "v_exp_f32 v60, v60\n"
        "v_exp_f32 v61, v61\n"
        "s_waitcnt vmcnt(8)\n"
        "v_mfma_f32_32x32x16_bf16 v[148:163], v[196:199], v[180:183], v[148:163]\n"
        "ds_read_b128 v[180:183], v7 offset:24576\n"
        "v_exp_f32 v62, v62\n"
        "v_exp_f32 v63, v63\n"
        "v_exp_f32 v32, v32\n"
        "v_exp_f32 v33, v33\n"
        "v_smfmac_f32_32x32x32_bf16 v[112:127], v[144:147], v[56:63], v20\n"
        "s_mov_b64 exec, s[60:61]\n"
        "ds_read_b128 v[144:147], v9 offset:45568\n"
        "ds_read_b128 v[252:255], v9 offset:48128\n"
        "s_mov_b64 exec, -1\n"
        "v_exp_f32 v34, v34\n"
        "v_exp_f32 v35, v35\n"
        "v_mfma_f32_32x32x16_bf16 v[48:63], v[136:139], v[132:135], v[80:95]\n"
        "ds_read_b128 v[136:139], v8 offset:41984\n"
        "s_mov_b32 s62, 8192\n"
        "buffer_load_dwordx4 v[220:223], v0, s[40:43], s62 offen offset:0 sc1\n"
        "v_exp_f32 v36, v36\n"
        "v_exp_f32 v37, v37\n"
        "v_exp_f32 v38, v38\n"
        "s_waitcnt lgkmcnt(1)\n"
        "v_mfma_f32_32x32x16_bf16 v[164:179], v[196:199], v[184:187], v[164:179]\n"
        "ds_read_b128 v[184:187], v7 offset:30720\n"
        "buffer_load_dwordx4 v[224:227], v0, s[40:43], s62 offen offset:1024 sc1\n"
        "v_exp_f32 v39, v39\n"
        "v_exp_f32 v40, v40\n"
        "v_exp_f32 v41, v41\n"
        "v_smfmac_f32_32x32x32_bf16 v[96:111], v[140:143], v[32:39], v20\n"
        "v_exp_f32 v42, v42\n"
        "v_exp_f32 v43, v43\n"
        "s_waitcnt vmcnt(9) lgkmcnt(1)\n"
        "v_mfma_f32_32x32x16_bf16 v[148:163], v[200:203], v[180:183], v[148:163]\n"
        "ds_read_b128 v[180:183], v4 offset:36864\n"
        "v_exp_f32 v44, v44\n"
        "v_exp_f32 v45, v45\n"
        "v_exp_f32 v46, v46\n"
        "s_waitcnt lgkmcnt(1)\n"
        "v_mfma_f32_32x32x16_bf16 v[164:179], v[200:203], v[184:187], v[164:179]\n"
        "ds_read_b128 v[184:187], v4 offset:43008\n"
        "v_exp_f32 v47, v47\n"
        "v_exp_f32 v48, v48\n"
        "v_exp_f32 v49, v49\n"
        "v_smfmac_f32_32x32x32_bf16 v[96:111], v[144:147], v[40:47], v20\n"
        "buffer_load_dwordx4 v[228:231], v0, s[40:43], s62 offen offset:2048 sc1\n"
        "v_exp_f32 v50, v50\n"
        "v_exp_f32 v51, v51\n"
        "v_exp_f32 v52, v52\n"
        "v_mfma_f32_32x32x16_bf16 v[32:47], v[136:139], v[128:131], v[64:79]\n"
        "buffer_load_dwordx4 v[232:235], v0, s[40:43], s62 offen offset:3072 sc1\n"
        "v_exp_f32 v53, v53\n"
        "v_exp_f32 v54, v54\n"
        "v_exp_f32 v55, v55\n"
        "s_waitcnt vmcnt(10) lgkmcnt(0)\n"
        "v_mfma_f32_32x32x16_bf16 v[148:163], v[204:207], v[180:183], v[148:163]\n"
        "ds_read_b128 v[180:183], v5 offset:36864\n"
        "s_add_u32 m0, s56, 12288\n"
        "s_mov_b32 s63, 16384\n"
        "buffer_load_dwordx4 v15, s[20:23], s63 offen offset:2048 sc1 lds\n"
        "v_exp_f32 v56, v56\n"
        "v_exp_f32 v57, v57\n"
        "v_exp_f32 v58, v58\n"
        "v_smfmac_f32_32x32x32_bf16 v[112:127], v[140:143], v[48:55], v20\n"
        "ds_add_u32 v14, v11\n"
        "v_exp_f32 v59, v59\n"
        "v_exp_f32 v60, v60\n"
        "v_exp_f32 v61, v61\n"
        "v_mfma_f32_32x32x16_bf16 v[164:179], v[204:207], v[184:187], v[164:179]\n"
        "ds_read_b128 v[184:187], v5 offset:43008\n"
        "v_exp_f32 v62, v62\n"
        "v_exp_f32 v63, v63\n"
        "v_exp_f32 v32, v32\n"
        "v_exp_f32 v33, v33\n"
        "v_smfmac_f32_32x32x32_bf16 v[112:127], v[144:147], v[56:63], v20\n"
        "s_mov_b64 exec, s[60:61]\n"
        "ds_read_b128 v[144:147], v9 offset:48640\n"
        "ds_read_b128 v[140:143], v9 offset:57344\n"
        "s_mov_b64 exec, -1\n"
        "ds_read_b32 v10, v14\n"
        "ds_read_b32 v24, v25\n"
        "v_exp_f32 v34, v34\n"
        "v_exp_f32 v35, v35\n"
        "v_mfma_f32_32x32x16_bf16 v[48:63], v[136:139], v[132:135], v[80:95]\n"
        "ds_read_b128 v[136:139], v8 offset:51200\n"
        "s_movk_i32 s7, 512\n"
        "buffer_load_dwordx4 v12, s[24:27], s7 offen offset:0 lds\n"
        "v_exp_f32 v36, v36\n"
        "v_exp_f32 v37, v37\n"
        "v_exp_f32 v38, v38\n"
        "s_waitcnt vmcnt(11) lgkmcnt(1)\n"
        "v_mfma_f32_32x32x16_bf16 v[148:163], v[208:211], v[180:183], v[148:163]\n"
        "ds_read_b128 v[180:183], v6 offset:36864\n"
        "buffer_load_dwordx4 v13, s[24:27], s7 offen offset:1024 lds\n"
        "v_exp_f32 v39, v39\n"
        "v_exp_f32 v40, v40\n"
        "v_exp_f32 v41, v41\n"
        "v_cmp_gt_u32 vcc, 8, v10\n"
        "v_smfmac_f32_32x32x32_bf16 v[96:111], v[252:255], v[32:39], v20\n"
        "s_cbranch_vccnz 125f\n"
        "126:\n"
        "v_cmp_gt_u32 vcc, 8, v24\n"
        "v_exp_f32 v42, v42\n"
        "v_exp_f32 v43, v43\n"
        "v_mfma_f32_32x32x16_bf16 v[164:179], v[208:211], v[184:187], v[164:179]\n"
        "ds_read_b128 v[184:187], v6 offset:43008\n"
        "s_setprio 3\n"
        "s_cbranch_vccz 127f\n"
        "s_setprio 2\n"
        "127:\n"
        "v_exp_f32 v44, v44\n"
        "v_exp_f32 v45, v45\n"
        "v_exp_f32 v46, v46\n"
        "s_waitcnt vmcnt(11) lgkmcnt(1)\n"
        "v_mfma_f32_32x32x16_bf16 v[148:163], v[212:215], v[180:183], v[148:163]\n"
        "ds_read_b128 v[180:183], v7 offset:36864\n"
        "v_exp_f32 v47, v47\n"
        "v_exp_f32 v48, v48\n"
        "v_exp_f32 v49, v49\n"
        "v_smfmac_f32_32x32x32_bf16 v[96:111], v[144:147], v[40:47], v20\n"
        "s_mov_b32 s62, 12288\n"
        "buffer_load_dwordx4 v[236:239], v0, s[40:43], s62 offen offset:0 sc1\n"
        "v_exp_f32 v50, v50\n"
        "v_exp_f32 v51, v51\n"
        "v_exp_f32 v52, v52\n"
        "v_mfma_f32_32x32x16_bf16 v[32:47], v[136:139], v[128:131], v[64:79]\n"
        "buffer_load_dwordx4 v[240:243], v0, s[40:43], s62 offen offset:1024 sc1\n"
        "v_exp_f32 v53, v53\n"
        "v_exp_f32 v54, v54\n"
        "v_exp_f32 v55, v55\n"
        "s_waitcnt lgkmcnt(0)\n"
        "v_mfma_f32_32x32x16_bf16 v[164:179], v[212:215], v[184:187], v[164:179]\n"
        "ds_read_b128 v[184:187], v7 offset:43008\n"
        "v_exp_f32 v56, v56\n"
        "v_exp_f32 v57, v57\n"
        "v_exp_f32 v58, v58\n"
        "v_smfmac_f32_32x32x32_bf16 v[112:127], v[252:255], v[48:55], v20\n"
        "v_exp_f32 v59, v59\n"
        "v_exp_f32 v60, v60\n"
        "v_exp_f32 v61, v61\n"
        "s_waitcnt vmcnt(12)\n"
        "v_mfma_f32_32x32x16_bf16 v[148:163], v[216:219], v[180:183], v[148:163]\n"
        "ds_read_b128 v[180:183], v4 offset:49152\n"
        "v_exp_f32 v62, v62\n"
        "v_exp_f32 v63, v63\n"
        "v_exp_f32 v32, v32\n"
        "v_exp_f32 v33, v33\n"
        "v_smfmac_f32_32x32x32_bf16 v[112:127], v[144:147], v[56:63], v20\n"
        "s_mov_b64 exec, s[60:61]\n"
        "ds_read_b128 v[144:147], v9 offset:57856\n"
        "ds_read_b128 v[252:255], v9 offset:60416\n"
        "s_mov_b64 exec, -1\n"
        "v_exp_f32 v34, v34\n"
        "v_exp_f32 v35, v35\n"
        "v_mfma_f32_32x32x16_bf16 v[48:63], v[136:139], v[132:135], v[80:95]\n"
        "ds_read_b128 v[136:139], v8 offset:54272\n"
        "buffer_store_dwordx4 v[16:19], v2, s[40:43], s70 offen offset:0 sc1\n"
        "v_exp_f32 v36, v36\n"
        "v_exp_f32 v37, v37\n"
        "v_exp_f32 v38, v38\n"
        "s_waitcnt lgkmcnt(1)\n"
        "v_mfma_f32_32x32x16_bf16 v[164:179], v[216:219], v[184:187], v[164:179]\n"
        "ds_read_b128 v[184:187], v4 offset:55296\n"
        "buffer_load_dwordx4 v[244:247], v0, s[40:43], s62 offen offset:2048 sc1\n"
        "v_exp_f32 v39, v39\n"
        "v_exp_f32 v40, v40\n"
        "v_exp_f32 v41, v41\n"
        "s_waitcnt vmcnt(11) lgkmcnt(1)\n"
        "v_smfmac_f32_32x32x32_bf16 v[96:111], v[140:143], v[32:39], v20\n"
        "ds_add_u32 v14, v11\n"
        "s_add_u32 m0, s56, 24576\n"
        "s_mov_b32 s63, 20480\n"
        "buffer_load_dwordx4 v15, s[20:23], s63 offen offset:2048 sc1 lds\n"
        "v_exp_f32 v42, v42\n"
        "v_exp_f32 v43, v43\n"
        "s_waitcnt vmcnt(11) lgkmcnt(1)\n"
        "v_mfma_f32_32x32x16_bf16 v[148:163], v[220:223], v[180:183], v[148:163]\n"
        "ds_read_b128 v[180:183], v5 offset:49152\n"
        "v_exp_f32 v44, v44\n"
        "v_exp_f32 v45, v45\n"
        "v_exp_f32 v46, v46\n"
        "v_mfma_f32_32x32x16_bf16 v[164:179], v[220:223], v[184:187], v[164:179]\n"
        "ds_read_b128 v[184:187], v5 offset:55296\n"
        "v_exp_f32 v47, v47\n"
        "v_exp_f32 v48, v48\n"
        "v_exp_f32 v49, v49\n"
        "v_smfmac_f32_32x32x32_bf16 v[96:111], v[144:147], v[40:47], v20\n"
        "ds_read_b32 v10, v14\n"
        "ds_read_b32 v24, v25\n"
        "v_exp_f32 v50, v50\n"
        "v_exp_f32 v51, v51\n"
        "v_exp_f32 v52, v52\n"
        "v_mfma_f32_32x32x16_bf16 v[32:47], v[136:139], v[128:131], v[64:79]\n"
        "buffer_store_dwordx4 v[28:31], v2, s[40:43], s70 offen offset:1024 sc1\n"
        "v_exp_f32 v53, v53\n"
        "v_exp_f32 v54, v54\n"
        "v_exp_f32 v55, v55\n"
        "s_waitcnt vmcnt(11) lgkmcnt(2)\n"
        "v_mfma_f32_32x32x16_bf16 v[148:163], v[224:227], v[180:183], v[148:163]\n"
        "ds_read_b128 v[180:183], v6 offset:49152\n"
        "s_movk_i32 s7, 640\n"
        "buffer_load_dwordx4 v12, s[24:27], s7 offen offset:0 lds\n"
        "v_exp_f32 v56, v56\n"
        "v_exp_f32 v57, v57\n"
        "v_exp_f32 v58, v58\n"
        "s_waitcnt lgkmcnt(1)\n"
        "v_cmp_gt_u32 vcc, 12, v10\n"
        "v_smfmac_f32_32x32x32_bf16 v[112:127], v[140:143], v[48:55], v20\n"
        "s_cbranch_vccnz 128f\n"
        "129:\n"
        "v_cmp_gt_u32 vcc, 12, v24\n"
        "v_exp_f32 v59, v59\n"
        "v_exp_f32 v60, v60\n"
        "v_exp_f32 v61, v61\n"
        "v_mfma_f32_32x32x16_bf16 v[164:179], v[224:227], v[184:187], v[164:179]\n"
        "ds_read_b128 v[184:187], v6 offset:55296\n"
        "s_setprio 3\n"
        "s_cbranch_vccz 130f\n"
        "s_setprio 2\n"
        "130:\n"
        "v_exp_f32 v62, v62\n"
        "v_exp_f32 v63, v63\n"
        "v_exp_f32 v32, v32\n"
        "v_exp_f32 v33, v33\n"
        "v_smfmac_f32_32x32x32_bf16 v[112:127], v[144:147], v[56:63], v20\n"
        "s_mov_b64 exec, s[60:61]\n"
        "ds_read_b128 v[144:147], v9 offset:60928\n"
        "ds_read_b128 v[140:143], v9 offset:8192\n"
        "s_mov_b64 exec, -1\n"
        "v_exp_f32 v34, v34\n"
        "v_exp_f32 v35, v35\n"
        "v_mfma_f32_32x32x16_bf16 v[48:63], v[136:139], v[132:135], v[80:95]\n"
        "ds_read_b128 v[136:139], v8 offset:2048\n"
        "buffer_load_dwordx4 v13, s[24:27], s7 offen offset:1024 lds\n"
        "v_exp_f32 v36, v36\n"
        "v_exp_f32 v37, v37\n"
        "v_exp_f32 v38, v38\n"
        "s_waitcnt vmcnt(12) lgkmcnt(1)\n"
        "v_mfma_f32_32x32x16_bf16 v[148:163], v[228:231], v[180:183], v[148:163]\n"
        "ds_read_b128 v[180:183], v7 offset:49152\n"
        "buffer_load_dwordx4 v[248:251], v0, s[40:43], s62 offen offset:3072 sc1\n"
        "v_exp_f32 v39, v39\n"
        "v_exp_f32 v40, v40\n"
        "v_exp_f32 v41, v41\n"
        "v_smfmac_f32_32x32x32_bf16 v[96:111], v[252:255], v[32:39], v20\n"
        "v_exp_f32 v42, v42\n"
        "v_exp_f32 v43, v43\n"
        "v_mfma_f32_32x32x16_bf16 v[164:179], v[228:231], v[184:187], v[164:179]\n"
        "ds_read_b128 v[184:187], v7 offset:55296\n"
        "v_exp_f32 v44, v44\n"
        "v_exp_f32 v45, v45\n"
        "v_exp_f32 v46, v46\n"
        "s_waitcnt vmcnt(12) lgkmcnt(1)\n"
        "v_mfma_f32_32x32x16_bf16 v[148:163], v[232:235], v[180:183], v[148:163]\n"
        "ds_read_b128 v[180:183], v4 offset:0\n"
        "v_exp_f32 v47, v47\n"
        "v_exp_f32 v48, v48\n"
        "v_exp_f32 v49, v49\n"
        "v_smfmac_f32_32x32x32_bf16 v[96:111], v[144:147], v[40:47], v20\n"
        "s_mov_b32 s62, 16384\n"
        "buffer_load_dwordx4 v[188:191], v0, s[40:43], s62 offen offset:0 sc1\n"
        "s_sub_u32 s64, s56, 61440\n"
        "v_exp_f32 v50, v50\n"
        "v_exp_f32 v51, v51\n"
        "v_exp_f32 v52, v52\n"
        "v_mfma_f32_32x32x16_bf16 v[32:47], v[136:139], v[128:131], v[64:79]\n"
        "buffer_load_dwordx4 v[192:195], v0, s[40:43], s62 offen offset:1024 sc1\n"
        "s_add_u32 s65, s56, 61440\n"
        "v_exp_f32 v53, v53\n"
        "v_exp_f32 v54, v54\n"
        "v_exp_f32 v55, v55\n"
        "s_waitcnt lgkmcnt(0)\n"
        "v_mfma_f32_32x32x16_bf16 v[164:179], v[232:235], v[184:187], v[164:179]\n"
        "ds_read_b128 v[184:187], v4 offset:6144\n"
        "buffer_load_dwordx4 v[196:199], v0, s[40:43], s62 offen offset:2048 sc1\n"
        "v_exp_f32 v56, v56\n"
        "v_exp_f32 v57, v57\n"
        "v_exp_f32 v58, v58\n"
        "s_waitcnt vmcnt(12)\n"
        "v_smfmac_f32_32x32x32_bf16 v[112:127], v[252:255], v[48:55], v20\n"
        "ds_add_u32 v14, v11\n"
        "v_exp_f32 v59, v59\n"
        "v_exp_f32 v60, v60\n"
        "v_exp_f32 v61, v61\n"
        "s_waitcnt vmcnt(11) lgkmcnt(1)\n"
        "v_mfma_f32_32x32x16_bf16 v[148:163], v[236:239], v[180:183], v[148:163]\n"
        "ds_read_b128 v[180:183], v5 offset:0\n"
        "v_exp_f32 v62, v62\n"
        "v_exp_f32 v63, v63\n"
        "v_exp_f32 v32, v32\n"
        "v_exp_f32 v33, v33\n"
        "v_smfmac_f32_32x32x32_bf16 v[112:127], v[144:147], v[56:63], v20\n"
        "s_mov_b64 exec, s[60:61]\n"
        "ds_read_b128 v[144:147], v9 offset:8704\n"
        "ds_read_b128 v[252:255], v9 offset:11264\n"
        "s_mov_b64 exec, -1\n"
        "s_cmp_eq_u32 s52, 0\n"
        "s_cselect_b32 s64, s65, s64\n"
        "v_exp_f32 v34, v34\n"
        "v_exp_f32 v35, v35\n"
        "v_mfma_f32_32x32x16_bf16 v[48:63], v[136:139], v[132:135], v[80:95]\n"
        "ds_read_b128 v[136:139], v8 offset:5120\n"
        "buffer_load_dwordx4 v[200:203], v0, s[40:43], s62 offen offset:3072 sc1\n"
        "v_add_u32 v28, s56, v2\n"
        "v_exp_f32 v36, v36\n"
        "v_exp_f32 v37, v37\n"
        "v_exp_f32 v38, v38\n"
        "v_mfma_f32_32x32x16_bf16 v[164:179], v[236:239], v[184:187], v[164:179]\n"
        "ds_read_b128 v[184:187], v5 offset:6144\n"
        "s_add_u32 m0, s56, 36864\n"
        "s_mov_b32 s63, 24576\n"
        "buffer_load_dwordx4 v15, s[20:23], s63 offen offset:2048 sc1 lds\n"
        "v_exp_f32 v39, v39\n"
        "v_exp_f32 v40, v40\n"
        "v_exp_f32 v41, v41\n"
        "v_smfmac_f32_32x32x32_bf16 v[96:111], v[140:143], v[32:39], v20\n"
        "v_exp_f32 v42, v42\n"
        "v_exp_f32 v43, v43\n"
        "s_waitcnt vmcnt(12) lgkmcnt(0)\n"
        "v_mfma_f32_32x32x16_bf16 v[148:163], v[240:243], v[180:183], v[148:163]\n"
        "ds_read_b128 v[180:183], v6 offset:0\n"
        "v_exp_f32 v44, v44\n"
        "v_exp_f32 v45, v45\n"
        "v_exp_f32 v46, v46\n"
        "v_mfma_f32_32x32x16_bf16 v[164:179], v[240:243], v[184:187], v[164:179]\n"
        "ds_read_b128 v[184:187], v6 offset:6144\n"
        "v_exp_f32 v47, v47\n"
        "v_exp_f32 v48, v48\n"
        "v_exp_f32 v49, v49\n"
        "v_smfmac_f32_32x32x32_bf16 v[96:111], v[144:147], v[40:47], v20\n"
        "ds_read_b32 v10, v14\n"
        "ds_read_b32 v24, v25\n"
        "v_add_u32 v29, s64, v2\n"
        "v_exp_f32 v50, v50\n"
        "v_exp_f32 v51, v51\n"
        "v_exp_f32 v52, v52\n"
        "v_mfma_f32_32x32x16_bf16 v[32:47], v[136:139], v[128:131], v[64:79]\n"
        "s_movk_i32 s7, 768\n"
        "buffer_load_dwordx4 v12, s[24:27], s7 offen offset:0 lds\n"
        "v_mov_b32 v22, s45\n"
        "v_exp_f32 v53, v53\n"
        "v_exp_f32 v54, v54\n"
        "v_exp_f32 v55, v55\n"
        "s_waitcnt vmcnt(11) lgkmcnt(0)\n"
        "v_mfma_f32_32x32x16_bf16 v[148:163], v[244:247], v[180:183], v[148:163]\n"
        "ds_read_b128 v[180:183], v7 offset:0\n"
        "buffer_load_dwordx4 v13, s[24:27], s7 offen offset:1024 lds\n"
        "v_exp_f32 v56, v56\n"
        "v_exp_f32 v57, v57\n"
        "v_exp_f32 v58, v58\n"
        "v_cmp_gt_u32 vcc, 16, v10\n"
        "v_smfmac_f32_32x32x32_bf16 v[112:127], v[140:143], v[48:55], v20\n"
        "s_cbranch_vccnz 131f\n"
        "132:\n"
        "v_cmp_gt_u32 vcc, 16, v24\n"
        "v_exp_f32 v59, v59\n"
        "v_exp_f32 v60, v60\n"
        "v_exp_f32 v61, v61\n"
        "v_mfma_f32_32x32x16_bf16 v[164:179], v[244:247], v[184:187], v[164:179]\n"
        "ds_read_b128 v[184:187], v7 offset:6144\n"
        "s_setprio 3\n"
        "s_cbranch_vccz 133f\n"
        "s_setprio 2\n"
        "133:\n"
        "v_exp_f32 v62, v62\n"
        "v_exp_f32 v63, v63\n"
        "v_exp_f32 v32, v32\n"
        "v_exp_f32 v33, v33\n"
        "v_smfmac_f32_32x32x32_bf16 v[112:127], v[144:147], v[56:63], v20\n"
        "s_mov_b64 exec, s[60:61]\n"
        "ds_read_b128 v[144:147], v9 offset:11776\n"
        "ds_read_b128 v[140:143], v9 offset:20480\n"
        "s_mov_b64 exec, -1\n"
        "v_writelane_b32 v22, s89, 0\n"
        "v_exp_f32 v34, v34\n"
        "v_exp_f32 v35, v35\n"
        "v_exp_f32 v36, v36\n"
        "v_mfma_f32_32x32x16_bf16 v[48:63], v[136:139], v[132:135], v[80:95]\n"
        "ds_read_b128 v[136:139], v8 offset:14336\n"
        "s_mov_b32 s62, 20480\n"
        "buffer_load_dwordx4 v[204:207], v0, s[40:43], s62 offen offset:0 sc1\n"
        "v_exp_f32 v37, v37\n"
        "v_exp_f32 v38, v38\n"
        "v_exp_f32 v39, v39\n"
        "s_waitcnt vmcnt(8) lgkmcnt(1)\n"
        "v_mfma_f32_32x32x16_bf16 v[148:163], v[248:251], v[180:183], v[148:163]\n"
        "ds_read_b128 v[180:183], v4 offset:12288\n"
        "buffer_load_dwordx4 v[208:211], v0, s[40:43], s62 offen offset:1024 sc1\n"
        "v_exp_f32 v40, v40\n"
        "v_exp_f32 v41, v41\n"
        "v_exp_f32 v42, v42\n"
        "v_smfmac_f32_32x32x32_bf16 v[96:111], v[252:255], v[32:39], v20\n"
        "ds_add_u32 v14, v11\n"
        "buffer_load_dwordx4 v[212:215], v0, s[40:43], s62 offen offset:2048 sc1\n"
        "v_exp_f32 v43, v43\n"
        "v_exp_f32 v44, v44\n"
        "v_exp_f32 v45, v45\n"
        "v_mfma_f32_32x32x16_bf16 v[164:179], v[248:251], v[184:187], v[164:179]\n"
        "ds_read_b128 v[184:187], v4 offset:18432\n"
        "v_exp_f32 v46, v46\n"
        "v_exp_f32 v47, v47\n"
        "v_exp_f32 v48, v48\n"
        "v_exp_f32 v49, v49\n"
        "v_smfmac_f32_32x32x32_bf16 v[96:111], v[144:147], v[40:47], v20\n"
        "buffer_load_dwordx4 v[216:219], v0, s[40:43], s62 offen offset:3072 sc1\n"
        "v_exp_f32 v50, v50\n"
        "v_exp_f32 v51, v51\n"
        "s_waitcnt lgkmcnt(1)\n"
        "v_mfma_f32_32x32x16_bf16 v[32:47], v[136:139], v[128:131], v[64:79]\n"
        "s_add_u32 m0, s56, 49152\n"
        "s_mov_b32 s63, 28672\n"
        "buffer_load_dwordx4 v15, s[20:23], s63 offen offset:2048 sc1 lds\n"
        "v_exp_f32 v52, v52\n"
        "v_exp_f32 v53, v53\n"
        "v_exp_f32 v54, v54\n"
        "s_waitcnt vmcnt(11) lgkmcnt(0)\n"
        "v_mfma_f32_32x32x16_bf16 v[148:163], v[188:191], v[180:183], v[148:163]\n"
        "ds_read_b128 v[180:183], v5 offset:12288\n"
        "v_exp_f32 v55, v55\n"
        "v_exp_f32 v56, v56\n"
        "v_exp_f32 v57, v57\n"
        "v_smfmac_f32_32x32x32_bf16 v[112:127], v[252:255], v[48:55], v20\n"
        "v_exp_f32 v58, v58\n"
        "v_exp_f32 v59, v59\n"
        "v_mfma_f32_32x32x16_bf16 v[164:179], v[188:191], v[184:187], v[164:179]\n"
        "ds_read_b128 v[184:187], v5 offset:18432\n"
        "v_exp_f32 v60, v60\n"
        "v_exp_f32 v61, v61\n"
        "v_exp_f32 v62, v62\n"
        "s_waitcnt vmcnt(10) lgkmcnt(1)\n"
        "v_mfma_f32_32x32x16_bf16 v[148:163], v[192:195], v[180:183], v[148:163]\n"
        "ds_read_b128 v[180:183], v6 offset:12288\n"
        "v_exp_f32 v63, v63\n"
        "v_exp_f32 v32, v32\n"
        "v_exp_f32 v33, v33\n"
        "v_smfmac_f32_32x32x32_bf16 v[112:127], v[144:147], v[56:63], v20\n"
        "s_mov_b64 exec, s[60:61]\n"
        "ds_read_b128 v[144:147], v9 offset:20992\n"
        "ds_read_b128 v[252:255], v9 offset:23552\n"
        "s_mov_b64 exec, -1\n"
        "ds_read_b32 v10, v14\n"
        "ds_read_b32 v24, v25\n"
        "v_exp_f32 v34, v34\n"
        "v_exp_f32 v35, v35\n"
        "v_exp_f32 v36, v36\n"
        "v_mfma_f32_32x32x16_bf16 v[48:63], v[136:139], v[132:135], v[80:95]\n"
        "ds_read_b128 v[136:139], v8 offset:17408\n"
        "s_movk_i32 s7, 896\n"
        "buffer_load_dwordx4 v12, s[24:27], s7 offen offset:0 lds\n"
        "v_exp_f32 v37, v37\n"
        "v_exp_f32 v38, v38\n"
        "v_exp_f32 v39, v39\n"
        "s_waitcnt lgkmcnt(1)\n"
        "v_mfma_f32_32x32x16_bf16 v[164:179], v[192:195], v[184:187], v[164:179]\n"
        "ds_read_b128 v[184:187], v6 offset:18432\n"
        "buffer_load_dwordx4 v13, s[24:27], s7 offen offset:1024 lds\n"
        "v_exp_f32 v40, v40\n"
        "v_exp_f32 v41, v41\n"
        "v_exp_f32 v42, v42\n"
        "v_cmp_gt_u32 vcc, 20, v10\n"
        "v_smfmac_f32_32x32x32_bf16 v[96:111], v[140:143], v[32:39], v20\n"
        "s_cbranch_vccnz 134f\n"
        "135:\n"
        "v_cmp_gt_u32 vcc, 20, v24\n"
        "v_exp_f32 v43, v43\n"
        "v_exp_f32 v44, v44\n"
        "v_exp_f32 v45, v45\n"
        "s_waitcnt vmcnt(11) lgkmcnt(0)\n"
        "v_mfma_f32_32x32x16_bf16 v[148:163], v[196:199], v[180:183], v[148:163]\n"
        "ds_read_b128 v[180:183], v7 offset:12288\n"
        "s_setprio 3\n"
        "s_cbranch_vccz 136f\n"
        "s_setprio 2\n"
        "136:\n"
        "v_exp_f32 v46, v46\n"
        "v_exp_f32 v47, v47\n"
        "v_exp_f32 v48, v48\n"
        "v_exp_f32 v49, v49\n"
        "v_smfmac_f32_32x32x32_bf16 v[96:111], v[144:147], v[40:47], v20\n"
        "s_mov_b32 s62, 24576\n"
        "buffer_load_dwordx4 v[220:223], v0, s[40:43], s62 offen offset:0 sc1\n"
        "v_exp_f32 v50, v50\n"
        "v_exp_f32 v51, v51\n"
        "v_mfma_f32_32x32x16_bf16 v[32:47], v[136:139], v[128:131], v[64:79]\n"
        "buffer_load_dwordx4 v[224:227], v0, s[40:43], s62 offen offset:1024 sc1\n"
        "v_exp_f32 v52, v52\n"
        "v_exp_f32 v53, v53\n"
        "v_exp_f32 v54, v54\n"
        "v_mfma_f32_32x32x16_bf16 v[164:179], v[196:199], v[184:187], v[164:179]\n"
        "ds_read_b128 v[184:187], v7 offset:18432\n"
        "buffer_load_dwordx4 v[228:231], v0, s[40:43], s62 offen offset:2048 sc1\n"
        "v_exp_f32 v55, v55\n"
        "v_exp_f32 v56, v56\n"
        "v_exp_f32 v57, v57\n"
        "v_smfmac_f32_32x32x32_bf16 v[112:127], v[140:143], v[48:55], v20\n"
        "v_exp_f32 v58, v58\n"
        "v_exp_f32 v59, v59\n"
        "s_waitcnt vmcnt(13) lgkmcnt(1)\n"
        "v_mfma_f32_32x32x16_bf16 v[148:163], v[200:203], v[180:183], v[148:163]\n"
        "ds_read_b128 v[180:183], v4 offset:24576\n"
        "v_exp_f32 v60, v60\n"
        "v_exp_f32 v61, v61\n"
        "v_exp_f32 v62, v62\n"
        "s_waitcnt lgkmcnt(1)\n"
        "v_mfma_f32_32x32x16_bf16 v[164:179], v[200:203], v[184:187], v[164:179]\n"
        "ds_read_b128 v[184:187], v4 offset:30720\n"
        "v_exp_f32 v63, v63\n"
        "v_exp_f32 v32, v32\n"
        "v_exp_f32 v33, v33\n"
        "v_smfmac_f32_32x32x32_bf16 v[112:127], v[144:147], v[56:63], v20\n"
        "s_mov_b64 exec, s[60:61]\n"
        "ds_read_b128 v[144:147], v9 offset:24064\n"
        "ds_read_b128 v[140:143], v9 offset:32768\n"
        "s_mov_b64 exec, -1\n"
        "v_exp_f32 v34, v34\n"
        "v_exp_f32 v35, v35\n"
        "v_exp_f32 v36, v36\n"
        "v_mfma_f32_32x32x16_bf16 v[48:63], v[136:139], v[132:135], v[80:95]\n"
        "ds_read_b128 v[136:139], v8 offset:26624\n"
        "buffer_load_dwordx4 v[232:235], v0, s[40:43], s62 offen offset:3072 sc1\n"
        "v_exp_f32 v37, v37\n"
        "v_exp_f32 v38, v38\n"
        "v_exp_f32 v39, v39\n"
        "s_waitcnt vmcnt(10) lgkmcnt(1)\n"
        "v_mfma_f32_32x32x16_bf16 v[148:163], v[204:207], v[180:183], v[148:163]\n"
        "ds_read_b128 v[180:183], v5 offset:24576\n"
        "s_add_u32 m0, s56, 0\n"
        "s_movk_i32 s7, 1024\n"
        "buffer_load_dwordx4 v12, s[24:27], s7 offen offset:0 lds\n"
        "v_exp_f32 v40, v40\n"
        "v_exp_f32 v41, v41\n"
        "v_exp_f32 v42, v42\n"
        "v_smfmac_f32_32x32x32_bf16 v[96:111], v[252:255], v[32:39], v20\n"
        "ds_add_u32 v14, v11\n"
        "v_exp_f32 v43, v43\n"
        "v_exp_f32 v44, v44\n"
        "v_exp_f32 v45, v45\n"
        "v_mfma_f32_32x32x16_bf16 v[164:179], v[204:207], v[184:187], v[164:179]\n"
        "ds_read_b128 v[184:187], v5 offset:30720\n"
        "v_exp_f32 v46, v46\n"
        "v_exp_f32 v47, v47\n"
        "v_exp_f32 v48, v48\n"
        "v_exp_f32 v49, v49\n"
        "v_smfmac_f32_32x32x32_bf16 v[96:111], v[144:147], v[40:47], v20\n"
        "ds_read_b32 v10, v14\n"
        "ds_read_b32 v24, v25\n"
        "v_exp_f32 v50, v50\n"
        "v_exp_f32 v51, v51\n"
        "s_waitcnt lgkmcnt(2)\n"
        "v_mfma_f32_32x32x16_bf16 v[32:47], v[136:139], v[128:131], v[64:79]\n"
        "buffer_load_dwordx4 v13, s[24:27], s7 offen offset:1024 lds\n"
        "v_exp_f32 v52, v52\n"
        "v_exp_f32 v53, v53\n"
        "v_exp_f32 v54, v54\n"
        "s_waitcnt vmcnt(11)\n"
        "v_mfma_f32_32x32x16_bf16 v[148:163], v[208:211], v[180:183], v[148:163]\n"
        "ds_read_b128 v[180:183], v6 offset:24576\n"
        "s_mov_b32 s62, 28672\n"
        "buffer_load_dwordx4 v[236:239], v0, s[40:43], s62 offen offset:0 sc1\n"
        "v_exp_f32 v55, v55\n"
        "v_exp_f32 v56, v56\n"
        "v_exp_f32 v57, v57\n"
        "s_waitcnt lgkmcnt(1)\n"
        "v_cmp_gt_u32 vcc, 24, v10\n"
        "v_smfmac_f32_32x32x32_bf16 v[112:127], v[252:255], v[48:55], v20\n"
        "s_cbranch_vccnz 137f\n"
        "138:\n"
        "v_cmp_gt_u32 vcc, 24, v24\n"
        "v_exp_f32 v58, v58\n"
        "v_exp_f32 v59, v59\n"
        "v_mfma_f32_32x32x16_bf16 v[164:179], v[208:211], v[184:187], v[164:179]\n"
        "ds_read_b128 v[184:187], v6 offset:30720\n"
        "s_setprio 3\n"
        "s_cbranch_vccz 139f\n"
        "s_setprio 2\n"
        "139:\n"
        "v_exp_f32 v60, v60\n"
        "v_exp_f32 v61, v61\n"
        "v_exp_f32 v62, v62\n"
        "s_waitcnt vmcnt(11) lgkmcnt(1)\n"
        "v_mfma_f32_32x32x16_bf16 v[148:163], v[212:215], v[180:183], v[148:163]\n"
        "ds_read_b128 v[180:183], v7 offset:24576\n"
        "v_exp_f32 v63, v63\n"
        "v_exp_f32 v32, v32\n"
        "v_exp_f32 v33, v33\n"
        "v_smfmac_f32_32x32x32_bf16 v[112:127], v[144:147], v[56:63], v20\n"
        "s_mov_b64 exec, s[60:61]\n"
        "ds_read_b128 v[144:147], v9 offset:33280\n"
        "ds_read_b128 v[252:255], v9 offset:35840\n"
        "s_mov_b64 exec, -1\n"
        "v_exp_f32 v34, v34\n"
        "v_exp_f32 v35, v35\n"
        "v_exp_f32 v36, v36\n"
        "v_mfma_f32_32x32x16_bf16 v[48:63], v[136:139], v[132:135], v[80:95]\n"
        "ds_read_b128 v[136:139], v8 offset:29696\n"
        "buffer_store_dword v3, v27, s[20:23], 0 offen sc1\n"
        "v_exp_f32 v37, v37\n"
        "v_exp_f32 v38, v38\n"
        "v_exp_f32 v39, v39\n"
        "s_waitcnt lgkmcnt(1)\n"
        "v_mfma_f32_32x32x16_bf16 v[164:179], v[212:215], v[184:187], v[164:179]\n"
        "ds_read_b128 v[184:187], v7 offset:30720\n"
        "buffer_load_dwordx4 v[240:243], v0, s[40:43], s62 offen offset:1024 sc1\n"
        "v_exp_f32 v40, v40\n"
        "v_exp_f32 v41, v41\n"
        "v_exp_f32 v42, v42\n"
        "v_smfmac_f32_32x32x32_bf16 v[96:111], v[140:143], v[32:39], v20\n"
        "buffer_load_dwordx4 v[244:247], v0, s[40:43], s62 offen offset:2048 sc1\n"
        "v_exp_f32 v43, v43\n"
        "v_exp_f32 v44, v44\n"
        "v_exp_f32 v45, v45\n"
        "s_waitcnt vmcnt(13) lgkmcnt(0)\n"
        "v_mfma_f32_32x32x16_bf16 v[148:163], v[216:219], v[180:183], v[148:163]\n"
        "ds_read_b128 v[180:183], v4 offset:36864\n"
        "v_exp_f32 v46, v46\n"
        "v_exp_f32 v47, v47\n"
        "v_exp_f32 v48, v48\n"
        "v_exp_f32 v49, v49\n"
        "v_smfmac_f32_32x32x32_bf16 v[96:111], v[144:147], v[40:47], v20\n"
        "s_mov_b32 s63, 32768\n"
        "buffer_load_dwordx4 v15, s[20:23], s63 offen offset:2048 sc1 lds\n"
        "v_exp_f32 v50, v50\n"
        "v_exp_f32 v51, v51\n"
        "v_mfma_f32_32x32x16_bf16 v[32:47], v[136:139], v[128:131], v[64:79]\n"
        "s_add_u32 m0, s56, 12288\n"
        "s_movk_i32 s7, 1152\n"
        "buffer_load_dwordx4 v12, s[24:27], s7 offen offset:0 lds\n"
        "v_exp_f32 v52, v52\n"
        "v_exp_f32 v53, v53\n"
        "v_exp_f32 v54, v54\n"
        "v_mfma_f32_32x32x16_bf16 v[164:179], v[216:219], v[184:187], v[164:179]\n"
        "ds_read_b128 v[184:187], v4 offset:43008\n"
        "v_exp_f32 v55, v55\n"
        "v_exp_f32 v56, v56\n"
        "v_exp_f32 v57, v57\n"
        "s_waitcnt vmcnt(12) lgkmcnt(1)\n"
        "v_smfmac_f32_32x32x32_bf16 v[112:127], v[140:143], v[48:55], v20\n"
        "ds_add_u32 v14, v11\n"
        "v_exp_f32 v58, v58\n"
        "v_exp_f32 v59, v59\n"
        "s_waitcnt vmcnt(11)\n"
        "v_mfma_f32_32x32x16_bf16 v[148:163], v[220:223], v[180:183], v[148:163]\n"
        "ds_read_b128 v[180:183], v5 offset:36864\n"
        "v_exp_f32 v60, v60\n"
        "v_exp_f32 v61, v61\n"
        "v_exp_f32 v62, v62\n"
        "s_waitcnt lgkmcnt(2)\n"
        "v_mfma_f32_32x32x16_bf16 v[164:179], v[220:223], v[184:187], v[164:179]\n"
        "ds_read_b128 v[184:187], v5 offset:43008\n"
        "v_exp_f32 v63, v63\n"
        "v_exp_f32 v32, v32\n"
        "v_exp_f32 v33, v33\n"
        "v_smfmac_f32_32x32x32_bf16 v[112:127], v[144:147], v[56:63], v20\n"
        "s_mov_b64 exec, s[60:61]\n"
        "ds_read_b128 v[144:147], v9 offset:36352\n"
        "ds_read_b128 v[140:143], v9 offset:45056\n"
        "s_mov_b64 exec, -1\n"
        "ds_read_b32 v10, v14\n"
        "ds_read_b32 v24, v25\n"
        "v_exp_f32 v34, v34\n"
        "v_exp_f32 v35, v35\n"
        "v_exp_f32 v36, v36\n"
        "v_mfma_f32_32x32x16_bf16 v[48:63], v[136:139], v[132:135], v[80:95]\n"
        "ds_read_b128 v[136:139], v8 offset:38912\n"
        "buffer_load_dwordx4 v13, s[24:27], s7 offen offset:1024 lds\n"
        "v_exp_f32 v37, v37\n"
        "v_exp_f32 v38, v38\n"
        "v_exp_f32 v39, v39\n"
        "s_waitcnt vmcnt(11) lgkmcnt(1)\n"
        "v_mfma_f32_32x32x16_bf16 v[148:163], v[224:227], v[180:183], v[148:163]\n"
        "ds_read_b128 v[180:183], v6 offset:36864\n"
        "buffer_load_dwordx4 v[248:251], v0, s[40:43], s62 offen offset:3072 sc1\n"
        "v_exp_f32 v40, v40\n"
        "v_exp_f32 v41, v41\n"
        "v_exp_f32 v42, v42\n"
        "v_cmp_gt_u32 vcc, 28, v10\n"
        "v_smfmac_f32_32x32x32_bf16 v[96:111], v[252:255], v[32:39], v20\n"
        "s_cbranch_vccnz 140f\n"
        "141:\n"
        "v_cmp_gt_u32 vcc, 28, v24\n"
        "v_exp_f32 v43, v43\n"
        "v_exp_f32 v44, v44\n"
        "v_exp_f32 v45, v45\n"
        "v_mfma_f32_32x32x16_bf16 v[164:179], v[224:227], v[184:187], v[164:179]\n"
        "ds_read_b128 v[184:187], v6 offset:43008\n"
        "s_setprio 3\n"
        "s_cbranch_vccz 142f\n"
        "s_setprio 2\n"
        "142:\n"
        "v_exp_f32 v46, v46\n"
        "v_exp_f32 v47, v47\n"
        "v_exp_f32 v48, v48\n"
        "v_exp_f32 v49, v49\n"
        "v_smfmac_f32_32x32x32_bf16 v[96:111], v[144:147], v[40:47], v20\n"
        "s_mov_b32 s62, 32768\n"
        "buffer_load_dwordx4 v[188:191], v0, s[40:43], s62 offen offset:0 sc1\n"
        "v_exp_f32 v50, v50\n"
        "v_exp_f32 v51, v51\n"
        "s_waitcnt lgkmcnt(0)\n"
        "v_mfma_f32_32x32x16_bf16 v[32:47], v[136:139], v[128:131], v[64:79]\n"
        "buffer_load_dwordx4 v[192:195], v0, s[40:43], s62 offen offset:1024 sc1\n"
        "v_exp_f32 v52, v52\n"
        "v_exp_f32 v53, v53\n"
        "v_exp_f32 v54, v54\n"
        "s_waitcnt vmcnt(13)\n"
        "v_mfma_f32_32x32x16_bf16 v[148:163], v[228:231], v[180:183], v[148:163]\n"
        "ds_read_b128 v[180:183], v7 offset:36864\n"
        "buffer_load_dwordx4 v[196:199], v0, s[40:43], s62 offen offset:2048 sc1\n"
        "v_exp_f32 v55, v55\n"
        "v_exp_f32 v56, v56\n"
        "v_exp_f32 v57, v57\n"
        "v_smfmac_f32_32x32x32_bf16 v[112:127], v[252:255], v[48:55], v20\n"
        "v_exp_f32 v58, v58\n"
        "v_exp_f32 v59, v59\n"
        "v_mfma_f32_32x32x16_bf16 v[164:179], v[228:231], v[184:187], v[164:179]\n"
        "ds_read_b128 v[184:187], v7 offset:43008\n"
        "v_exp_f32 v60, v60\n"
        "v_exp_f32 v61, v61\n"
        "v_exp_f32 v62, v62\n"
        "s_waitcnt vmcnt(13) lgkmcnt(1)\n"
        "v_mfma_f32_32x32x16_bf16 v[148:163], v[232:235], v[180:183], v[148:163]\n"
        "ds_read_b128 v[180:183], v4 offset:49152\n"
        "v_exp_f32 v63, v63\n"
        "v_exp_f32 v32, v32\n"
        "v_exp_f32 v33, v33\n"
        "v_smfmac_f32_32x32x32_bf16 v[112:127], v[144:147], v[56:63], v20\n"
        "s_mov_b64 exec, s[60:61]\n"
        "ds_read_b128 v[144:147], v9 offset:45568\n"
        "ds_read_b128 v[252:255], v9 offset:48128\n"
        "s_mov_b64 exec, -1\n"
        "v_exp_f32 v34, v34\n"
        "v_exp_f32 v35, v35\n"
        "v_exp_f32 v36, v36\n"
        "v_mfma_f32_32x32x16_bf16 v[48:63], v[136:139], v[132:135], v[80:95]\n"
        "ds_read_b128 v[136:139], v8 offset:41984\n"
        "buffer_load_dwordx4 v[200:203], v0, s[40:43], s62 offen offset:3072 sc1\n"
        "v_exp_f32 v37, v37\n"
        "v_exp_f32 v38, v38\n"
        "v_exp_f32 v39, v39\n"
        "s_waitcnt lgkmcnt(1)\n"
        "v_mfma_f32_32x32x16_bf16 v[164:179], v[232:235], v[184:187], v[164:179]\n"
        "ds_read_b128 v[184:187], v4 offset:55296\n"
        "s_mov_b32 s63, 36864\n"
        "buffer_load_dwordx4 v15, s[20:23], s63 offen offset:2048 sc1 lds\n"
        "v_exp_f32 v40, v40\n"
        "v_exp_f32 v41, v41\n"
        "v_exp_f32 v42, v42\n"
        "s_waitcnt vmcnt(12) lgkmcnt(1)\n"
        "v_smfmac_f32_32x32x32_bf16 v[96:111], v[140:143], v[32:39], v20\n"
        "ds_add_u32 v14, v11\n"
        "v_exp_f32 v43, v43\n"
        "v_exp_f32 v44, v44\n"
        "v_exp_f32 v45, v45\n"
        "v_mfma_f32_32x32x16_bf16 v[148:163], v[236:239], v[180:183], v[148:163]\n"
        "ds_read_b128 v[180:183], v5 offset:49152\n"
        "v_exp_f32 v46, v46\n"
        "v_exp_f32 v47, v47\n"
        "v_exp_f32 v48, v48\n"
        "v_exp_f32 v49, v49\n"
        "v_smfmac_f32_32x32x32_bf16 v[96:111], v[144:147], v[40:47], v20\n"
        "s_add_u32 m0, s56, 24576\n"
        "s_movk_i32 s7, 1280\n"
        "buffer_load_dwordx4 v12, s[24:27], s7 offen offset:0 lds\n"
        "v_exp_f32 v50, v50\n"
        "v_exp_f32 v51, v51\n"
        "v_mfma_f32_32x32x16_bf16 v[32:47], v[136:139], v[128:131], v[64:79]\n"
        "buffer_load_dwordx4 v13, s[24:27], s7 offen offset:1024 lds\n"
        "v_exp_f32 v52, v52\n"
        "v_exp_f32 v53, v53\n"
        "v_exp_f32 v54, v54\n"
        "s_waitcnt lgkmcnt(0)\n"
        "v_mfma_f32_32x32x16_bf16 v[164:179], v[236:239], v[184:187], v[164:179]\n"
        "ds_read_b128 v[184:187], v5 offset:55296\n"
        "s_mov_b32 s62, 36864\n"
        "buffer_load_dwordx4 v[204:207], v0, s[40:43], s62 offen offset:0 sc1\n"
        "v_exp_f32 v55, v55\n"
        "v_exp_f32 v56, v56\n"
        "v_exp_f32 v57, v57\n"
        "v_smfmac_f32_32x32x32_bf16 v[112:127], v[140:143], v[48:55], v20\n"
        "v_exp_f32 v58, v58\n"
        "v_exp_f32 v59, v59\n"
        "s_waitcnt vmcnt(13)\n"
        "v_mfma_f32_32x32x16_bf16 v[148:163], v[240:243], v[180:183], v[148:163]\n"
        "ds_read_b128 v[180:183], v6 offset:49152\n"
        "v_exp_f32 v60, v60\n"
        "v_exp_f32 v61, v61\n"
        "v_exp_f32 v62, v62\n"
        "s_waitcnt lgkmcnt(1)\n"
        "v_mfma_f32_32x32x16_bf16 v[164:179], v[240:243], v[184:187], v[164:179]\n"
        "ds_read_b128 v[184:187], v6 offset:55296\n"
        "v_exp_f32 v63, v63\n"
        "v_exp_f32 v32, v32\n"
        "v_exp_f32 v33, v33\n"
        "v_smfmac_f32_32x32x32_bf16 v[112:127], v[144:147], v[56:63], v20\n"
        "s_mov_b64 exec, s[60:61]\n"
        "ds_read_b128 v[144:147], v9 offset:48640\n"
        "ds_read_b128 v[140:143], v9 offset:57344\n"
        "s_mov_b64 exec, -1\n"
        "ds_read_b32 v10, v14\n"
        "ds_read_b32 v24, v25\n"
        "v_exp_f32 v34, v34\n"
        "v_exp_f32 v35, v35\n"
        "v_exp_f32 v36, v36\n"
        "v_mfma_f32_32x32x16_bf16 v[48:63], v[136:139], v[132:135], v[80:95]\n"
        "ds_read_b128 v[136:139], v8 offset:51200\n"
        "buffer_load_dwordx4 v[208:211], v0, s[40:43], s62 offen offset:1024 sc1\n"
        "v_exp_f32 v37, v37\n"
        "v_exp_f32 v38, v38\n"
        "v_exp_f32 v39, v39\n"
        "s_waitcnt vmcnt(13) lgkmcnt(1)\n"
        "v_mfma_f32_32x32x16_bf16 v[148:163], v[244:247], v[180:183], v[148:163]\n"
        "ds_read_b128 v[180:183], v7 offset:49152\n"
        "buffer_load_dwordx4 v[212:215], v0, s[40:43], s62 offen offset:2048 sc1\n"
        "v_exp_f32 v40, v40\n"
        "v_exp_f32 v41, v41\n"
        "v_exp_f32 v42, v42\n"
        "v_cmp_gt_u32 vcc, 32, v10\n"
        "v_smfmac_f32_32x32x32_bf16 v[96:111], v[252:255], v[32:39], v20\n"
        "s_cbranch_vccnz 143f\n"
        "144:\n"
        "v_cmp_gt_u32 vcc, 32, v24\n"
        "v_exp_f32 v43, v43\n"
        "v_exp_f32 v44, v44\n"
        "v_exp_f32 v45, v45\n"
        "v_mfma_f32_32x32x16_bf16 v[164:179], v[244:247], v[184:187], v[164:179]\n"
        "ds_read_b128 v[184:187], v7 offset:55296\n"
        "s_setprio 3\n"
        "s_cbranch_vccz 145f\n"
        "s_setprio 2\n"
        "145:\n"
        "v_exp_f32 v46, v46\n"
        "v_exp_f32 v47, v47\n"
        "v_exp_f32 v48, v48\n"
        "v_exp_f32 v49, v49\n"
        "v_smfmac_f32_32x32x32_bf16 v[96:111], v[144:147], v[40:47], v20\n"
        "buffer_load_dwordx4 v[216:219], v0, s[40:43], s62 offen offset:3072 sc1\n"
        "v_exp_f32 v50, v50\n"
        "v_exp_f32 v51, v51\n"
        "v_exp_f32 v52, v52\n"
        "s_waitcnt lgkmcnt(0)\n"
        "v_mfma_f32_32x32x16_bf16 v[32:47], v[136:139], v[128:131], v[64:79]\n"
        "s_mov_b32 s63, 40960\n"
        "buffer_load_dwordx4 v15, s[20:23], s63 offen offset:2048 sc1 lds\n"
        "v_exp_f32 v53, v53\n"
        "v_exp_f32 v54, v54\n"
        "v_exp_f32 v55, v55\n"
        "s_waitcnt vmcnt(12)\n"
        "v_mfma_f32_32x32x16_bf16 v[148:163], v[248:251], v[180:183], v[148:163]\n"
        "ds_read_b128 v[180:183], v4 offset:0\n"
        "v_exp_f32 v56, v56\n"
        "v_exp_f32 v57, v57\n"
        "v_exp_f32 v58, v58\n"
        "v_smfmac_f32_32x32x32_bf16 v[112:127], v[252:255], v[48:55], v20\n"
        "ds_add_u32 v14, v11\n"
        "v_exp_f32 v59, v59\n"
        "v_exp_f32 v60, v60\n"
        "v_exp_f32 v61, v61\n"
        "v_mfma_f32_32x32x16_bf16 v[164:179], v[248:251], v[184:187], v[164:179]\n"
        "ds_read_b128 v[184:187], v4 offset:6144\n"
        "v_exp_f32 v62, v62\n"
        "v_exp_f32 v63, v63\n"
        "v_exp_f32 v32, v32\n"
        "v_exp_f32 v33, v33\n"
        "v_smfmac_f32_32x32x32_bf16 v[112:127], v[144:147], v[56:63], v20\n"
        "s_mov_b64 exec, s[60:61]\n"
        "ds_read_b128 v[144:147], v9 offset:57856\n"
        "ds_read_b128 v[252:255], v9 offset:60416\n"
        "s_mov_b64 exec, -1\n"
        "v_exp_f32 v34, v34\n"
        "v_exp_f32 v35, v35\n"
        "v_mfma_f32_32x32x16_bf16 v[48:63], v[136:139], v[132:135], v[80:95]\n"
        "ds_read_b128 v[136:139], v8 offset:54272\n"
        "s_add_u32 m0, s56, 36864\n"
        "s_movk_i32 s7, 1408\n"
        "buffer_load_dwordx4 v12, s[24:27], s7 offen offset:0 lds\n"
        "v_exp_f32 v36, v36\n"
        "v_exp_f32 v37, v37\n"
        "v_exp_f32 v38, v38\n"
        "s_waitcnt vmcnt(12) lgkmcnt(1)\n"
        "v_mfma_f32_32x32x16_bf16 v[148:163], v[188:191], v[180:183], v[148:163]\n"
        "ds_read_b128 v[180:183], v5 offset:0\n"
        "buffer_load_dwordx4 v13, s[24:27], s7 offen offset:1024 lds\n"
        "v_exp_f32 v39, v39\n"
        "v_exp_f32 v40, v40\n"
        "v_exp_f32 v41, v41\n"
        "v_smfmac_f32_32x32x32_bf16 v[96:111], v[140:143], v[32:39], v20\n"
        "s_mov_b32 s62, 40960\n"
        "buffer_load_dwordx4 v[220:223], v0, s[40:43], s62 offen offset:0 sc1\n"
        "v_exp_f32 v42, v42\n"
        "v_exp_f32 v43, v43\n"
        "v_mfma_f32_32x32x16_bf16 v[164:179], v[188:191], v[184:187], v[164:179]\n"
        "ds_read_b128 v[184:187], v5 offset:6144\n"
        "v_exp_f32 v44, v44\n"
        "v_exp_f32 v45, v45\n"
        "v_exp_f32 v46, v46\n"
        "s_waitcnt vmcnt(13) lgkmcnt(1)\n"
        "v_mfma_f32_32x32x16_bf16 v[148:163], v[192:195], v[180:183], v[148:163]\n"
        "ds_read_b128 v[180:183], v6 offset:0\n"
        "v_exp_f32 v47, v47\n"
        "v_exp_f32 v48, v48\n"
        "v_exp_f32 v49, v49\n"
        "v_smfmac_f32_32x32x32_bf16 v[96:111], v[144:147], v[40:47], v20\n"
        "ds_read_b32 v10, v14\n"
        "ds_read_b32 v24, v25\n"
        "v_exp_f32 v50, v50\n"
        "v_exp_f32 v51, v51\n"
        "v_exp_f32 v52, v52\n"
        "v_mfma_f32_32x32x16_bf16 v[32:47], v[136:139], v[128:131], v[64:79]\n"
        "buffer_load_dwordx4 v[224:227], v0, s[40:43], s62 offen offset:1024 sc1\n"
        "v_exp_f32 v53, v53\n"
        "v_exp_f32 v54, v54\n"
        "v_exp_f32 v55, v55\n"
        "s_waitcnt lgkmcnt(2)\n"
        "v_mfma_f32_32x32x16_bf16 v[164:179], v[192:195], v[184:187], v[164:179]\n"
        "ds_read_b128 v[184:187], v6 offset:6144\n"
        "buffer_load_dwordx4 v[228:231], v0, s[40:43], s62 offen offset:2048 sc1\n"
        "v_exp_f32 v56, v56\n"
        "v_exp_f32 v57, v57\n"
        "v_exp_f32 v58, v58\n"
        "s_waitcnt lgkmcnt(1)\n"
        "v_cmp_gt_u32 vcc, 36, v10\n"
        "v_smfmac_f32_32x32x32_bf16 v[112:127], v[140:143], v[48:55], v20\n"
        "s_cbranch_vccnz 146f\n"
        "147:\n"
        "v_cmp_gt_u32 vcc, 36, v24\n"
        "v_exp_f32 v59, v59\n"
        "v_exp_f32 v60, v60\n"
        "v_exp_f32 v61, v61\n"
        "s_waitcnt vmcnt(14) lgkmcnt(0)\n"
        "v_mfma_f32_32x32x16_bf16 v[148:163], v[196:199], v[180:183], v[148:163]\n"
        "ds_read_b128 v[180:183], v7 offset:0\n"
        "s_setprio 3\n"
        "s_cbranch_vccz 148f\n"
        "s_setprio 2\n"
        "148:\n"
        "v_exp_f32 v62, v62\n"
        "v_exp_f32 v63, v63\n"
        "v_exp_f32 v32, v32\n"
        "v_exp_f32 v33, v33\n"
        "v_smfmac_f32_32x32x32_bf16 v[112:127], v[144:147], v[56:63], v20\n"
        "s_mov_b64 exec, s[60:61]\n"
        "ds_read_b128 v[144:147], v9 offset:60928\n"
        "ds_read_b128 v[140:143], v9 offset:8192\n"
        "s_mov_b64 exec, -1\n"
        "v_exp_f32 v34, v34\n"
        "v_exp_f32 v35, v35\n"
        "v_mfma_f32_32x32x16_bf16 v[48:63], v[136:139], v[132:135], v[80:95]\n"
        "ds_read_b128 v[136:139], v8 offset:2048\n"
        "buffer_load_dwordx4 v[232:235], v0, s[40:43], s62 offen offset:3072 sc1\n"
        "v_exp_f32 v36, v36\n"
        "v_exp_f32 v37, v37\n"
        "v_exp_f32 v38, v38\n"
        "v_mfma_f32_32x32x16_bf16 v[164:179], v[196:199], v[184:187], v[164:179]\n"
        "ds_read_b128 v[184:187], v7 offset:6144\n"
        "s_mov_b32 s63, 45056\n"
        "buffer_load_dwordx4 v15, s[20:23], s63 offen offset:2048 sc1 lds\n"
        "v_exp_f32 v39, v39\n"
        "v_exp_f32 v40, v40\n"
        "v_exp_f32 v41, v41\n"
        "v_smfmac_f32_32x32x32_bf16 v[96:111], v[252:255], v[32:39], v20\n"
        "v_exp_f32 v42, v42\n"
        "v_exp_f32 v43, v43\n"
        "s_waitcnt vmcnt(15) lgkmcnt(1)\n"
        "v_mfma_f32_32x32x16_bf16 v[148:163], v[200:203], v[180:183], v[148:163]\n"
        "ds_read_b128 v[180:183], v4 offset:12288\n"
        "v_exp_f32 v44, v44\n"
        "v_exp_f32 v45, v45\n"
        "v_exp_f32 v46, v46\n"
        "s_waitcnt lgkmcnt(1)\n"
        "v_mfma_f32_32x32x16_bf16 v[164:179], v[200:203], v[184:187], v[164:179]\n"
        "ds_read_b128 v[184:187], v4 offset:18432\n"
        "v_exp_f32 v47, v47\n"
        "v_exp_f32 v48, v48\n"
        "v_exp_f32 v49, v49\n"
        "v_smfmac_f32_32x32x32_bf16 v[96:111], v[144:147], v[40:47], v20\n"
        "s_add_u32 m0, s56, 49152\n"
        "s_movk_i32 s7, 1536\n"
        "buffer_load_dwordx4 v12, s[24:27], s7 offen offset:0 lds\n"
        "v_exp_f32 v50, v50\n"
        "v_exp_f32 v51, v51\n"
        "v_exp_f32 v52, v52\n"
        "v_mfma_f32_32x32x16_bf16 v[32:47], v[136:139], v[128:131], v[64:79]\n"
        "buffer_load_dwordx4 v13, s[24:27], s7 offen offset:1024 lds\n"
        "v_exp_f32 v53, v53\n"
        "v_exp_f32 v54, v54\n"
        "v_exp_f32 v55, v55\n"
        "s_waitcnt vmcnt(13) lgkmcnt(0)\n"
        "v_mfma_f32_32x32x16_bf16 v[148:163], v[204:207], v[180:183], v[148:163]\n"
        "ds_read_b128 v[180:183], v5 offset:12288\n"
        "s_mov_b32 s62, 45056\n"
        "buffer_load_dwordx4 v[236:239], v0, s[40:43], s62 offen offset:0 sc1\n"
        "v_exp_f32 v56, v56\n"
        "v_exp_f32 v57, v57\n"
        "v_exp_f32 v58, v58\n"
        "s_waitcnt vmcnt(12)\n"
        "v_smfmac_f32_32x32x32_bf16 v[112:127], v[252:255], v[48:55], v20\n"
        "ds_add_u32 v14, v11\n"
        "v_exp_f32 v59, v59\n"
        "v_exp_f32 v60, v60\n"
        "v_exp_f32 v61, v61\n"
        "v_mfma_f32_32x32x16_bf16 v[164:179], v[204:207], v[184:187], v[164:179]\n"
        "ds_read_b128 v[184:187], v5 offset:18432\n"
        "v_exp_f32 v62, v62\n"
        "v_exp_f32 v63, v63\n"
        "v_exp_f32 v32, v32\n"
        "v_exp_f32 v33, v33\n"
        "v_smfmac_f32_32x32x32_bf16 v[112:127], v[144:147], v[56:63], v20\n"
        "s_mov_b64 exec, s[60:61]\n"
        "ds_read_b128 v[144:147], v9 offset:8704\n"
        "ds_read_b128 v[252:255], v9 offset:11264\n"
        "s_mov_b64 exec, -1\n"
        "ds_read_b32 v10, v14\n"
        "ds_read_b32 v24, v25\n"
        "v_exp_f32 v34, v34\n"
        "v_exp_f32 v35, v35\n"
        "v_mfma_f32_32x32x16_bf16 v[48:63], v[136:139], v[132:135], v[80:95]\n"
        "ds_read_b128 v[136:139], v8 offset:5120\n"
        "buffer_load_dwordx4 v[240:243], v0, s[40:43], s62 offen offset:1024 sc1\n"
        "v_exp_f32 v36, v36\n"
        "v_exp_f32 v37, v37\n"
        "v_exp_f32 v38, v38\n"
        "s_waitcnt lgkmcnt(3)\n"
        "v_mfma_f32_32x32x16_bf16 v[148:163], v[208:211], v[180:183], v[148:163]\n"
        "ds_read_b128 v[180:183], v6 offset:12288\n"
        "buffer_load_dwordx4 v[244:247], v0, s[40:43], s62 offen offset:2048 sc1\n"
        "v_exp_f32 v39, v39\n"
        "v_exp_f32 v40, v40\n"
        "v_exp_f32 v41, v41\n"
        "s_waitcnt lgkmcnt(1)\n"
        "v_cmp_gt_u32 vcc, 40, v10\n"
        "v_smfmac_f32_32x32x32_bf16 v[96:111], v[140:143], v[32:39], v20\n"
        "s_cbranch_vccnz 149f\n"
        "150:\n"
        "v_cmp_gt_u32 vcc, 40, v24\n"
        "v_exp_f32 v42, v42\n"
        "v_exp_f32 v43, v43\n"
        "v_mfma_f32_32x32x16_bf16 v[164:179], v[208:211], v[184:187], v[164:179]\n"
        "ds_read_b128 v[184:187], v6 offset:18432\n"
        "s_setprio 3\n"
        "s_cbranch_vccz 151f\n"
        "s_setprio 2\n"
        "151:\n"
        "v_exp_f32 v44, v44\n"
        "v_exp_f32 v45, v45\n"
        "v_exp_f32 v46, v46\n"
        "s_waitcnt lgkmcnt(1)\n"
        "v_mfma_f32_32x32x16_bf16 v[148:163], v[212:215], v[180:183], v[148:163]\n"
        "ds_read_b128 v[180:183], v7 offset:12288\n"
        "v_exp_f32 v47, v47\n"
        "v_exp_f32 v48, v48\n"
        "v_exp_f32 v49, v49\n"
        "v_smfmac_f32_32x32x32_bf16 v[96:111], v[144:147], v[40:47], v20\n"
        "s_add_u32 m0, s56, 0\n"
        "s_movk_i32 s7, 1664\n"
        "buffer_load_dwordx4 v12, s[24:27], s7 offen offset:0 lds\n"
        "v_exp_f32 v50, v50\n"
        "v_exp_f32 v51, v51\n"
        "v_exp_f32 v52, v52\n"
        "v_mfma_f32_32x32x16_bf16 v[32:47], v[136:139], v[128:131], v[64:79]\n"
        "buffer_load_dwordx4 v13, s[24:27], s7 offen offset:1024 lds\n"
        "v_exp_f32 v53, v53\n"
        "v_exp_f32 v54, v54\n"
        "v_exp_f32 v55, v55\n"
        "s_waitcnt lgkmcnt(0)\n"
        "v_mfma_f32_32x32x16_bf16 v[164:179], v[212:215], v[184:187], v[164:179]\n"
        "ds_read_b128 v[184:187], v7 offset:18432\n"
        "v_exp_f32 v56, v56\n"
        "v_exp_f32 v57, v57\n"
        "v_exp_f32 v58, v58\n"
        "v_smfmac_f32_32x32x32_bf16 v[112:127], v[140:143], v[48:55], v20\n"
        "v_exp_f32 v59, v59\n"
        "v_exp_f32 v60, v60\n"
        "v_exp_f32 v61, v61\n"
        "s_waitcnt vmcnt(15)\n"
        "v_mfma_f32_32x32x16_bf16 v[148:163], v[216:219], v[180:183], v[148:163]\n"
        "ds_read_b128 v[180:183], v4 offset:24576\n"
        "v_exp_f32 v62, v62\n"
        "v_exp_f32 v63, v63\n"
        "v_exp_f32 v32, v32\n"
        "v_exp_f32 v33, v33\n"
        "v_smfmac_f32_32x32x32_bf16 v[112:127], v[144:147], v[56:63], v20\n"
        "s_mov_b64 exec, s[60:61]\n"
        "ds_read_b128 v[144:147], v9 offset:11776\n"
        "ds_read_b128 v[140:143], v9 offset:20480\n"
        "s_mov_b64 exec, -1\n"
        "v_exp_f32 v34, v34\n"
        "v_exp_f32 v35, v35\n"
        "v_mfma_f32_32x32x16_bf16 v[48:63], v[136:139], v[132:135], v[80:95]\n"
        "ds_read_b128 v[136:139], v8 offset:14336\n"
        "buffer_load_dwordx4 v[248:251], v0, s[40:43], s62 offen offset:3072 sc1\n"
        "v_exp_f32 v36, v36\n"
        "v_exp_f32 v37, v37\n"
        "v_exp_f32 v38, v38\n"
        "s_waitcnt lgkmcnt(1)\n"
        "v_mfma_f32_32x32x16_bf16 v[164:179], v[216:219], v[184:187], v[164:179]\n"
        "ds_read_b128 v[184:187], v4 offset:30720\n"
        "s_mov_b32 s62, 49152\n"
        "buffer_load_dwordx4 v[188:191], v0, s[40:43], s62 offen offset:0 sc1\n"
        "v_exp_f32 v39, v39\n"
        "v_exp_f32 v40, v40\n"
        "v_exp_f32 v41, v41\n"
        "s_waitcnt vmcnt(11) lgkmcnt(1)\n"
        "v_smfmac_f32_32x32x32_bf16 v[96:111], v[252:255], v[32:39], v20\n"
        "ds_add_u32 v14, v11\n"
        "buffer_load_dwordx4 v[192:195], v0, s[40:43], s62 offen offset:1024 sc1\n"
        "v_exp_f32 v42, v42\n"
        "v_exp_f32 v43, v43\n"
        "v_mfma_f32_32x32x16_bf16 v[148:163], v[220:223], v[180:183], v[148:163]\n"
        "ds_read_b128 v[180:183], v5 offset:24576\n"
        "v_exp_f32 v44, v44\n"
        "v_exp_f32 v45, v45\n"
        "v_exp_f32 v46, v46\n"
        "s_waitcnt lgkmcnt(2)\n"
        "v_mfma_f32_32x32x16_bf16 v[164:179], v[220:223], v[184:187], v[164:179]\n"
        "ds_read_b128 v[184:187], v5 offset:30720\n"
        "v_exp_f32 v47, v47\n"
        "v_exp_f32 v48, v48\n"
        "v_exp_f32 v49, v49\n"
        "v_smfmac_f32_32x32x32_bf16 v[96:111], v[144:147], v[40:47], v20\n"
        "ds_read_b32 v10, v14\n"
        "ds_read_b32 v24, v25\n"
        "v_exp_f32 v50, v50\n"
        "v_exp_f32 v51, v51\n"
        "v_exp_f32 v52, v52\n"
        "v_mfma_f32_32x32x16_bf16 v[32:47], v[136:139], v[128:131], v[64:79]\n"
        "buffer_load_dwordx4 v[196:199], v0, s[40:43], s62 offen offset:2048 sc1\n"
        "v_exp_f32 v53, v53\n"
        "v_exp_f32 v54, v54\n"
        "v_exp_f32 v55, v55\n"
        "s_waitcnt lgkmcnt(2)\n"
        "v_mfma_f32_32x32x16_bf16 v[148:163], v[224:227], v[180:183], v[148:163]\n"
        "ds_read_b128 v[180:183], v6 offset:24576\n"
        "buffer_load_dwordx4 v[200:203], v0, s[40:43], s62 offen offset:3072 sc1\n"
        "v_exp_f32 v56, v56\n"
        "v_exp_f32 v57, v57\n"
        "v_exp_f32 v58, v58\n"
        "s_waitcnt lgkmcnt(1)\n"
        "v_cmp_gt_u32 vcc, 44, v10\n"
        "v_smfmac_f32_32x32x32_bf16 v[112:127], v[252:255], v[48:55], v20\n"
        "s_cbranch_vccnz 152f\n"
        "153:\n"
        "v_cmp_gt_u32 vcc, 44, v24\n"
        "v_exp_f32 v59, v59\n"
        "v_exp_f32 v60, v60\n"
        "v_exp_f32 v61, v61\n"
        "v_mfma_f32_32x32x16_bf16 v[164:179], v[224:227], v[184:187], v[164:179]\n"
        "ds_read_b128 v[184:187], v6 offset:30720\n"
        "s_setprio 3\n"
        "s_cbranch_vccz 154f\n"
        "s_setprio 2\n"
        "154:\n"
        "v_exp_f32 v62, v62\n"
        "v_exp_f32 v63, v63\n"
        "v_exp_f32 v32, v32\n"
        "v_exp_f32 v33, v33\n"
        "v_smfmac_f32_32x32x32_bf16 v[112:127], v[144:147], v[56:63], v20\n"
        "s_mov_b64 exec, s[60:61]\n"
        "ds_read_b128 v[144:147], v9 offset:20992\n"
        "ds_read_b128 v[252:255], v9 offset:23552\n"
        "s_mov_b64 exec, -1\n"
        "v_exp_f32 v34, v34\n"
        "v_exp_f32 v35, v35\n"
        "v_mfma_f32_32x32x16_bf16 v[48:63], v[136:139], v[132:135], v[80:95]\n"
        "ds_read_b128 v[136:139], v8 offset:17408\n"
        "s_add_u32 m0, s56, 49152\n"
        "s_mov_b32 s63, 49152\n"
        "buffer_load_dwordx4 v15, s[20:23], s63 offen offset:2048 sc1 lds\n"
        "v_exp_f32 v36, v36\n"
        "v_exp_f32 v37, v37\n"
        "v_exp_f32 v38, v38\n"
        "s_waitcnt lgkmcnt(1)\n"
        "v_mfma_f32_32x32x16_bf16 v[148:163], v[228:231], v[180:183], v[148:163]\n"
        "ds_read_b128 v[180:183], v7 offset:24576\n"
        "s_add_u32 m0, s56, 12288\n"
        "s_movk_i32 s7, 1792\n"
        "buffer_load_dwordx4 v12, s[24:27], s7 offen offset:0 lds\n"
        "v_exp_f32 v39, v39\n"
        "v_exp_f32 v40, v40\n"
        "v_exp_f32 v41, v41\n"
        "v_smfmac_f32_32x32x32_bf16 v[96:111], v[140:143], v[32:39], v20\n"
        "v_exp_f32 v42, v42\n"
        "v_exp_f32 v43, v43\n"
        "v_mfma_f32_32x32x16_bf16 v[164:179], v[228:231], v[184:187], v[164:179]\n"
        "ds_read_b128 v[184:187], v7 offset:30720\n"
        "v_exp_f32 v44, v44\n"
        "v_exp_f32 v45, v45\n"
        "v_exp_f32 v46, v46\n"
        "s_waitcnt vmcnt(15) lgkmcnt(1)\n"
        "v_mfma_f32_32x32x16_bf16 v[148:163], v[232:235], v[180:183], v[148:163]\n"
        "ds_read_b128 v[180:183], v4 offset:36864\n"
        "v_exp_f32 v47, v47\n"
        "v_exp_f32 v48, v48\n"
        "v_exp_f32 v49, v49\n"
        "v_smfmac_f32_32x32x32_bf16 v[96:111], v[144:147], v[40:47], v20\n"
        "buffer_load_dwordx4 v13, s[24:27], s7 offen offset:1024 lds\n"
        "v_exp_f32 v50, v50\n"
        "v_exp_f32 v51, v51\n"
        "v_exp_f32 v52, v52\n"
        "v_mfma_f32_32x32x16_bf16 v[32:47], v[136:139], v[128:131], v[64:79]\n"
        "s_mov_b32 s62, 53248\n"
        "buffer_load_dwordx4 v[204:207], v0, s[40:43], s62 offen offset:0 sc1\n"
        "v_exp_f32 v53, v53\n"
        "v_exp_f32 v54, v54\n"
        "v_exp_f32 v55, v55\n"
        "s_waitcnt lgkmcnt(0)\n"
        "v_mfma_f32_32x32x16_bf16 v[164:179], v[232:235], v[184:187], v[164:179]\n"
        "ds_read_b128 v[184:187], v4 offset:43008\n"
        "buffer_load_dwordx4 v[208:211], v0, s[40:43], s62 offen offset:1024 sc1\n"
        "v_exp_f32 v56, v56\n"
        "v_exp_f32 v57, v57\n"
        "v_exp_f32 v58, v58\n"
        "s_waitcnt vmcnt(12)\n"
        "v_smfmac_f32_32x32x32_bf16 v[112:127], v[140:143], v[48:55], v20\n"
        "ds_add_u32 v14, v11\n"
        "v_exp_f32 v59, v59\n"
        "v_exp_f32 v60, v60\n"
        "v_exp_f32 v61, v61\n"
        "v_mfma_f32_32x32x16_bf16 v[148:163], v[236:239], v[180:183], v[148:163]\n"
        "ds_read_b128 v[180:183], v5 offset:36864\n"
        "v_exp_f32 v62, v62\n"
        "v_exp_f32 v63, v63\n"
        "v_exp_f32 v32, v32\n"
        "v_exp_f32 v33, v33\n"
        "v_smfmac_f32_32x32x32_bf16 v[112:127], v[144:147], v[56:63], v20\n"
        "s_mov_b64 exec, s[60:61]\n"
        "ds_read_b128 v[144:147], v9 offset:24064\n"
        "ds_read_b128 v[140:143], v9 offset:32768\n"
        "s_mov_b64 exec, -1\n"
        "v_exp_f32 v34, v34\n"
        "v_exp_f32 v35, v35\n"
        "v_mfma_f32_32x32x16_bf16 v[48:63], v[136:139], v[132:135], v[80:95]\n"
        "ds_read_b128 v[136:139], v8 offset:26624\n"
        "buffer_load_dwordx4 v[212:215], v0, s[40:43], s62 offen offset:2048 sc1\n"
        "v_exp_f32 v36, v36\n"
        "v_exp_f32 v37, v37\n"
        "v_exp_f32 v38, v38\n"
        "s_waitcnt lgkmcnt(1)\n"
        "v_mfma_f32_32x32x16_bf16 v[164:179], v[236:239], v[184:187], v[164:179]\n"
        "ds_read_b128 v[184:187], v5 offset:43008\n"
        "buffer_load_dwordx4 v[216:219], v0, s[40:43], s62 offen offset:3072 sc1\n"
        "v_exp_f32 v39, v39\n"
        "v_exp_f32 v40, v40\n"
        "v_exp_f32 v41, v41\n"
        "v_smfmac_f32_32x32x32_bf16 v[96:111], v[252:255], v[32:39], v20\n"
        "v_exp_f32 v42, v42\n"
        "v_exp_f32 v43, v43\n"
        "v_mfma_f32_32x32x16_bf16 v[148:163], v[240:243], v[180:183], v[148:163]\n"
        "ds_read_b128 v[180:183], v6 offset:36864\n"
        "v_exp_f32 v44, v44\n"
        "v_exp_f32 v45, v45\n"
        "v_exp_f32 v46, v46\n"
        "s_waitcnt lgkmcnt(1)\n"
        "v_mfma_f32_32x32x16_bf16 v[164:179], v[240:243], v[184:187], v[164:179]\n"
        "ds_read_b128 v[184:187], v6 offset:43008\n"
        "v_exp_f32 v47, v47\n"
        "v_exp_f32 v48, v48\n"
        "v_exp_f32 v49, v49\n"
        "v_smfmac_f32_32x32x32_bf16 v[96:111], v[144:147], v[40:47], v20\n"
        "ds_read_b32 v10, v14\n"
        "ds_read_b32 v24, v25\n"
        "v_exp_f32 v50, v50\n"
        "v_exp_f32 v51, v51\n"
        "v_exp_f32 v52, v52\n"
        "v_mfma_f32_32x32x16_bf16 v[32:47], v[136:139], v[128:131], v[64:79]\n"
        "s_mov_b32 s62, 57344\n"
        "buffer_load_dwordx4 v[220:223], v0, s[40:43], s62 offen offset:0 sc1\n"
        "v_exp_f32 v53, v53\n"
        "v_exp_f32 v54, v54\n"
        "v_exp_f32 v55, v55\n"
        "s_waitcnt lgkmcnt(0)\n"
        "v_mfma_f32_32x32x16_bf16 v[148:163], v[244:247], v[180:183], v[148:163]\n"
        "ds_read_b128 v[180:183], v7 offset:36864\n"
        "buffer_load_dwordx4 v[224:227], v0, s[40:43], s62 offen offset:1024 sc1\n"
        "v_exp_f32 v56, v56\n"
        "v_exp_f32 v57, v57\n"
        "v_exp_f32 v58, v58\n"
        "v_cmp_gt_u32 vcc, 48, v10\n"
        "v_smfmac_f32_32x32x32_bf16 v[112:127], v[252:255], v[48:55], v20\n"
        "s_cbranch_vccnz 155f\n"
        "156:\n"
        "v_cmp_gt_u32 vcc, 48, v24\n"
        "v_exp_f32 v59, v59\n"
        "v_exp_f32 v60, v60\n"
        "v_exp_f32 v61, v61\n"
        "v_mfma_f32_32x32x16_bf16 v[164:179], v[244:247], v[184:187], v[164:179]\n"
        "ds_read_b128 v[184:187], v7 offset:43008\n"
        "s_setprio 3\n"
        "s_cbranch_vccz 157f\n"
        "s_setprio 2\n"
        "157:\n"
        "v_exp_f32 v62, v62\n"
        "v_exp_f32 v63, v63\n"
        "v_exp_f32 v32, v32\n"
        "v_exp_f32 v33, v33\n"
        "v_smfmac_f32_32x32x32_bf16 v[112:127], v[144:147], v[56:63], v20\n"
        "s_mov_b64 exec, s[60:61]\n"
        "ds_read_b128 v[144:147], v9 offset:33280\n"
        "ds_read_b128 v[252:255], v9 offset:35840\n"
        "s_mov_b64 exec, -1\n"
        "v_exp_f32 v34, v34\n"
        "v_exp_f32 v35, v35\n"
        "v_exp_f32 v36, v36\n"
        "v_mfma_f32_32x32x16_bf16 v[48:63], v[136:139], v[132:135], v[80:95]\n"
        "ds_read_b128 v[136:139], v8 offset:29696\n"
        "s_add_u32 m0, s56, 0\n"
        "s_mov_b32 s63, 53248\n"
        "buffer_load_dwordx4 v15, s[20:23], s63 offen offset:2048 sc1 lds\n"
        "v_exp_f32 v37, v37\n"
        "v_exp_f32 v38, v38\n"
        "v_exp_f32 v39, v39\n"
        "s_waitcnt vmcnt(14) lgkmcnt(1)\n"
        "v_mfma_f32_32x32x16_bf16 v[148:163], v[248:251], v[180:183], v[148:163]\n"
        "ds_read_b128 v[180:183], v4 offset:49152\n"
        "s_add_u32 m0, s56, 24576\n"
        "s_movk_i32 s7, 1920\n"
        "buffer_load_dwordx4 v12, s[24:27], s7 offen offset:0 lds\n"
        "v_exp_f32 v40, v40\n"
        "v_exp_f32 v41, v41\n"
        "v_exp_f32 v42, v42\n"
        "s_waitcnt vmcnt(11) lgkmcnt(1)\n"
        "v_smfmac_f32_32x32x32_bf16 v[96:111], v[140:143], v[32:39], v20\n"
        "ds_add_u32 v14, v11\n"
        "buffer_load_dwordx4 v13, s[24:27], s7 offen offset:1024 lds\n"
        "v_exp_f32 v43, v43\n"
        "v_exp_f32 v44, v44\n"
        "v_exp_f32 v45, v45\n"
        "v_mfma_f32_32x32x16_bf16 v[164:179], v[248:251], v[184:187], v[164:179]\n"
        "ds_read_b128 v[184:187], v4 offset:55296\n"
        "v_exp_f32 v46, v46\n"
        "v_exp_f32 v47, v47\n"
        "v_exp_f32 v48, v48\n"
        "v_exp_f32 v49, v49\n"
        "v_smfmac_f32_32x32x32_bf16 v[96:111], v[144:147], v[40:47], v20\n"
        "buffer_load_dword v21, v22, s[20:23], 0 offen sc1\n"
        "v_exp_f32 v50, v50\n"
        "v_exp_f32 v51, v51\n"
        "v_mfma_f32_32x32x16_bf16 v[32:47], v[136:139], v[128:131], v[64:79]\n"
        "buffer_load_dwordx4 v[228:231], v0, s[40:43], s62 offen offset:2048 sc1\n"
        "v_exp_f32 v52, v52\n"
        "v_exp_f32 v53, v53\n"
        "v_exp_f32 v54, v54\n"
        "s_waitcnt lgkmcnt(0)\n"
        "v_mfma_f32_32x32x16_bf16 v[148:163], v[188:191], v[180:183], v[148:163]\n"
        "ds_read_b128 v[180:183], v5 offset:49152\n"
        "v_exp_f32 v55, v55\n"
        "v_exp_f32 v56, v56\n"
        "v_exp_f32 v57, v57\n"
        "v_smfmac_f32_32x32x32_bf16 v[112:127], v[140:143], v[48:55], v20\n"
        "v_exp_f32 v58, v58\n"
        "v_exp_f32 v59, v59\n"
        "v_mfma_f32_32x32x16_bf16 v[164:179], v[188:191], v[184:187], v[164:179]\n"
        "ds_read_b128 v[184:187], v5 offset:55296\n"
        "v_exp_f32 v60, v60\n"
        "v_exp_f32 v61, v61\n"
        "v_exp_f32 v62, v62\n"
        "s_waitcnt lgkmcnt(1)\n"
        "v_mfma_f32_32x32x16_bf16 v[148:163], v[192:195], v[180:183], v[148:163]\n"
        "ds_read_b128 v[180:183], v6 offset:49152\n"
        "v_exp_f32 v63, v63\n"
        "v_exp_f32 v32, v32\n"
        "v_exp_f32 v33, v33\n"
        "v_smfmac_f32_32x32x32_bf16 v[112:127], v[144:147], v[56:63], v20\n"
        "s_mov_b64 exec, s[60:61]\n"
        "ds_read_b128 v[144:147], v9 offset:36352\n"
        "ds_read_b128 v[140:143], v9 offset:45056\n"
        "s_mov_b64 exec, -1\n"
        "ds_read_b32 v10, v14\n"
        "ds_read_b32 v24, v25\n"
        "v_exp_f32 v34, v34\n"
        "v_exp_f32 v35, v35\n"
        "v_exp_f32 v36, v36\n"
        "v_mfma_f32_32x32x16_bf16 v[48:63], v[136:139], v[132:135], v[80:95]\n"
        "ds_read_b128 v[136:139], v8 offset:38912\n"
        "buffer_load_dwordx4 v[232:235], v0, s[40:43], s62 offen offset:3072 sc1\n"
        "v_exp_f32 v37, v37\n"
        "v_exp_f32 v38, v38\n"
        "v_exp_f32 v39, v39\n"
        "s_waitcnt lgkmcnt(1)\n"
        "v_mfma_f32_32x32x16_bf16 v[164:179], v[192:195], v[184:187], v[164:179]\n"
        "ds_read_b128 v[184:187], v6 offset:55296\n"
        "s_mov_b32 s62, 61440\n"
        "buffer_load_dwordx4 v[236:239], v0, s[40:43], s62 offen offset:0 sc1\n"
        "v_exp_f32 v40, v40\n"
        "v_exp_f32 v41, v41\n"
        "v_exp_f32 v42, v42\n"
        "v_cmp_gt_u32 vcc, 52, v10\n"
        "v_smfmac_f32_32x32x32_bf16 v[96:111], v[252:255], v[32:39], v20\n"
        "s_cbranch_vccnz 158f\n"
        "159:\n"
        "v_cmp_gt_u32 vcc, 52, v24\n"
        "v_exp_f32 v43, v43\n"
        "v_exp_f32 v44, v44\n"
        "v_exp_f32 v45, v45\n"
        "v_mfma_f32_32x32x16_bf16 v[148:163], v[196:199], v[180:183], v[148:163]\n"
        "ds_read_b128 v[180:183], v7 offset:49152\n"
        "s_setprio 3\n"
        "s_cbranch_vccz 160f\n"
        "s_setprio 2\n"
        "160:\n"
        "v_exp_f32 v46, v46\n"
        "v_exp_f32 v47, v47\n"
        "v_exp_f32 v48, v48\n"
        "v_exp_f32 v49, v49\n"
        "v_smfmac_f32_32x32x32_bf16 v[96:111], v[144:147], v[40:47], v20\n"
        "s_add_u32 m0, s56, 12288\n"
        "s_mov_b32 s63, 57344\n"
        "buffer_load_dwordx4 v15, s[20:23], s63 offen offset:2048 sc1 lds\n"
        "v_exp_f32 v50, v50\n"
        "v_exp_f32 v51, v51\n"
        "s_waitcnt lgkmcnt(0)\n"
        "v_mfma_f32_32x32x16_bf16 v[32:47], v[136:139], v[128:131], v[64:79]\n"
        "buffer_load_dwordx4 v[240:243], v0, s[40:43], s62 offen offset:1024 sc1\n"
        "v_exp_f32 v52, v52\n"
        "v_exp_f32 v53, v53\n"
        "v_exp_f32 v54, v54\n"
        "v_mfma_f32_32x32x16_bf16 v[164:179], v[196:199], v[184:187], v[164:179]\n"
        "ds_read_b128 v[184:187], v7 offset:55296\n"
        "buffer_load_dwordx4 v[244:247], v0, s[40:43], s62 offen offset:2048 sc1\n"
        "v_exp_f32 v55, v55\n"
        "v_exp_f32 v56, v56\n"
        "v_exp_f32 v57, v57\n"
        "v_smfmac_f32_32x32x32_bf16 v[112:127], v[252:255], v[48:55], v20\n"
        "v_exp_f32 v58, v58\n"
        "v_exp_f32 v59, v59\n"
        "v_mfma_f32_32x32x16_bf16 v[148:163], v[200:203], v[180:183], v[148:163]\n"
        "ds_read_b128 v[180:183], v4 offset:0\n"
        "v_exp_f32 v60, v60\n"
        "v_exp_f32 v61, v61\n"
        "v_exp_f32 v62, v62\n"
        "s_waitcnt lgkmcnt(1)\n"
        "v_mfma_f32_32x32x16_bf16 v[164:179], v[200:203], v[184:187], v[164:179]\n"
        "ds_read_b128 v[184:187], v4 offset:6144\n"
        "v_exp_f32 v63, v63\n"
        "v_exp_f32 v32, v32\n"
        "v_exp_f32 v33, v33\n"
        "v_smfmac_f32_32x32x32_bf16 v[112:127], v[144:147], v[56:63], v20\n"
        "s_mov_b64 exec, s[60:61]\n"
        "ds_read_b128 v[144:147], v9 offset:45568\n"
        "ds_read_b128 v[252:255], v9 offset:48128\n"
        "s_mov_b64 exec, -1\n"
        "v_exp_f32 v34, v34\n"
        "v_exp_f32 v35, v35\n"
        "v_exp_f32 v36, v36\n"
        "v_mfma_f32_32x32x16_bf16 v[48:63], v[136:139], v[132:135], v[80:95]\n"
        "ds_read_b128 v[136:139], v8 offset:41984\n"
        "buffer_load_dwordx4 v[248:251], v0, s[40:43], s62 offen offset:3072 sc1\n"
        "v_exp_f32 v37, v37\n"
        "v_exp_f32 v38, v38\n"
        "v_exp_f32 v39, v39\n"
        "s_waitcnt vmcnt(16) lgkmcnt(1)\n"
        "v_mfma_f32_32x32x16_bf16 v[148:163], v[204:207], v[180:183], v[148:163]\n"
        "ds_read_b128 v[180:183], v5 offset:0\n"
        "v_exp_f32 v40, v40\n"
        "v_exp_f32 v41, v41\n"
        "v_exp_f32 v42, v42\n"
        "s_waitcnt vmcnt(7) lgkmcnt(1)\n"
        "v_smfmac_f32_32x32x32_bf16 v[96:111], v[140:143], v[32:39], v20\n"
        "ds_add_u32 v14, v11\n"
        "v_exp_f32 v43, v43\n"
        "v_exp_f32 v44, v44\n"
        "v_exp_f32 v45, v45\n"
        "v_mfma_f32_32x32x16_bf16 v[164:179], v[204:207], v[184:187], v[164:179]\n"
        "ds_read_b128 v[184:187], v5 offset:6144\n"
        "v_exp_f32 v46, v46\n"
        "v_exp_f32 v47, v47\n"
        "v_exp_f32 v48, v48\n"
        "v_exp_f32 v49, v49\n"
        "v_smfmac_f32_32x32x32_bf16 v[96:111], v[144:147], v[40:47], v20\n"
        "ds_read_b32 v10, v14\n"
        "ds_read_b32 v24, v25\n"
        "v_exp_f32 v50, v50\n"
        "v_exp_f32 v51, v51\n"
        "v_mfma_f32_32x32x16_bf16 v[32:47], v[136:139], v[128:131], v[64:79]\n"
        "v_exp_f32 v52, v52\n"
        "v_exp_f32 v53, v53\n"
        "v_exp_f32 v54, v54\n"
        "s_waitcnt lgkmcnt(2)\n"
        "v_mfma_f32_32x32x16_bf16 v[148:163], v[208:211], v[180:183], v[148:163]\n"
        "ds_read_b128 v[180:183], v6 offset:0\n"
        "v_exp_f32 v55, v55\n"
        "v_exp_f32 v56, v56\n"
        "v_exp_f32 v57, v57\n"
        "s_waitcnt lgkmcnt(1)\n"
        "v_cmp_gt_u32 vcc, 56, v10\n"
        "v_smfmac_f32_32x32x32_bf16 v[112:127], v[140:143], v[48:55], v20\n"
        "s_cbranch_vccnz 161f\n"
        "162:\n"
        "v_cmp_gt_u32 vcc, 56, v24\n"
        "v_exp_f32 v58, v58\n"
        "v_exp_f32 v59, v59\n"
        "v_mfma_f32_32x32x16_bf16 v[164:179], v[208:211], v[184:187], v[164:179]\n"
        "ds_read_b128 v[184:187], v6 offset:6144\n"
        "s_add_u32 m0, s56, 24576\n"
        "s_mov_b32 s63, 61440\n"
        "buffer_load_dwordx4 v15, s[20:23], s63 offen offset:2048 sc1 lds\n"
        "s_setprio 3\n"
        "s_cbranch_vccz 163f\n"
        "s_setprio 2\n"
        "163:\n"
        "v_exp_f32 v60, v60\n"
        "v_exp_f32 v61, v61\n"
        "v_exp_f32 v62, v62\n"
        "s_waitcnt lgkmcnt(0)\n"
        "v_mfma_f32_32x32x16_bf16 v[148:163], v[212:215], v[180:183], v[148:163]\n"
        "ds_read_b128 v[180:183], v7 offset:0\n"
        "v_exp_f32 v63, v63\n"
        "v_exp_f32 v32, v32\n"
        "v_exp_f32 v33, v33\n"
        "v_smfmac_f32_32x32x32_bf16 v[112:127], v[144:147], v[56:63], v20\n"
        "s_mov_b64 exec, s[60:61]\n"
        "ds_read_b128 v[144:147], v9 offset:48640\n"
        "ds_read_b128 v[140:143], v9 offset:57344\n"
        "s_mov_b64 exec, -1\n"
        "s_lshl3_add_u32 s64, s5, s48\n"
        "v_exp_f32 v34, v34\n"
        "v_exp_f32 v35, v35\n"
        "v_exp_f32 v36, v36\n"
        "v_mfma_f32_32x32x16_bf16 v[48:63], v[136:139], v[132:135], v[80:95]\n"
        "ds_read_b128 v[136:139], v8 offset:51200\n"
        "s_lshl_b32 s64, s64, 13\n"
        "v_exp_f32 v37, v37\n"
        "v_exp_f32 v38, v38\n"
        "v_exp_f32 v39, v39\n"
        "v_mfma_f32_32x32x16_bf16 v[164:179], v[212:215], v[184:187], v[164:179]\n"
        "ds_read_b128 v[184:187], v7 offset:6144\n"
        "s_lshr_b32 s65, s50, 2\n"
        "v_exp_f32 v40, v40\n"
        "v_exp_f32 v41, v41\n"
        "v_exp_f32 v42, v42\n"
        "v_cmp_ne_u32 vcc, v21, v3\n"
        "v_smfmac_f32_32x32x32_bf16 v[96:111], v[252:255], v[32:39], v20\n"
        "s_cbranch_vccnz 164f\n"
        "165:\n"
        "s_lshl_b32 s65, s65, 7\n"
        "v_exp_f32 v43, v43\n"
        "v_exp_f32 v44, v44\n"
        "v_exp_f32 v45, v45\n"
        "s_waitcnt lgkmcnt(0)\n"
        "v_mfma_f32_32x32x16_bf16 v[148:163], v[216:219], v[180:183], v[148:163]\n"
        "ds_read_b128 v[180:183], v4 offset:12288\n"
        "s_add_u32 s64, s64, s65\n"
        "v_exp_f32 v46, v46\n"
        "v_exp_f32 v47, v47\n"
        "v_exp_f32 v48, v48\n"
        "v_exp_f32 v49, v49\n"
        "v_smfmac_f32_32x32x32_bf16 v[96:111], v[144:147], v[40:47], v20\n"
        "buffer_load_dwordx4 v[188:191], v2, s[40:43], s4 offen offset:0 sc1\n"
        "v_and_b32 v31, 7, v1\n"
        "v_exp_f32 v50, v50\n"
        "v_exp_f32 v51, v51\n"
        "v_mfma_f32_32x32x16_bf16 v[32:47], v[136:139], v[128:131], v[64:79]\n"
        "buffer_load_dwordx4 v[192:195], v2, s[40:43], s4 offen offset:1024 sc1\n"
        "v_lshl_add_u32 v30, v31, 4, s64\n"
        "v_exp_f32 v52, v52\n"
        "v_exp_f32 v53, v53\n"
        "v_exp_f32 v54, v54\n"
        "v_mfma_f32_32x32x16_bf16 v[164:179], v[216:219], v[184:187], v[164:179]\n"
        "ds_read_b128 v[184:187], v4 offset:18432\n"
        "s_add_u32 s69, s44, 0\n"
        "buffer_load_dwordx4 v[196:199], v2, s[20:23], s69 offen offset:0 sc1\n"
        "v_lshrrev_b32 v16, 3, v1\n"
        "v_exp_f32 v55, v55\n"
        "v_exp_f32 v56, v56\n"
        "v_exp_f32 v57, v57\n"
        "s_waitcnt vmcnt(8) lgkmcnt(1)\n"
        "v_smfmac_f32_32x32x32_bf16 v[112:127], v[252:255], v[48:55], v20\n"
        "ds_add_u32 v14, v11\n"
        "v_lshl_add_u32 v30, v16, 10, v30\n"
        "v_exp_f32 v58, v58\n"
        "v_exp_f32 v59, v59\n"
        "v_mfma_f32_32x32x16_bf16 v[148:163], v[220:223], v[180:183], v[148:163]\n"
        "ds_read_b128 v[180:183], v5 offset:12288\n"
        "v_exp_f32 v60, v60\n"
        "v_exp_f32 v61, v61\n"
        "v_exp_f32 v62, v62\n"
        "s_waitcnt lgkmcnt(2)\n"
        "v_mfma_f32_32x32x16_bf16 v[164:179], v[220:223], v[184:187], v[164:179]\n"
        "ds_read_b128 v[184:187], v5 offset:18432\n"
        "v_exp_f32 v63, v63\n"
        "v_exp_f32 v32, v32\n"
        "v_exp_f32 v33, v33\n"
        "v_smfmac_f32_32x32x32_bf16 v[112:127], v[144:147], v[56:63], v20\n"
        "s_mov_b64 exec, s[60:61]\n"
        "ds_read_b128 v[144:147], v9 offset:57856\n"
        "ds_read_b128 v[252:255], v9 offset:60416\n"
        "s_mov_b64 exec, -1\n"
        "ds_read_b32 v10, v14\n"
        "ds_read_b32 v24, v25\n"
        "v_exp_f32 v34, v34\n"
        "v_exp_f32 v35, v35\n"
        "v_exp_f32 v36, v36\n"
        "v_mfma_f32_32x32x16_bf16 v[48:63], v[136:139], v[132:135], v[80:95]\n"
        "ds_read_b128 v[136:139], v8 offset:54272\n"
        "buffer_load_dwordx4 v[16:19], v30, s[8:11], 0 offen\n"
        "v_exp_f32 v37, v37\n"
        "v_exp_f32 v38, v38\n"
        "v_exp_f32 v39, v39\n"
        "s_waitcnt lgkmcnt(1)\n"
        "v_mfma_f32_32x32x16_bf16 v[148:163], v[224:227], v[180:183], v[148:163]\n"
        "ds_read_b128 v[180:183], v6 offset:12288\n"
        "buffer_load_dwordx4 v[200:203], v2, s[20:23], s69 offen offset:1024 sc1\n"
        "v_exp_f32 v40, v40\n"
        "v_exp_f32 v41, v41\n"
        "v_exp_f32 v42, v42\n"
        "v_cmp_gt_u32 vcc, 60, v10\n"
        "v_smfmac_f32_32x32x32_bf16 v[96:111], v[140:143], v[32:39], v20\n"
        "s_cbranch_vccnz 166f\n"
        "167:\n"
        "v_cmp_gt_u32 vcc, 60, v24\n"
        "v_exp_f32 v43, v43\n"
        "v_exp_f32 v44, v44\n"
        "v_exp_f32 v45, v45\n"
        "v_mfma_f32_32x32x16_bf16 v[164:179], v[224:227], v[184:187], v[164:179]\n"
        "ds_read_b128 v[184:187], v6 offset:18432\n"
        "buffer_load_dwordx4 v[204:207], v2, s[20:23], s69 offen offset:2048 sc1\n"
        "s_setprio 3\n"
        "s_cbranch_vccz 168f\n"
        "s_setprio 2\n"
        "168:\n"
        "v_exp_f32 v46, v46\n"
        "v_exp_f32 v47, v47\n"
        "v_exp_f32 v48, v48\n"
        "v_exp_f32 v49, v49\n"
        "v_smfmac_f32_32x32x32_bf16 v[96:111], v[144:147], v[40:47], v20\n"
        "buffer_load_dwordx4 v[208:211], v2, s[20:23], s69 offen offset:3072 sc1\n"
        "v_exp_f32 v50, v50\n"
        "v_exp_f32 v51, v51\n"
        "s_waitcnt lgkmcnt(0)\n"
        "v_mfma_f32_32x32x16_bf16 v[32:47], v[136:139], v[128:131], v[64:79]\n"
        "s_add_u32 s69, s44, 4096\n"
        "buffer_load_dwordx4 v[212:215], v2, s[20:23], s69 offen offset:0 sc1\n"
        "v_exp_f32 v52, v52\n"
        "v_exp_f32 v53, v53\n"
        "v_exp_f32 v54, v54\n"
        "v_mfma_f32_32x32x16_bf16 v[148:163], v[228:231], v[180:183], v[148:163]\n"
        "ds_read_b128 v[180:183], v7 offset:12288\n"
        "v_exp_f32 v55, v55\n"
        "v_exp_f32 v56, v56\n"
        "v_exp_f32 v57, v57\n"
        "v_smfmac_f32_32x32x32_bf16 v[112:127], v[140:143], v[48:55], v20\n"
        "v_exp_f32 v58, v58\n"
        "v_exp_f32 v59, v59\n"
        "v_mfma_f32_32x32x16_bf16 v[164:179], v[228:231], v[184:187], v[164:179]\n"
        "ds_read_b128 v[184:187], v7 offset:18432\n"
        "v_exp_f32 v60, v60\n"
        "v_exp_f32 v61, v61\n"
        "v_exp_f32 v62, v62\n"
        "s_waitcnt lgkmcnt(1)\n"
        "v_mfma_f32_32x32x16_bf16 v[148:163], v[232:235], v[180:183], v[148:163]\n"
        "ds_read_b128 v[180:183], v4 offset:24576\n"
        "v_exp_f32 v63, v63\n"
        "v_exp_f32 v32, v32\n"
        "v_exp_f32 v33, v33\n"
        "v_smfmac_f32_32x32x32_bf16 v[112:127], v[144:147], v[56:63], v20\n"
        "s_mov_b64 exec, s[60:61]\n"
        "ds_read_b128 v[144:147], v9 offset:60928\n"
        "ds_read_b128 v[140:143], v9 offset:8192\n"
        "s_mov_b64 exec, -1\n"
        "v_exp_f32 v34, v34\n"
        "v_exp_f32 v35, v35\n"
        "v_exp_f32 v36, v36\n"
        "v_mfma_f32_32x32x16_bf16 v[48:63], v[136:139], v[132:135], v[80:95]\n"
        "ds_read_b128 v[136:139], v8 offset:2048\n"
        "buffer_load_dwordx4 v[216:219], v2, s[20:23], s69 offen offset:1024 sc1\n"
        "v_exp_f32 v37, v37\n"
        "v_exp_f32 v38, v38\n"
        "v_exp_f32 v39, v39\n"
        "s_waitcnt lgkmcnt(1)\n"
        "v_mfma_f32_32x32x16_bf16 v[164:179], v[232:235], v[184:187], v[164:179]\n"
        "ds_read_b128 v[184:187], v4 offset:30720\n"
        "buffer_load_dwordx4 v[220:223], v2, s[20:23], s69 offen offset:2048 sc1\n"
        "v_exp_f32 v40, v40\n"
        "v_exp_f32 v41, v41\n"
        "v_exp_f32 v42, v42\n"
        "s_waitcnt vmcnt(11) lgkmcnt(1)\n"
        "v_smfmac_f32_32x32x32_bf16 v[96:111], v[252:255], v[32:39], v20\n"
        "ds_add_u32 v14, v11\n"
        "buffer_load_dwordx4 v[224:227], v2, s[20:23], s69 offen offset:3072 sc1\n"
        "v_exp_f32 v43, v43\n"
        "v_exp_f32 v44, v44\n"
        "v_exp_f32 v45, v45\n"
        "v_mfma_f32_32x32x16_bf16 v[148:163], v[236:239], v[180:183], v[148:163]\n"
        "ds_read_b128 v[180:183], v5 offset:24576\n"
        "v_exp_f32 v46, v46\n"
        "v_exp_f32 v47, v47\n"
        "v_exp_f32 v48, v48\n"
        "v_exp_f32 v49, v49\n"
        "v_smfmac_f32_32x32x32_bf16 v[96:111], v[144:147], v[40:47], v20\n"
        "s_add_u32 s69, s44, 8192\n"
        "buffer_load_dwordx4 v[228:231], v2, s[20:23], s69 offen offset:0 sc1\n"
        "v_exp_f32 v50, v50\n"
        "v_exp_f32 v51, v51\n"
        "v_mfma_f32_32x32x16_bf16 v[32:47], v[136:139], v[128:131], v[64:79]\n"
        "buffer_load_dwordx4 v[232:235], v2, s[20:23], s69 offen offset:1024 sc1\n"
        "v_exp_f32 v52, v52\n"
        "v_exp_f32 v53, v53\n"
        "v_exp_f32 v54, v54\n"
        "s_waitcnt lgkmcnt(0)\n"
        "v_mfma_f32_32x32x16_bf16 v[164:179], v[236:239], v[184:187], v[164:179]\n"
        "ds_read_b128 v[184:187], v5 offset:30720\n"
        "v_exp_f32 v55, v55\n"
        "v_exp_f32 v56, v56\n"
        "v_exp_f32 v57, v57\n"
        "v_smfmac_f32_32x32x32_bf16 v[112:127], v[252:255], v[48:55], v20\n"
        "v_exp_f32 v58, v58\n"
        "v_exp_f32 v59, v59\n"
        "v_mfma_f32_32x32x16_bf16 v[148:163], v[240:243], v[180:183], v[148:163]\n"
        "ds_read_b128 v[180:183], v6 offset:24576\n"
        "v_exp_f32 v60, v60\n"
        "v_exp_f32 v61, v61\n"
        "v_exp_f32 v62, v62\n"
        "s_waitcnt lgkmcnt(1)\n"
        "v_mfma_f32_32x32x16_bf16 v[164:179], v[240:243], v[184:187], v[164:179]\n"
        "ds_read_b128 v[184:187], v6 offset:30720\n"
        "v_exp_f32 v63, v63\n"
        "v_exp_f32 v32, v32\n"
        "v_exp_f32 v33, v33\n"
        "v_smfmac_f32_32x32x32_bf16 v[112:127], v[144:147], v[56:63], v20\n"
        "s_mov_b64 exec, s[60:61]\n"
        "ds_read_b128 v[144:147], v9 offset:8704\n"
        "ds_read_b128 v[252:255], v9 offset:11264\n"
        "s_mov_b64 exec, -1\n"
        "ds_read_b32 v10, v14\n"
        "ds_read_b32 v24, v25\n"
        "v_exp_f32 v34, v34\n"
        "v_exp_f32 v35, v35\n"
        "v_exp_f32 v36, v36\n"
        "v_mfma_f32_32x32x16_bf16 v[48:63], v[136:139], v[132:135], v[80:95]\n"
        "ds_read_b128 v[136:139], v8 offset:5120\n"
        "buffer_load_dwordx4 v[236:239], v2, s[20:23], s69 offen offset:2048 sc1\n"
        "v_exp_f32 v37, v37\n"
        "v_exp_f32 v38, v38\n"
        "v_exp_f32 v39, v39\n"
        "s_waitcnt lgkmcnt(1)\n"
        "v_mfma_f32_32x32x16_bf16 v[148:163], v[244:247], v[180:183], v[148:163]\n"
        "ds_read_b128 v[180:183], v7 offset:24576\n"
        "buffer_load_dwordx4 v[240:243], v2, s[20:23], s69 offen offset:3072 sc1\n"
        "v_exp_f32 v40, v40\n"
        "v_exp_f32 v41, v41\n"
        "v_exp_f32 v42, v42\n"
        "v_cmp_gt_u32 vcc, 64, v10\n"
        "v_smfmac_f32_32x32x32_bf16 v[96:111], v[140:143], v[32:39], v20\n"
        "s_cbranch_vccnz 169f\n"
        "170:\n"
        "v_cmp_gt_u32 vcc, 64, v24\n"
        "v_exp_f32 v43, v43\n"
        "v_exp_f32 v44, v44\n"
        "v_exp_f32 v45, v45\n"
        "v_mfma_f32_32x32x16_bf16 v[164:179], v[244:247], v[184:187], v[164:179]\n"
        "ds_read_b128 v[184:187], v7 offset:30720\n"
        "s_setprio 3\n"
        "s_cbranch_vccz 171f\n"
        "s_setprio 2\n"
        "171:\n"
        "v_exp_f32 v46, v46\n"
        "v_exp_f32 v47, v47\n"
        "v_exp_f32 v48, v48\n"
        "v_exp_f32 v49, v49\n"
        "v_smfmac_f32_32x32x32_bf16 v[96:111], v[144:147], v[40:47], v20\n"
        "s_add_u32 s69, s44, 12288\n"
        "buffer_load_dwordx4 v[244:247], v2, s[20:23], s69 offen offset:0 sc1\n"
        "v_exp_f32 v50, v50\n"
        "v_exp_f32 v51, v51\n"
        "v_exp_f32 v52, v52\n"
        "s_waitcnt lgkmcnt(0)\n"
        "v_mfma_f32_32x32x16_bf16 v[32:47], v[136:139], v[128:131], v[64:79]\n"
        "v_exp_f32 v53, v53\n"
        "v_exp_f32 v54, v54\n"
        "v_exp_f32 v55, v55\n"
        "v_mfma_f32_32x32x16_bf16 v[148:163], v[248:251], v[180:183], v[148:163]\n"
        "v_exp_f32 v56, v56\n"
        "v_exp_f32 v57, v57\n"
        "v_exp_f32 v58, v58\n"
        "v_smfmac_f32_32x32x32_bf16 v[112:127], v[140:143], v[48:55], v20\n"
        "v_exp_f32 v59, v59\n"
        "v_exp_f32 v60, v60\n"
        "v_exp_f32 v61, v61\n"
        "v_mfma_f32_32x32x16_bf16 v[164:179], v[248:251], v[184:187], v[164:179]\n"
        "v_exp_f32 v62, v62\n"
        "v_exp_f32 v63, v63\n"
        "v_exp_f32 v32, v32\n"
        "v_exp_f32 v33, v33\n"
        "v_smfmac_f32_32x32x32_bf16 v[112:127], v[144:147], v[56:63], v20\n"
        "s_mov_b64 exec, s[60:61]\n"
        "ds_read_b128 v[144:147], v9 offset:11776\n"
        "ds_read_b128 v[140:143], v9 offset:20480\n"
        "s_mov_b64 exec, -1\n"
        "s_nop 2\n"
        "ds_write_b128 v28, v[164:167] offset:36864\n"
        "v_exp_f32 v34, v34\n"
        "v_exp_f32 v35, v35\n"
        "v_exp_f32 v36, v36\n"
        "v_exp_f32 v37, v37\n"
        "v_mfma_f32_32x32x16_bf16 v[48:63], v[136:139], v[132:135], v[80:95]\n"
        "ds_read_b128 v[136:139], v8 offset:14336\n"
        "buffer_load_dwordx4 v[248:251], v2, s[20:23], s69 offen offset:1024 sc1\n"
        "ds_write_b128 v28, v[168:171] offset:37888\n"
        "ds_write_b128 v28, v[172:175] offset:49152\n"
        "v_exp_f32 v38, v38\n"
        "v_exp_f32 v39, v39\n"
        "v_exp_f32 v40, v40\n"
        "v_exp_f32 v41, v41\n"
        "v_exp_f32 v42, v42\n"
        "v_exp_f32 v43, v43\n"
        "v_smfmac_f32_32x32x32_bf16 v[96:111], v[252:255], v[32:39], v20\n"
        "buffer_load_dwordx4 v[180:183], v2, s[20:23], s69 offen offset:2048 sc1\n"
        "ds_write_b128 v28, v[176:179] offset:50176\n"
        "v_exp_f32 v44, v44\n"
        "v_exp_f32 v45, v45\n"
        "v_exp_f32 v46, v46\n"
        "v_exp_f32 v47, v47\n"
        "v_exp_f32 v48, v48\n"
        "v_exp_f32 v49, v49\n"
        "s_waitcnt lgkmcnt(3)\n"
        "v_smfmac_f32_32x32x32_bf16 v[96:111], v[144:147], v[40:47], v20\n"
        "buffer_load_dwordx4 v[184:187], v2, s[20:23], s69 offen offset:3072 sc1\n"
        "s_lshl1_add_u32 s64, s6, s52\n"
        "s_lshl_b32 s64, s64, 7\n"
        "s_add_u32 s64, s64, 983040\n"
        "v_exp_f32 v50, v50\n"
        "v_exp_f32 v51, v51\n"
        "v_exp_f32 v52, v52\n"
        "v_exp_f32 v53, v53\n"
        "v_mfma_f32_32x32x16_bf16 v[32:47], v[136:139], v[128:131], v[64:79]\n"
        "s_cmp_eq_u32 s51, 0\n"
        "s_cselect_b32 s64, s64, 2147483632\n"
        "v_writelane_b32 v27, s64, 0\n"
        "s_mul_i32 s64, s48, 7\n"
        "v_exp_f32 v54, v54\n"
        "v_exp_f32 v55, v55\n"
        "v_exp_f32 v56, v56\n"
        "v_exp_f32 v57, v57\n"
        "v_exp_f32 v58, v58\n"
        "v_exp_f32 v59, v59\n"
        "s_waitcnt vmcnt(19) lgkmcnt(0)\n"
        "v_smfmac_f32_32x32x32_bf16 v[112:127], v[252:255], v[48:55], v20\n"
        "ds_add_u32 v14, v11\n"
        "s_and_b32 s65, s6, 31\n"
        "s_lshl_b32 s65, s65, 8\n"
        "v_exp_f32 v60, v60\n"
        "v_exp_f32 v61, v61\n"
        "v_exp_f32 v62, v62\n"
        "v_exp_f32 v63, v63\n"
        "v_exp_f32 v32, v32\n"
        "v_exp_f32 v33, v33\n"
        "v_smfmac_f32_32x32x32_bf16 v[112:127], v[144:147], v[56:63], v20\n"
        "s_mov_b64 exec, s[60:61]\n"
        "ds_read_b128 v[144:147], v9 offset:20992\n"
        "ds_read_b128 v[252:255], v9 offset:23552\n"
        "s_mov_b64 exec, -1\n"
        "ds_read_b32 v10, v14\n"
        "ds_read_b32 v24, v25\n"
        "s_add_u32 s65, s65, 983040\n"
        "v_bfe_u32 v6, v1, 1, 1\n"
        "v_lshl_add_u32 v5, v6, 7, s65\n"
        "v_exp_f32 v34, v34\n"
        "v_exp_f32 v35, v35\n"
        "v_exp_f32 v36, v36\n"
        "v_exp_f32 v37, v37\n"
        "v_mfma_f32_32x32x16_bf16 v[48:63], v[136:139], v[132:135], v[80:95]\n"
        "ds_read_b128 v[136:139], v8 offset:17408\n"
        "v_and_b32 v7, 1, v1\n"
        "v_mul_u32_u24 v7, 6, v7\n"
        "v_add_u32 v7, s64, v7\n"
        "v_exp_f32 v38, v38\n"
        "v_exp_f32 v39, v39\n"
        "v_exp_f32 v40, v40\n"
        "v_exp_f32 v41, v41\n"
        "v_exp_f32 v42, v42\n"
        "v_exp_f32 v43, v43\n"
        "s_waitcnt lgkmcnt(0)\n"
        "v_min_u32 v10, v10, v24\n"
        "v_cmp_gt_u32 vcc, 68, v10\n"
        "v_smfmac_f32_32x32x32_bf16 v[96:111], v[140:143], v[32:39], v20\n"
        "s_cbranch_vccnz 172f\n"
        "173:\n"
        "v_cmp_gt_u32 vcc, 68, v24\n"
        "s_setprio 3\n"
        "s_cbranch_vccz 174f\n"
        "s_setprio 2\n"
        "174:\n"
        "v_lshrrev_b32 v7, 3, v7\n"
        "v_lshl_add_u32 v5, v7, 13, v5\n"
        "v_exp_f32 v44, v44\n"
        "v_exp_f32 v45, v45\n"
        "v_exp_f32 v46, v46\n"
        "v_exp_f32 v47, v47\n"
        "v_exp_f32 v48, v48\n"
        "v_exp_f32 v49, v49\n"
        "v_smfmac_f32_32x32x32_bf16 v[96:111], v[144:147], v[40:47], v20\n"
        "ds_read_b128 v[164:167], v29 offset:36864\n"
        "ds_read_b128 v[168:171], v29 offset:37888\n"
        "ds_read_b128 v[172:175], v29 offset:49152\n"
        "ds_read_b128 v[176:179], v29 offset:50176\n"
        "s_waitcnt vmcnt(17)\n"
        "s_waitcnt lgkmcnt(3)\n"
        "v_add_f32 v148, v148, v164\n"
        "v_add_f32 v149, v149, v165\n"
        "v_add_f32 v150, v150, v166\n"
        "v_add_f32 v151, v151, v167\n"
        "s_waitcnt lgkmcnt(2)\n"
        "v_add_f32 v152, v152, v168\n"
        "v_add_f32 v153, v153, v169\n"
        "v_add_f32 v154, v154, v170\n"
        "v_add_f32 v155, v155, v171\n"
        "v_lshlrev_b32 v164, 16, v188\n"
        "v_and_b32 v165, 0xffff0000, v188\n"
        "v_lshlrev_b32 v166, 16, v189\n"
        "v_and_b32 v167, 0xffff0000, v189\n"
        "v_lshlrev_b32 v168, 16, v190\n"
        "v_and_b32 v169, 0xffff0000, v190\n"
        "v_lshlrev_b32 v170, 16, v191\n"
        "v_and_b32 v171, 0xffff0000, v191\n"
        "v_exp_f32 v50, v50\n"
        "v_exp_f32 v51, v51\n"
        "v_exp_f32 v52, v52\n"
        "v_exp_f32 v53, v53\n"
        "v_mfma_f32_32x32x16_bf16 v[32:47], v[136:139], v[128:131], v[64:79]\n"
        "v_mul_f32 v148, v148, v164\n"
        "v_mul_f32 v149, v149, v165\n"
        "v_mul_f32 v150, v150, v166\n"
        "v_mul_f32 v151, v151, v167\n"
        "v_mul_f32 v152, v152, v168\n"
        "v_mul_f32 v153, v153, v169\n"
        "v_mul_f32 v154, v154, v170\n"
        "v_mul_f32 v155, v155, v171\n"
        "v_cvt_pk_bf16_f32 v148, v148, v149\n"
        "v_cvt_pk_bf16_f32 v149, v150, v151\n"
        "v_cvt_pk_bf16_f32 v150, v152, v153\n"
        "v_cvt_pk_bf16_f32 v151, v154, v155\n"
        "buffer_store_dwordx4 v[148:151], v2, s[40:43], s4 offen offset:0 sc1\n"
        "s_waitcnt lgkmcnt(0)\n"
        "v_add_f32 v156, v156, v172\n"
        "v_add_f32 v157, v157, v173\n"
        "v_add_f32 v158, v158, v174\n"
        "v_add_f32 v159, v159, v175\n"
        "v_add_f32 v160, v160, v176\n"
        "v_add_f32 v161, v161, v177\n"
        "v_add_f32 v162, v162, v178\n"
        "v_add_f32 v163, v163, v179\n"
        "v_exp_f32 v54, v54\n"
        "v_exp_f32 v55, v55\n"
        "v_exp_f32 v56, v56\n"
        "v_exp_f32 v57, v57\n"
        "v_exp_f32 v58, v58\n"
        "v_exp_f32 v59, v59\n"
        "v_smfmac_f32_32x32x32_bf16 v[112:127], v[140:143], v[48:55], v20\n"
        "v_lshlrev_b32 v172, 16, v192\n"
        "v_and_b32 v173, 0xffff0000, v192\n"
        "v_lshlrev_b32 v174, 16, v193\n"
        "v_and_b32 v175, 0xffff0000, v193\n"
        "v_lshlrev_b32 v176, 16, v194\n"
        "v_and_b32 v177, 0xffff0000, v194\n"
        "v_lshlrev_b32 v178, 16, v195\n"
        "v_and_b32 v179, 0xffff0000, v195\n"
        "v_mul_f32 v156, v156, v172\n"
        "v_mul_f32 v157, v157, v173\n"
        "v_mul_f32 v158, v158, v174\n"
        "v_mul_f32 v159, v159, v175\n"
        "v_mul_f32 v160, v160, v176\n"
        "v_mul_f32 v161, v161, v177\n"
        "v_mul_f32 v162, v162, v178\n"
        "v_mul_f32 v163, v163, v179\n"
        "v_cvt_pk_bf16_f32 v156, v156, v157\n"
        "v_cvt_pk_bf16_f32 v157, v158, v159\n"
        "v_cvt_pk_bf16_f32 v158, v160, v161\n"
        "v_cvt_pk_bf16_f32 v159, v162, v163\n"
        "buffer_store_dwordx4 v[156:159], v2, s[40:43], s4 offen offset:1024 sc1\n"
        "v_exp_f32 v60, v60\n"
        "v_exp_f32 v61, v61\n"
        "v_exp_f32 v62, v62\n"
        "v_exp_f32 v63, v63\n"
        "s_setprio 1\n"
        "v_exp_f32 v32, v32\n"
        "v_exp_f32 v33, v33\n"
        "v_smfmac_f32_32x32x32_bf16 v[112:127], v[144:147], v[56:63], v20\n"
        "s_mov_b64 exec, s[60:61]\n"
        "ds_read_b128 v[144:147], v9 offset:24064\n"
        "ds_read_b128 v[140:143], v9 offset:32768\n"
        "s_mov_b64 exec, -1\n"
        "v_exp_f32 v34, v34\n"
        "v_exp_f32 v35, v35\n"
        "v_exp_f32 v36, v36\n"
        "v_exp_f32 v37, v37\n"
        "v_mfma_f32_32x32x16_bf16 v[48:63], v[136:139], v[132:135], v[80:95]\n"
        "ds_read_b128 v[136:139], v8 offset:26624\n"
        "s_add_u32 s69, s44, 16384\n"
        "buffer_load_dwordx4 v[164:167], v2, s[20:23], s69 offen offset:0 sc1\n"
        "v_exp_f32 v38, v38\n"
        "v_exp_f32 v39, v39\n"
        "v_exp_f32 v40, v40\n"
        "v_exp_f32 v41, v41\n"
        "v_exp_f32 v42, v42\n"
        "v_exp_f32 v43, v43\n"
        "v_smfmac_f32_32x32x32_bf16 v[96:111], v[252:255], v[32:39], v20\n"
        "buffer_load_dwordx4 v[168:171], v2, s[20:23], s69 offen offset:1024 sc1\n"
        "v_exp_f32 v44, v44\n"
        "v_exp_f32 v45, v45\n"
        "v_exp_f32 v46, v46\n"
        "v_exp_f32 v47, v47\n"
        "v_exp_f32 v48, v48\n"
        "v_exp_f32 v49, v49\n"
        "s_waitcnt lgkmcnt(0)\n"
        "v_smfmac_f32_32x32x32_bf16 v[96:111], v[144:147], v[40:47], v20\n"
        "buffer_load_dwordx4 v[172:175], v2, s[20:23], s69 offen offset:2048 sc1\n"
        "v_exp_f32 v50, v50\n"
        "v_exp_f32 v51, v51\n"
        "v_exp_f32 v52, v52\n"
        "v_exp_f32 v53, v53\n"
        "v_mfma_f32_32x32x16_bf16 v[32:47], v[136:139], v[128:131], v[64:79]\n"
        "buffer_load_dwordx4 v[176:179], v2, s[20:23], s69 offen offset:3072 sc1\n"
        "v_exp_f32 v54, v54\n"
        "v_exp_f32 v55, v55\n"
        "v_exp_f32 v56, v56\n"
        "v_exp_f32 v57, v57\n"
        "v_exp_f32 v58, v58\n"
        "v_exp_f32 v59, v59\n"
        "v_smfmac_f32_32x32x32_bf16 v[112:127], v[252:255], v[48:55], v20\n"
        "s_add_u32 s69, s44, 20480\n"
        "buffer_load_dwordx4 v[188:191], v2, s[20:23], s69 offen offset:0 sc1\n"
        "v_exp_f32 v60, v60\n"
        "v_exp_f32 v61, v61\n"
        "v_exp_f32 v62, v62\n"
        "v_exp_f32 v63, v63\n"
        "v_exp_f32 v32, v32\n"
        "v_exp_f32 v33, v33\n"
        "v_smfmac_f32_32x32x32_bf16 v[112:127], v[144:147], v[56:63], v20\n"
        "s_mov_b64 exec, s[60:61]\n"
        "ds_read_b128 v[144:147], v9 offset:33280\n"
        "ds_read_b128 v[252:255], v9 offset:35840\n"
        "s_mov_b64 exec, -1\n"
        "v_exp_f32 v34, v34\n"
        "v_exp_f32 v35, v35\n"
        "v_exp_f32 v36, v36\n"
        "v_exp_f32 v37, v37\n"
        "v_mfma_f32_32x32x16_bf16 v[48:63], v[136:139], v[132:135], v[80:95]\n"
        "ds_read_b128 v[136:139], v8 offset:29696\n"
        "buffer_load_dwordx4 v[192:195], v2, s[20:23], s69 offen offset:1024 sc1\n"
        "v_exp_f32 v38, v38\n"
        "v_exp_f32 v39, v39\n"
        "v_exp_f32 v40, v40\n"
        "v_exp_f32 v41, v41\n"
        "v_exp_f32 v42, v42\n"
        "v_exp_f32 v43, v43\n"
        "v_smfmac_f32_32x32x32_bf16 v[96:111], v[140:143], v[32:39], v20\n"
        "v_exp_f32 v44, v44\n"
        "v_exp_f32 v45, v45\n"
        "v_exp_f32 v46, v46\n"
        "v_exp_f32 v47, v47\n"
        "v_exp_f32 v48, v48\n"
        "v_exp_f32 v49, v49\n"
        "s_waitcnt lgkmcnt(0)\n"
        "v_smfmac_f32_32x32x32_bf16 v[96:111], v[144:147], v[40:47], v20\n"
        "v_exp_f32 v50, v50\n"
        "v_exp_f32 v51, v51\n"
        "v_exp_f32 v52, v52\n"
        "v_exp_f32 v53, v53\n"
        "v_mfma_f32_32x32x16_bf16 v[32:47], v[136:139], v[128:131], v[64:79]\n"
        "v_exp_f32 v54, v54\n"
        "v_exp_f32 v55, v55\n"
        "v_exp_f32 v56, v56\n"
        "v_exp_f32 v57, v57\n"
        "v_exp_f32 v58, v58\n"
        "v_exp_f32 v59, v59\n"
        "s_waitcnt vmcnt(6)\n"
        "v_smfmac_f32_32x32x32_bf16 v[112:127], v[140:143], v[48:55], v20\n"
        "ds_add_u32 v14, v11\n"
        "v_lshlrev_b32 v7, 7, v1\n"
        "s_mov_b32 m0, 33280\n"
        "s_mov_b64 exec, 0xffff\n"
        "buffer_load_dword v7, s[40:43], s4 offen sc1 lds\n"
        "s_mov_b64 exec, -1\n"
        "v_exp_f32 v60, v60\n"
        "v_exp_f32 v61, v61\n"
        "v_exp_f32 v62, v62\n"
        "v_exp_f32 v63, v63\n"
        "s_setprio 0\n"
        "v_exp_f32 v32, v32\n"
        "v_exp_f32 v33, v33\n"
        "v_smfmac_f32_32x32x32_bf16 v[112:127], v[144:147], v[56:63], v20\n"
        "s_mov_b64 exec, s[60:61]\n"
        "ds_read_b128 v[144:147], v9 offset:36352\n"
        "s_mov_b64 exec, -1\n"
        "ds_read_b32 v10, v14\n"
        "ds_read_b32 v24, v25\n"
        "v_exp_f32 v34, v34\n"
        "v_exp_f32 v35, v35\n"
        "v_exp_f32 v36, v36\n"
        "v_exp_f32 v37, v37\n"
        "v_mfma_f32_32x32x16_bf16 v[48:63], v[136:139], v[132:135], v[80:95]\n"
        "v_exp_f32 v38, v38\n"
        "v_exp_f32 v39, v39\n"
        "v_exp_f32 v40, v40\n"
        "v_exp_f32 v41, v41\n"
        "v_exp_f32 v42, v42\n"
        "v_exp_f32 v43, v43\n"
        "s_waitcnt lgkmcnt(0)\n"
        "v_cmp_gt_u32 vcc, 72, v10\n"
        "v_smfmac_f32_32x32x32_bf16 v[96:111], v[252:255], v[32:39], v20\n"
        "s_cbranch_vccnz 175f\n"
        "176:\n"
        "buffer_store_dword v3, v27, s[20:23], 0 offen sc1\n"
        "v_exp_f32 v44, v44\n"
        "v_exp_f32 v45, v45\n"
        "v_exp_f32 v46, v46\n"
        "v_exp_f32 v47, v47\n"
        "v_exp_f32 v48, v48\n"
        "v_exp_f32 v49, v49\n"
        "v_smfmac_f32_32x32x32_bf16 v[96:111], v[144:147], v[40:47], v20\n"
        "v_exp_f32 v50, v50\n"
        "v_exp_f32 v51, v51\n"
        "v_exp_f32 v52, v52\n"
        "v_exp_f32 v53, v53\n"
        "v_exp_f32 v54, v54\n"
        "v_exp_f32 v55, v55\n"
        "v_exp_f32 v56, v56\n"
        "v_exp_f32 v57, v57\n"
        "v_exp_f32 v58, v58\n"
        "v_exp_f32 v59, v59\n"
        "v_smfmac_f32_32x32x32_bf16 v[112:127], v[252:255], v[48:55], v20\n"
        "v_exp_f32 v60, v60\n"
        "v_exp_f32 v61, v61\n"
        "v_exp_f32 v62, v62\n"
        "v_exp_f32 v63, v63\n"
        "s_nop 1\n"
        "v_smfmac_f32_32x32x32_bf16 v[112:127], v[144:147], v[56:63], v20\n"
        "v_add_u32 v28, s55, v2\n"
        "v_xor_b32 v29, 16384, v28\n"
        "s_nop 9\n"
        "ds_write_b128 v28, v[112:115] offset:0\n"
        "ds_write_b128 v28, v[116:119] offset:1024\n"
        "ds_write_b32 v28, v120 offset:2048\n"
        "s_waitcnt lgkmcnt(0)\n"
        "121:\n"
        "s_barrier\n"
        "buffer_load_dword v6, v5, s[20:23], 0 offen sc1\n"
        "ds_read_b128 v[112:115], v29 offset:0\n"
        "ds_read_b128 v[116:119], v29 offset:1024\n"
        "ds_read_b32 v120, v29 offset:2048\n"
        "s_waitcnt lgkmcnt(0)\n"
        "v_pk_add_f32 v[96:97], v[96:97], v[112:113]\n"
        "v_pk_add_f32 v[98:99], v[98:99], v[114:115]\n"
        "v_pk_add_f32 v[100:101], v[100:101], v[116:117]\n"
        "v_pk_add_f32 v[102:103], v[102:103], v[118:119]\n"
        "v_add_f32 v104, v104, v120\n"
        "v_mov_b32 v120, v104\n"
        "v_mov_b32 v121, v104\n"
        "s_nop 1\n"
        "v_permlane32_swap_b32 v120, v121\n"
        "v_rcp_f32 v120, v120\n"
        "s_nop 0\n"
        "v_pk_mul_f32 v[64:65], v[120:121], v[96:97] op_sel:[0,0] op_sel_hi:[0,1]\n"
        "v_pk_mul_f32 v[66:67], v[120:121], v[98:99] op_sel:[0,0] op_sel_hi:[0,1]\n"
        "v_pk_mul_f32 v[68:69], v[120:121], v[100:101] op_sel:[0,0] op_sel_hi:[0,1]\n"
        "v_pk_mul_f32 v[70:71], v[120:121], v[102:103] op_sel:[0,0] op_sel_hi:[0,1]\n"
        "v_cvt_pk_bf16_f32 v108, v64, v65\n"
        "v_cvt_pk_bf16_f32 v109, v66, v67\n"
        "v_cvt_pk_bf16_f32 v110, v68, v69\n"
        "v_cvt_pk_bf16_f32 v111, v70, v71\n"
        "buffer_store_dwordx4 v[108:111], v2, s[20:23], s1 offen sc1\n"
        "v_max3_f32 v112, |v96|, |v97|, |v98|\n"
        "v_max3_f32 v112, v112, |v99|, |v100|\n"
        "v_max3_f32 v112, v112, |v101|, |v102|\n"
        "v_max_f32_e64 v112, v112, |v103|\n"
        "v_pk_add_f32 v[116:117], v[96:97], v[98:99]\n"
        "v_pk_add_f32 v[118:119], v[100:101], v[102:103]\n"
        "v_pk_add_f32 v[116:117], v[116:117], v[118:119]\n"
        "v_mov_b32 v113, v112\n"
        "v_add_f32 v116, v116, v117\n"
        "v_add_f32 v116, v116, v104\n"
        "v_permlane32_swap_b32 v112, v113\n"
        "v_max_f32 v112, v112, v113\n"
        "v_max3_f32 v117, v112, v104, |v116|\n"
        "v_mov_b32 v118, 0x7cf0bdc2\n"
        "v_cmp_ngt_f32 vcc, v118, v117\n"
        "v_cmp_u_f32_e64 s[62:63], v116, v116\n"
        "s_nop 0\n"
        "s_or_b64 s[62:63], s[62:63], vcc\n"
        "v_cmp_nlt_f32 vcc, 0x03800000, v112\n"
        "s_nop 0\n"
        "s_or_b64 vcc, vcc, s[62:63]\n"
        "s_cbranch_vccnz 230f\n"
        "231:\n"
        "s_mul_i32 s76, s5, 180224\n"
        "s_add_u32 s76, s76, 8912896\n"
        "s_mul_i32 s71, s48, 7168\n"
        "s_add_u32 s71, s71, s76\n"
        "s_lshl_b32 s62, s6, 7\n"
        "s_add_u32 s62, s62, 851968\n"
        "s_cmp_eq_u32 s48, 0\n"
        "s_cselect_b32 s62, s62, 2147483632\n"
        "v_mov_b32 v4, 2147483632\n"
        "v_writelane_b32 v4, s62, 0\n"
        "s_lshl3_add_u32 s62, s49, s48\n"
        "s_lshl_b32 s62, s62, 7\n"
        "s_add_u32 s62, s62, 851968\n"
        "v_and_b32 v83, 3, v1\n"
        "v_lshl_add_u32 v9, v83, 13, s62\n"
        "s_lshl_b32 s56, s48, 14\n"
        "s_add_u32 s56, s56, 32768\n"
        "s_lshl_b32 s78, s48, 12\n"
        "s_add_u32 s78, s78, s76\n"
        "v_mov_b32 v7, v30\n"
        "v_mov_b32 v252, v16\n"
        "v_mov_b32 v253, v17\n"
        "v_mov_b32 v254, v18\n"
        "v_mov_b32 v255, v19\n"
        "s_barrier\n"
        "s_waitcnt vmcnt(1)\n"
        "v_cmp_ne_u32 vcc, v6, v3\n"
        "s_cbranch_vccnz 234f\n"
        "s_add_u32 s64, s71, 32768\n"
        "s_add_u32 s65, s64, 90112\n"
        "buffer_load_dwordx4 v[12:15], v2, s[20:23], s64 offen offset:0 sc1\n"
        "buffer_load_dwordx4 v[16:19], v2, s[20:23], s65 offen offset:0 sc1\n"
        "buffer_load_dwordx4 v[20:23], v2, s[20:23], s64 offen offset:1024 sc1\n"
        "buffer_load_dwordx4 v[24:27], v2, s[20:23], s65 offen offset:1024 sc1\n"
        "s_waitcnt vmcnt(4)\n"
        "s_barrier\n"
        "buffer_store_dword v3, v4, s[20:23], 0 offen sc1\n"
        "235:\n"
        "buffer_load_dwordx4 v[28:31], v2, s[20:23], s64 offen offset:2048 sc1\n"
        "buffer_load_dwordx4 v[32:35], v2, s[20:23], s65 offen offset:2048 sc1\n"
        "buffer_load_dwordx4 v[36:39], v2, s[20:23], s64 offen offset:3072 sc1\n"
        "buffer_load_dwordx4 v[40:43], v2, s[20:23], s65 offen offset:3072 sc1\n"
        "s_add_u32 s64, s71, 36864\n"
        "s_add_u32 s65, s64, 90112\n"
        "buffer_load_dwordx4 v[44:47], v2, s[20:23], s64 offen offset:0 sc1\n"
        "buffer_load_dwordx4 v[48:51], v2, s[20:23], s65 offen offset:0 sc1\n"
        "s_waitcnt vmcnt(9)\n"
        "v_mfma_f32_32x32x16_bf16 v[100:115], v[196:199], v[12:15], 0\n"
        "v_mfma_f32_32x32x16_bf16 v[116:131], v[196:199], v[16:19], 0\n"
        "v_mfma_f32_32x32x16_bf16 v[132:147], v[200:203], v[12:15], 0\n"
        "v_mfma_f32_32x32x16_bf16 v[148:163], v[200:203], v[16:19], 0\n"
        "buffer_load_dwordx4 v[52:55], v2, s[20:23], s64 offen offset:1024 sc1\n"
        "buffer_load_dwordx4 v[56:59], v2, s[20:23], s65 offen offset:1024 sc1\n"
        "s_waitcnt vmcnt(9)\n"
        "v_mfma_f32_32x32x16_bf16 v[100:115], v[204:207], v[20:23], v[100:115]\n"
        "v_mfma_f32_32x32x16_bf16 v[116:131], v[204:207], v[24:27], v[116:131]\n"
        "v_mfma_f32_32x32x16_bf16 v[132:147], v[208:211], v[20:23], v[132:147]\n"
        "v_mfma_f32_32x32x16_bf16 v[148:163], v[208:211], v[24:27], v[148:163]\n"
        "buffer_load_dwordx4 v[60:63], v2, s[20:23], s64 offen offset:2048 sc1\n"
        "buffer_load_dwordx4 v[64:67], v2, s[20:23], s65 offen offset:2048 sc1\n"
        "s_waitcnt vmcnt(8)\n"
        "v_mfma_f32_32x32x16_bf16 v[100:115], v[212:215], v[28:31], v[100:115]\n"
        "v_mfma_f32_32x32x16_bf16 v[116:131], v[212:215], v[32:35], v[116:131]\n"
        "v_mfma_f32_32x32x16_bf16 v[132:147], v[216:219], v[28:31], v[132:147]\n"
        "v_mfma_f32_32x32x16_bf16 v[148:163], v[216:219], v[32:35], v[148:163]\n"
        "s_waitcnt vmcnt(6)\n"
        "v_mfma_f32_32x32x16_bf16 v[100:115], v[220:223], v[36:39], v[100:115]\n"
        "v_mfma_f32_32x32x16_bf16 v[116:131], v[220:223], v[40:43], v[116:131]\n"
        "v_mfma_f32_32x32x16_bf16 v[132:147], v[224:227], v[36:39], v[132:147]\n"
        "v_mfma_f32_32x32x16_bf16 v[148:163], v[224:227], v[40:43], v[148:163]\n"
        "buffer_load_dword v8, v9, s[20:23], 0 offen sc1\n"
        "s_waitcnt vmcnt(5)\n"
        "v_mfma_f32_32x32x16_bf16 v[100:115], v[228:231], v[44:47], v[100:115]\n"
        "v_mfma_f32_32x32x16_bf16 v[116:131], v[228:231], v[48:51], v[116:131]\n"
        "v_mfma_f32_32x32x16_bf16 v[132:147], v[232:235], v[44:47], v[132:147]\n"
        "v_mfma_f32_32x32x16_bf16 v[148:163], v[232:235], v[48:51], v[148:163]\n"
        "s_waitcnt vmcnt(3)\n"
        "v_mfma_f32_32x32x16_bf16 v[100:115], v[236:239], v[52:55], v[100:115]\n"
        "v_mfma_f32_32x32x16_bf16 v[116:131], v[236:239], v[56:59], v[116:131]\n"
        "v_mfma_f32_32x32x16_bf16 v[132:147], v[240:243], v[52:55], v[132:147]\n"
        "v_mfma_f32_32x32x16_bf16 v[148:163], v[240:243], v[56:59], v[148:163]\n"
        "s_waitcnt vmcnt(0)\n"
        "v_cmp_ne_u32 vcc, v8, v3\n"
        "s_cbranch_vccnz 238f\n"
        "239:\n"
        "s_add_u32 s64, s78, 0\n"
        "s_add_u32 s65, s64, 90112\n"
        "buffer_load_dwordx4 v[68:71], v2, s[20:23], s64 offen offset:0 sc1\n"
        "buffer_load_dwordx4 v[72:75], v2, s[20:23], s65 offen offset:0 sc1\n"
        "buffer_load_dwordx4 v[76:79], v2, s[20:23], s64 offen offset:1024 sc1\n"
        "buffer_load_dwordx4 v[80:83], v2, s[20:23], s65 offen offset:1024 sc1\n"
        "buffer_load_dwordx4 v[84:87], v2, s[20:23], s64 offen offset:2048 sc1\n"
        "buffer_load_dwordx4 v[88:91], v2, s[20:23], s65 offen offset:2048 sc1\n"
        "buffer_load_dwordx4 v[92:95], v2, s[20:23], s64 offen offset:3072 sc1\n"
        "buffer_load_dwordx4 v[96:99], v2, s[20:23], s65 offen offset:3072 sc1\n"
        "v_mfma_f32_32x32x16_bf16 v[100:115], v[244:247], v[60:63], v[100:115]\n"
        "v_mfma_f32_32x32x16_bf16 v[116:131], v[244:247], v[64:67], v[116:131]\n"
        "v_mfma_f32_32x32x16_bf16 v[132:147], v[248:251], v[60:63], v[132:147]\n"
        "v_mfma_f32_32x32x16_bf16 v[148:163], v[248:251], v[64:67], v[148:163]\n"
        "v_lshrrev_b32 v42, 5, v1\n"
        "v_and_b32 v41, 15, v1\n"
        "v_xor_b32 v42, v42, v41\n"
        "v_lshl_add_u32 v12, v42, 4, s56\n"
        "v_and_b32 v43, 31, v1\n"
        "v_lshl_add_u32 v12, v43, 8, v12\n"
        "v_xor_b32 v13, 32, v12\n"
        "v_xor_b32 v14, 64, v12\n"
        "v_xor_b32 v15, 96, v12\n"
        "v_xor_b32 v16, 128, v12\n"
        "v_xor_b32 v17, 160, v12\n"
        "v_xor_b32 v18, 192, v12\n"
        "v_xor_b32 v19, 224, v12\n"
        "s_lshl_b32 s62, s48, 3\n"
        "v_lshrrev_b32 v22, 3, v1\n"
        "v_add_u32 v22, s62, v22\n"
        "v_and_b32 v20, 7, v1\n"
        "v_lshlrev_b32 v20, 1, v20\n"
        "v_and_b32 v42, 15, v22\n"
        "v_xor_b32 v20, v20, v42\n"
        "v_lshlrev_b32 v20, 4, v20\n"
        "v_lshl_add_u32 v20, v22, 8, v20\n"
        "v_add_u32 v20, 0x8000, v20\n"
        "v_xor_b32 v21, 16, v20\n"
        "v_add_u32 v22, 65536, v20\n"
        "v_add_u32 v23, 65536, v21\n"
        "v_lshlrev_b32 v24, 16, v252\n"
        "v_and_b32 v25, 0xffff0000, v252\n"
        "v_lshlrev_b32 v26, 16, v253\n"
        "v_and_b32 v27, 0xffff0000, v253\n"
        "v_lshlrev_b32 v28, 16, v254\n"
        "v_and_b32 v29, 0xffff0000, v254\n"
        "v_lshlrev_b32 v30, 16, v255\n"
        "v_and_b32 v31, 0xffff0000, v255\n"
        "s_waitcnt vmcnt(6)\n"
        "v_mfma_f32_32x32x16_bf16 v[100:115], v[180:183], v[68:71], v[100:115]\n"
        "v_mfma_f32_32x32x16_bf16 v[116:131], v[180:183], v[72:75], v[116:131]\n"
        "v_mfma_f32_32x32x16_bf16 v[132:147], v[184:187], v[68:71], v[132:147]\n"
        "v_mfma_f32_32x32x16_bf16 v[148:163], v[184:187], v[72:75], v[148:163]\n"
        "s_waitcnt vmcnt(4)\n"
        "v_mfma_f32_32x32x16_bf16 v[100:115], v[164:167], v[76:79], v[100:115]\n"
        "v_mfma_f32_32x32x16_bf16 v[116:131], v[164:167], v[80:83], v[116:131]\n"
        "v_mfma_f32_32x32x16_bf16 v[132:147], v[168:171], v[76:79], v[132:147]\n"
        "v_mfma_f32_32x32x16_bf16 v[148:163], v[168:171], v[80:83], v[148:163]\n"
        "s_waitcnt vmcnt(2)\n"
        "v_mfma_f32_32x32x16_bf16 v[100:115], v[172:175], v[84:87], v[100:115]\n"
        "s_waitcnt vmcnt(0)\n"
        "v_mfma_f32_32x32x16_bf16 v[100:115], v[188:191], v[92:95], v[100:115]\n"
        "v_mfma_f32_32x32x16_bf16 v[116:131], v[172:175], v[88:91], v[116:131]\n"
        "v_mfma_f32_32x32x16_bf16 v[116:131], v[188:191], v[96:99], v[116:131]\n"
        "s_nop 9\n"
        "ds_write_b128 v12, v[100:103] offset:0\n"
        "v_mfma_f32_32x32x16_bf16 v[132:147], v[176:179], v[84:87], v[132:147]\n"
        "ds_write_b128 v13, v[104:107] offset:0\n"
        "v_mfma_f32_32x32x16_bf16 v[132:147], v[192:195], v[92:95], v[132:147]\n"
        "ds_write_b128 v14, v[108:111] offset:0\n"
        "v_mfma_f32_32x32x16_bf16 v[148:163], v[176:179], v[88:91], v[148:163]\n"
        "ds_write_b128 v15, v[112:115] offset:0\n"
        "v_mfma_f32_32x32x16_bf16 v[148:163], v[192:195], v[96:99], v[148:163]\n"
        "ds_write_b128 v12, v[116:119] offset:8192\n"
        "ds_write_b128 v13, v[120:123] offset:8192\n"
        "ds_write_b128 v14, v[124:127] offset:8192\n"
        "ds_write_b128 v15, v[128:131] offset:8192\n"
        "s_nop 3\n"
        "ds_write_b128 v16, v[132:135] offset:0\n"
        "ds_write_b128 v17, v[136:139] offset:0\n"
        "ds_write_b128 v18, v[140:143] offset:0\n"
        "ds_write_b128 v19, v[144:147] offset:0\n"
        "ds_write_b128 v16, v[148:151] offset:8192\n"
        "ds_write_b128 v17, v[152:155] offset:8192\n"
        "ds_write_b128 v18, v[156:159] offset:8192\n"
        "s_waitcnt lgkmcnt(14)\n"
        "ds_write_b128 v19, v[160:163] offset:8192\n"
        "s_waitcnt lgkmcnt(0)\n"
        "s_barrier\n"
        "ds_read_b128 v[100:103], v20 offset:0\n"
        "ds_read_b128 v[104:107], v21 offset:0\n"
        "ds_read_b128 v[108:111], v20 offset:16384\n"
        "ds_read_b128 v[112:115], v21 offset:16384\n"
        "ds_read_b128 v[116:119], v20 offset:32768\n"
        "ds_read_b128 v[120:123], v21 offset:32768\n"
        "ds_read_b128 v[124:127], v20 offset:49152\n"
        "ds_read_b128 v[128:131], v21 offset:49152\n"
        "ds_read_b128 v[132:135], v22 offset:0\n"
        "ds_read_b128 v[136:139], v23 offset:0\n"
        "ds_read_b128 v[140:143], v22 offset:16384\n"
        "ds_read_b128 v[144:147], v23 offset:16384\n"
        "ds_read_b128 v[148:151], v22 offset:32768\n"
        "ds_read_b128 v[152:155], v23 offset:32768\n"
        "s_waitcnt lgkmcnt(12)\n"
        "ds_read_b128 v[156:159], v22 offset:49152\n"
        "ds_read_b128 v[160:163], v23 offset:49152\n"
        "v_pk_add_f32 v[24:25], v[24:25], v[100:101]\n"
        "v_pk_add_f32 v[26:27], v[26:27], v[102:103]\n"
        "v_pk_add_f32 v[28:29], v[28:29], v[104:105]\n"
        "v_pk_add_f32 v[30:31], v[30:31], v[106:107]\n"
        "s_waitcnt lgkmcnt(12)\n"
        "v_pk_add_f32 v[24:25], v[24:25], v[108:109]\n"
        "v_pk_add_f32 v[26:27], v[26:27], v[110:111]\n"
        "v_pk_add_f32 v[28:29], v[28:29], v[112:113]\n"
        "v_pk_add_f32 v[30:31], v[30:31], v[114:115]\n"
        "s_waitcnt lgkmcnt(10)\n"
        "v_pk_add_f32 v[24:25], v[24:25], v[116:117]\n"
        "v_pk_add_f32 v[26:27], v[26:27], v[118:119]\n"
        "v_pk_add_f32 v[28:29], v[28:29], v[120:121]\n"
        "v_pk_add_f32 v[30:31], v[30:31], v[122:123]\n"
        "s_waitcnt lgkmcnt(8)\n"
        "v_pk_add_f32 v[24:25], v[24:25], v[124:125]\n"
        "v_pk_add_f32 v[26:27], v[26:27], v[126:127]\n"
        "v_pk_add_f32 v[28:29], v[28:29], v[128:129]\n"
        "v_pk_add_f32 v[30:31], v[30:31], v[130:131]\n"
        "s_waitcnt lgkmcnt(6)\n"
        "v_pk_add_f32 v[24:25], v[24:25], v[132:133]\n"
        "v_pk_add_f32 v[26:27], v[26:27], v[134:135]\n"
        "v_pk_add_f32 v[28:29], v[28:29], v[136:137]\n"
        "v_pk_add_f32 v[30:31], v[30:31], v[138:139]\n"
        "s_waitcnt lgkmcnt(4)\n"
        "v_pk_add_f32 v[24:25], v[24:25], v[140:141]\n"
        "v_pk_add_f32 v[26:27], v[26:27], v[142:143]\n"
        "v_pk_add_f32 v[28:29], v[28:29], v[144:145]\n"
        "v_pk_add_f32 v[30:31], v[30:31], v[146:147]\n"
        "s_waitcnt lgkmcnt(2)\n"
        "v_pk_add_f32 v[24:25], v[24:25], v[148:149]\n"
        "v_pk_add_f32 v[26:27], v[26:27], v[150:151]\n"
        "v_pk_add_f32 v[28:29], v[28:29], v[152:153]\n"
        "v_pk_add_f32 v[30:31], v[30:31], v[154:155]\n"
        "s_waitcnt lgkmcnt(0)\n"
        "v_pk_add_f32 v[24:25], v[24:25], v[156:157]\n"
        "v_pk_add_f32 v[26:27], v[26:27], v[158:159]\n"
        "v_pk_add_f32 v[28:29], v[28:29], v[160:161]\n"
        "v_pk_add_f32 v[30:31], v[30:31], v[162:163]\n"
        "v_cvt_pk_bf16_f32 v24, v24, v25\n"
        "v_cvt_pk_bf16_f32 v25, v26, v27\n"
        "v_cvt_pk_bf16_f32 v26, v28, v29\n"
        "v_cvt_pk_bf16_f32 v27, v30, v31\n"
        "buffer_store_dwordx4 v[24:27], v7, s[32:35], 0 offen sc1\n"
        "s_or_b32 s62, s6, s48\n"
        "s_mov_b32 s62, 2147483632\n"
        "s_cselect_b32 s62, s62, 0\n"
        "v_writelane_b32 v4, s62, 0\n"
        "buffer_store_dword v3, v4, s[20:23], 0 offen sc1\n"
        "s_branch 99f\n"
        "120:\n"
        "s_add_u32 m0, s56, 24576\n"
        "s_mov_b32 s63, 0\n"
        "buffer_load_dwordx4 v15, s[20:23], s63 offen offset:2048 sc1 lds\n"
        "s_add_u32 m0, s56, 36864\n"
        "s_mov_b32 s63, 4096\n"
        "buffer_load_dwordx4 v15, s[20:23], s63 offen offset:2048 sc1 lds\n"
        "s_add_u32 m0, s56, 49152\n"
        "s_mov_b32 s63, 8192\n"
        "buffer_load_dwordx4 v15, s[20:23], s63 offen offset:2048 sc1 lds\n"
        "s_add_u32 m0, s56, 0\n"
        "s_mov_b32 s63, 12288\n"
        "buffer_load_dwordx4 v15, s[20:23], s63 offen offset:2048 sc1 lds\n"
        "s_add_u32 m0, s56, 12288\n"
        "s_mov_b32 s63, 16384\n"
        "buffer_load_dwordx4 v15, s[20:23], s63 offen offset:2048 sc1 lds\n"
        "s_lshl_b32 s64, s49, 7\n"
        "v_and_b32 v158, 7, v1\n"
        "v_bfe_u32 v157, v1, 4, 1\n"
        "v_lshlrev_b32 v157, 2, v157\n"
        "v_xor_b32 v158, v158, v157\n"
        "v_lshl_add_u32 v150, v158, 4, s64\n"
        "v_lshrrev_b32 v159, 3, v1\n"
        "v_lshl_add_u32 v150, v159, 10, v150\n"
        "v_and_b32 v156, 3, v1\n"
        "v_lshl_add_u32 v148, v156, 3, s56\n"
        "v_bfe_u32 v157, v1, 4, 1\n"
        "v_lshl_add_u32 v148, v157, 5, v148\n"
        "v_bfe_u32 v158, v1, 3, 1\n"
        "v_lshl_add_u32 v148, v158, 6, v148\n"
        "v_bfe_u32 v159, v1, 2, 2\n"
        "v_lshrrev_b32 v158, 5, v1\n"
        "v_lshl_add_u32 v159, v158, 3, v159\n"
        "v_lshl_add_u32 v148, v159, 7, v148\n"
        "v_xor_b32 v149, 64, v148\n"
        "s_and_b32 s72, s50, 3\n"
        "s_lshl1_add_u32 s72, s72, s52\n"
        "s_lshl3_add_u32 s73, s49, s72\n"
        "s_mul_i32 s73, s73, 22528\n"
        "s_add_u32 s73, s73, 14680064\n"
        "s_mov_b32 s74, s51\n"
        "s_min_u32 s75, s51, 2\n"
        "s_add_u32 s75, s75, 4\n"
        "s_add_u32 s76, s51, 7\n"
        "ds_write_b32 v14, v164\n"
        "s_waitcnt vmcnt(5)\n"
        "v_mfma_f32_32x32x16_bf16 v[32:47], v[180:183], v[128:131], 0\n"
        "v_mfma_f32_32x32x16_bf16 v[48:63], v[180:183], v[132:135], 0\n"
        "v_mov_b32 v20, 0xdddd\n"
        "v_mov_b32 v11, 1\n"
        "v_xor_b32 v25, 256, v14\n"
        "v_cmp_gt_u32 vcc, 32, v1\n"
        "v_lshlrev_b32 v26, 16, v26\n"
        "s_nop 0\n"
        "v_cndmask_b32_e64 v21, 0, 1.0, vcc\n"
        "v_mov_b32 v252, v140\n"
        "v_mov_b32 v253, v140\n"
        "v_mov_b32 v254, v140\n"
        "v_mov_b32 v255, v140\n"
        "v_max3_f32 v64, v32, v33, v34\n"
        "v_max3_f32 v64, v64, v35, v36\n"
        "v_max3_f32 v64, v64, v37, v38\n"
        "v_max3_f32 v64, v64, v39, v40\n"
        "v_max3_f32 v64, v64, v41, v42\n"
        "v_max3_f32 v64, v64, v43, v44\n"
        "v_max3_f32 v64, v64, v45, v46\n"
        "v_max_f32 v64, v64, v47\n"
        "v_max3_f32 v80, v48, v49, v50\n"
        "v_max3_f32 v80, v80, v51, v52\n"
        "v_max3_f32 v80, v80, v53, v54\n"
        "v_max3_f32 v80, v80, v55, v56\n"
        "v_max3_f32 v80, v80, v57, v58\n"
        "v_max3_f32 v80, v80, v59, v60\n"
        "v_max3_f32 v80, v80, v61, v62\n"
        "v_max_f32 v80, v80, v63\n"
        "v_mov_b32 v65, v64\n"
        "v_mov_b32 v81, v80\n"
        "s_nop 0\n"
        "v_permlane32_swap_b32 v64, v65\n"
        "v_permlane32_swap_b32 v80, v81\n"
        "v_max_f32 v64, v64, v65\n"
        "v_max_f32 v80, v80, v81\n"
        "v_sub_f32 v22, 0xc2c80000, v64\n"
        "v_sub_f32 v23, 0xc2c80000, v80\n"
        "s_nop 0\n"
        "v_mfma_f32_32x32x2_f32 v[64:79], v21, v22, 0\n"
        "v_mfma_f32_32x32x2_f32 v[80:95], v21, v23, 0\n"
        "s_waitcnt vmcnt(4) lgkmcnt(0)\n"
        "s_barrier\n"
        "ds_read_b128 v[136:139], v8 offset:26624\n"
        "s_mov_b64 exec, s[60:61]\n"
        "ds_read_b128 v[140:143], v9 offset:32768\n"
        "s_mov_b64 exec, -1\n"
        "s_waitcnt lgkmcnt(1)\n"
        "s_nop 9\n"
        "v_mfma_f32_32x32x16_bf16 v[32:47], v[136:139], v[128:131], v[64:79]\n"
        "s_setprio 2\n"
        "s_nop 10\n"
        "v_exp_f32 v32, v32\n"
        "v_exp_f32 v33, v33\n"
        "s_mov_b64 exec, s[60:61]\n"
        "ds_read_b128 v[144:147], v9 offset:33280\n"
        "ds_read_b128 v[252:255], v9 offset:35840\n"
        "s_mov_b64 exec, -1\n"
        "v_mfma_f32_32x32x16_bf16 v[96:111], v[164:167], v[164:167], 0\n"
        "v_exp_f32 v34, v34\n"
        "v_exp_f32 v35, v35\n"
        "v_exp_f32 v36, v36\n"
        "v_exp_f32 v37, v37\n"
        "v_mfma_f32_32x32x16_bf16 v[48:63], v[136:139], v[132:135], v[80:95]\n"
        "ds_read_b128 v[136:139], v8 offset:29696\n"
        "v_mfma_f32_32x32x16_bf16 v[112:127], v[164:167], v[164:167], 0\n"
        "v_exp_f32 v38, v38\n"
        "v_exp_f32 v39, v39\n"
        "v_exp_f32 v40, v40\n"
        "v_exp_f32 v41, v41\n"
        "v_exp_f32 v42, v42\n"
        "v_exp_f32 v43, v43\n"
        "s_waitcnt vmcnt(2) lgkmcnt(1)\n"
        "v_smfmac_f32_32x32x32_bf16 v[96:111], v[140:143], v[32:39], v20\n"
        "ds_add_u32 v14, v11\n"
        "s_add_u32 m0, s56, 24576\n"
        "s_movk_i32 s7, 384\n"
        "buffer_load_dwordx4 v12, s[24:27], s7 offen offset:0 lds\n"
        "v_exp_f32 v44, v44\n"
        "v_exp_f32 v45, v45\n"
        "v_exp_f32 v46, v46\n"
        "v_exp_f32 v47, v47\n"
        "v_exp_f32 v48, v48\n"
        "v_exp_f32 v49, v49\n"
        "v_smfmac_f32_32x32x32_bf16 v[96:111], v[144:147], v[40:47], v20\n"
        "ds_read_b32 v10, v14\n"
        "ds_read_b32 v24, v25\n"
        "v_exp_f32 v50, v50\n"
        "v_exp_f32 v51, v51\n"
        "v_exp_f32 v52, v52\n"
        "v_exp_f32 v53, v53\n"
        "s_waitcnt lgkmcnt(2)\n"
        "v_mfma_f32_32x32x16_bf16 v[32:47], v[136:139], v[128:131], v[64:79]\n"
        "buffer_load_dwordx4 v13, s[24:27], s7 offen offset:1024 lds\n"
        "v_exp_f32 v54, v54\n"
        "v_exp_f32 v55, v55\n"
        "v_exp_f32 v56, v56\n"
        "v_exp_f32 v57, v57\n"
        "v_exp_f32 v58, v58\n"
        "v_exp_f32 v59, v59\n"
        "s_waitcnt lgkmcnt(0)\n"
        "v_cmp_gt_u32 vcc, 4, v10\n"
        "v_smfmac_f32_32x32x32_bf16 v[112:127], v[140:143], v[48:55], v20\n"
        "s_cbranch_vccnz 177f\n"
        "178:\n"
        "v_cmp_gt_u32 vcc, 4, v24\n"
        "s_setprio 3\n"
        "s_cbranch_vccz 179f\n"
        "s_setprio 2\n"
        "179:\n"
        "v_exp_f32 v60, v60\n"
        "v_exp_f32 v61, v61\n"
        "v_exp_f32 v62, v62\n"
        "v_exp_f32 v63, v63\n"
        "v_exp_f32 v32, v32\n"
        "v_exp_f32 v33, v33\n"
        "v_smfmac_f32_32x32x32_bf16 v[112:127], v[144:147], v[56:63], v20\n"
        "s_mov_b64 exec, s[60:61]\n"
        "ds_read_b128 v[144:147], v9 offset:36352\n"
        "ds_read_b128 v[140:143], v9 offset:45056\n"
        "s_mov_b64 exec, -1\n"
        "v_exp_f32 v34, v34\n"
        "v_exp_f32 v35, v35\n"
        "v_exp_f32 v36, v36\n"
        "v_exp_f32 v37, v37\n"
        "v_mfma_f32_32x32x16_bf16 v[48:63], v[136:139], v[132:135], v[80:95]\n"
        "ds_read_b128 v[136:139], v8 offset:38912\n"
        "s_movk_i32 s7, 512\n"
        "buffer_load_dwordx4 v12, s[24:27], s7 offen offset:0 lds\n"
        "v_exp_f32 v38, v38\n"
        "v_exp_f32 v39, v39\n"
        "v_exp_f32 v40, v40\n"
        "v_exp_f32 v41, v41\n"
        "v_exp_f32 v42, v42\n"
        "v_exp_f32 v43, v43\n"
        "v_smfmac_f32_32x32x32_bf16 v[96:111], v[252:255], v[32:39], v20\n"
        "v_exp_f32 v44, v44\n"
        "v_exp_f32 v45, v45\n"
        "v_exp_f32 v46, v46\n"
        "v_exp_f32 v47, v47\n"
        "v_exp_f32 v48, v48\n"
        "v_exp_f32 v49, v49\n"
        "s_waitcnt lgkmcnt(0)\n"
        "v_smfmac_f32_32x32x32_bf16 v[96:111], v[144:147], v[40:47], v20\n"
        "v_exp_f32 v50, v50\n"
        "v_exp_f32 v51, v51\n"
        "v_exp_f32 v52, v52\n"
        "v_exp_f32 v53, v53\n"
        "v_mfma_f32_32x32x16_bf16 v[32:47], v[136:139], v[128:131], v[64:79]\n"
        "buffer_load_dwordx4 v13, s[24:27], s7 offen offset:1024 lds\n"
        "v_exp_f32 v54, v54\n"
        "v_exp_f32 v55, v55\n"
        "v_exp_f32 v56, v56\n"
        "v_exp_f32 v57, v57\n"
        "v_exp_f32 v58, v58\n"
        "v_exp_f32 v59, v59\n"
        "v_smfmac_f32_32x32x32_bf16 v[112:127], v[252:255], v[48:55], v20\n"
        "v_exp_f32 v60, v60\n"
        "v_exp_f32 v61, v61\n"
        "v_exp_f32 v62, v62\n"
        "v_exp_f32 v63, v63\n"
        "v_exp_f32 v32, v32\n"
        "v_exp_f32 v33, v33\n"
        "v_smfmac_f32_32x32x32_bf16 v[112:127], v[144:147], v[56:63], v20\n"
        "s_mov_b64 exec, s[60:61]\n"
        "ds_read_b128 v[144:147], v9 offset:45568\n"
        "ds_read_b128 v[252:255], v9 offset:48128\n"
        "s_mov_b64 exec, -1\n"
        "v_exp_f32 v34, v34\n"
        "v_exp_f32 v35, v35\n"
        "v_exp_f32 v36, v36\n"
        "v_exp_f32 v37, v37\n"
        "v_mfma_f32_32x32x16_bf16 v[48:63], v[136:139], v[132:135], v[80:95]\n"
        "ds_read_b128 v[136:139], v8 offset:41984\n"
        "s_add_u32 m0, s56, 49152\n"
        "s_mul_i32 s64, s72, 7\n"
        "s_add_u32 s64, s64, s74\n"
        "s_lshl_b32 s64, s64, 14\n"
        "buffer_load_dwordx4 v150, s[12:15], s64 offen offset:0 lds\n"
        "v_exp_f32 v38, v38\n"
        "v_exp_f32 v39, v39\n"
        "v_exp_f32 v40, v40\n"
        "v_exp_f32 v41, v41\n"
        "v_exp_f32 v42, v42\n"
        "v_exp_f32 v43, v43\n"
        "v_smfmac_f32_32x32x32_bf16 v[96:111], v[140:143], v[32:39], v20\n"
        "v_exp_f32 v44, v44\n"
        "v_exp_f32 v45, v45\n"
        "v_exp_f32 v46, v46\n"
        "v_exp_f32 v47, v47\n"
        "v_exp_f32 v48, v48\n"
        "v_exp_f32 v49, v49\n"
        "s_waitcnt lgkmcnt(0)\n"
        "v_smfmac_f32_32x32x32_bf16 v[96:111], v[144:147], v[40:47], v20\n"
        "v_exp_f32 v50, v50\n"
        "v_exp_f32 v51, v51\n"
        "v_exp_f32 v52, v52\n"
        "v_exp_f32 v53, v53\n"
        "v_mfma_f32_32x32x16_bf16 v[32:47], v[136:139], v[128:131], v[64:79]\n"
        "s_add_u32 m0, s56, 49152\n"
        "s_mul_i32 s64, s72, 7\n"
        "s_add_u32 s64, s64, s74\n"
        "s_lshl_b32 s64, s64, 14\n"
        "s_add_u32 s64, s64, 7168\n"
        "buffer_load_dwordx4 v150, s[12:15], s64 offen offset:1024 lds\n"
        "v_exp_f32 v54, v54\n"
        "v_exp_f32 v55, v55\n"
        "v_exp_f32 v56, v56\n"
        "v_exp_f32 v57, v57\n"
        "v_exp_f32 v58, v58\n"
        "v_exp_f32 v59, v59\n"
        "v_smfmac_f32_32x32x32_bf16 v[112:127], v[140:143], v[48:55], v20\n"
        "ds_add_u32 v14, v11\n"
        "v_exp_f32 v60, v60\n"
        "v_exp_f32 v61, v61\n"
        "v_exp_f32 v62, v62\n"
        "v_exp_f32 v63, v63\n"
        "v_exp_f32 v32, v32\n"
        "v_exp_f32 v33, v33\n"
        "v_smfmac_f32_32x32x32_bf16 v[112:127], v[144:147], v[56:63], v20\n"
        "s_mov_b64 exec, s[60:61]\n"
        "ds_read_b128 v[144:147], v9 offset:48640\n"
        "ds_read_b128 v[140:143], v9 offset:57344\n"
        "s_mov_b64 exec, -1\n"
        "ds_read_b32 v10, v14\n"
        "ds_read_b32 v24, v25\n"
        "v_exp_f32 v34, v34\n"
        "v_exp_f32 v35, v35\n"
        "v_exp_f32 v36, v36\n"
        "v_exp_f32 v37, v37\n"
        "v_mfma_f32_32x32x16_bf16 v[48:63], v[136:139], v[132:135], v[80:95]\n"
        "ds_read_b128 v[136:139], v8 offset:51200\n"
        "s_add_u32 m0, s56, 0\n"
        "s_mul_i32 s64, s72, 7\n"
        "s_add_u32 s64, s64, s75\n"
        "s_lshl_b32 s64, s64, 14\n"
        "buffer_load_dwordx4 v150, s[12:15], s64 offen offset:0 lds\n"
        "v_exp_f32 v38, v38\n"
        "v_exp_f32 v39, v39\n"
        "v_exp_f32 v40, v40\n"
        "v_exp_f32 v41, v41\n"
        "v_exp_f32 v42, v42\n"
        "v_exp_f32 v43, v43\n"
        "s_waitcnt lgkmcnt(0)\n"
        "v_cmp_gt_u32 vcc, 8, v10\n"
        "v_smfmac_f32_32x32x32_bf16 v[96:111], v[252:255], v[32:39], v20\n"
        "s_cbranch_vccnz 180f\n"
        "181:\n"
        "v_cmp_gt_u32 vcc, 8, v24\n"
        "s_setprio 3\n"
        "s_cbranch_vccz 182f\n"
        "s_setprio 2\n"
        "182:\n"
        "v_exp_f32 v44, v44\n"
        "v_exp_f32 v45, v45\n"
        "v_exp_f32 v46, v46\n"
        "v_exp_f32 v47, v47\n"
        "v_exp_f32 v48, v48\n"
        "v_exp_f32 v49, v49\n"
        "v_smfmac_f32_32x32x32_bf16 v[96:111], v[144:147], v[40:47], v20\n"
        "s_add_u32 m0, s56, 24576\n"
        "s_mov_b32 s63, 20480\n"
        "buffer_load_dwordx4 v15, s[20:23], s63 offen offset:2048 sc1 lds\n"
        "v_exp_f32 v50, v50\n"
        "v_exp_f32 v51, v51\n"
        "v_exp_f32 v52, v52\n"
        "v_exp_f32 v53, v53\n"
        "v_mfma_f32_32x32x16_bf16 v[32:47], v[136:139], v[128:131], v[64:79]\n"
        "s_add_u32 m0, s56, 0\n"
        "s_mul_i32 s64, s72, 7\n"
        "s_add_u32 s64, s64, s75\n"
        "s_lshl_b32 s64, s64, 14\n"
        "s_add_u32 s64, s64, 7168\n"
        "buffer_load_dwordx4 v150, s[12:15], s64 offen offset:1024 lds\n"
        "v_exp_f32 v54, v54\n"
        "v_exp_f32 v55, v55\n"
        "v_exp_f32 v56, v56\n"
        "v_exp_f32 v57, v57\n"
        "v_exp_f32 v58, v58\n"
        "v_exp_f32 v59, v59\n"
        "v_smfmac_f32_32x32x32_bf16 v[112:127], v[252:255], v[48:55], v20\n"
        "v_exp_f32 v60, v60\n"
        "v_exp_f32 v61, v61\n"
        "v_exp_f32 v62, v62\n"
        "v_exp_f32 v63, v63\n"
        "v_exp_f32 v32, v32\n"
        "v_exp_f32 v33, v33\n"
        "v_smfmac_f32_32x32x32_bf16 v[112:127], v[144:147], v[56:63], v20\n"
        "s_mov_b64 exec, s[60:61]\n"
        "ds_read_b128 v[144:147], v9 offset:57856\n"
        "ds_read_b128 v[252:255], v9 offset:60416\n"
        "s_mov_b64 exec, -1\n"
        "v_exp_f32 v34, v34\n"
        "v_exp_f32 v35, v35\n"
        "v_exp_f32 v36, v36\n"
        "v_exp_f32 v37, v37\n"
        "v_mfma_f32_32x32x16_bf16 v[48:63], v[136:139], v[132:135], v[80:95]\n"
        "ds_read_b128 v[136:139], v8 offset:54272\n"
        "s_add_u32 m0, s56, 12288\n"
        "s_lshl2_add_u32 s64, s72, s51\n"
        "s_lshl_b32 s64, s64, 14\n"
        "buffer_load_dwordx4 v150, s[28:31], s64 offen offset:0 lds\n"
        "v_exp_f32 v38, v38\n"
        "v_exp_f32 v39, v39\n"
        "v_exp_f32 v40, v40\n"
        "v_exp_f32 v41, v41\n"
        "v_exp_f32 v42, v42\n"
        "v_exp_f32 v43, v43\n"
        "s_waitcnt vmcnt(11) lgkmcnt(0)\n"
        "v_smfmac_f32_32x32x32_bf16 v[96:111], v[140:143], v[32:39], v20\n"
        "ds_add_u32 v14, v11\n"
        "v_exp_f32 v44, v44\n"
        "v_exp_f32 v45, v45\n"
        "v_exp_f32 v46, v46\n"
        "v_exp_f32 v47, v47\n"
        "v_exp_f32 v48, v48\n"
        "v_exp_f32 v49, v49\n"
        "v_smfmac_f32_32x32x32_bf16 v[96:111], v[144:147], v[40:47], v20\n"
        "ds_read_b32 v10, v14\n"
        "ds_read_b32 v24, v25\n"
        "v_exp_f32 v50, v50\n"
        "v_exp_f32 v51, v51\n"
        "v_exp_f32 v52, v52\n"
        "v_exp_f32 v53, v53\n"
        "v_mfma_f32_32x32x16_bf16 v[32:47], v[136:139], v[128:131], v[64:79]\n"
        "s_add_u32 m0, s56, 12288\n"
        "s_lshl2_add_u32 s64, s72, s51\n"
        "s_lshl_b32 s64, s64, 14\n"
        "s_add_u32 s64, s64, 7168\n"
        "buffer_load_dwordx4 v150, s[28:31], s64 offen offset:1024 lds\n"
        "v_exp_f32 v54, v54\n"
        "v_exp_f32 v55, v55\n"
        "v_exp_f32 v56, v56\n"
        "v_exp_f32 v57, v57\n"
        "v_exp_f32 v58, v58\n"
        "v_exp_f32 v59, v59\n"
        "s_waitcnt lgkmcnt(0)\n"
        "v_cmp_gt_u32 vcc, 12, v10\n"
        "v_smfmac_f32_32x32x32_bf16 v[112:127], v[140:143], v[48:55], v20\n"
        "s_cbranch_vccnz 183f\n"
        "184:\n"
        "v_cmp_gt_u32 vcc, 12, v24\n"
        "s_setprio 3\n"
        "s_cbranch_vccz 185f\n"
        "s_setprio 2\n"
        "185:\n"
        "v_exp_f32 v60, v60\n"
        "v_exp_f32 v61, v61\n"
        "v_exp_f32 v62, v62\n"
        "v_exp_f32 v63, v63\n"
        "v_exp_f32 v32, v32\n"
        "v_exp_f32 v33, v33\n"
        "v_smfmac_f32_32x32x32_bf16 v[112:127], v[144:147], v[56:63], v20\n"
        "s_mov_b64 exec, s[60:61]\n"
        "ds_read_b128 v[144:147], v9 offset:60928\n"
        "ds_read_b128 v[140:143], v9 offset:8192\n"
        "s_mov_b64 exec, -1\n"
        "v_exp_f32 v34, v34\n"
        "v_exp_f32 v35, v35\n"
        "v_exp_f32 v36, v36\n"
        "v_exp_f32 v37, v37\n"
        "v_mfma_f32_32x32x16_bf16 v[48:63], v[136:139], v[132:135], v[80:95]\n"
        "ds_read_b128 v[136:139], v8 offset:2048\n"
        "s_add_u32 m0, s56, 36864\n"
        "s_mov_b32 s63, 24576\n"
        "buffer_load_dwordx4 v15, s[20:23], s63 offen offset:2048 sc1 lds\n"
        "v_exp_f32 v38, v38\n"
        "v_exp_f32 v39, v39\n"
        "v_exp_f32 v40, v40\n"
        "v_exp_f32 v41, v41\n"
        "v_exp_f32 v42, v42\n"
        "v_exp_f32 v43, v43\n"
        "v_smfmac_f32_32x32x32_bf16 v[96:111], v[252:255], v[32:39], v20\n"
        "v_exp_f32 v44, v44\n"
        "v_exp_f32 v45, v45\n"
        "v_exp_f32 v46, v46\n"
        "v_exp_f32 v47, v47\n"
        "v_exp_f32 v48, v48\n"
        "v_exp_f32 v49, v49\n"
        "s_waitcnt lgkmcnt(0)\n"
        "v_smfmac_f32_32x32x32_bf16 v[96:111], v[144:147], v[40:47], v20\n"
        "s_sub_u32 s64, s56, 61440\n"
        "v_exp_f32 v50, v50\n"
        "v_exp_f32 v51, v51\n"
        "v_exp_f32 v52, v52\n"
        "v_exp_f32 v53, v53\n"
        "v_mfma_f32_32x32x16_bf16 v[32:47], v[136:139], v[128:131], v[64:79]\n"
        "s_add_u32 s65, s56, 61440\n"
        "v_exp_f32 v54, v54\n"
        "v_exp_f32 v55, v55\n"
        "v_exp_f32 v56, v56\n"
        "v_exp_f32 v57, v57\n"
        "v_exp_f32 v58, v58\n"
        "v_exp_f32 v59, v59\n"
        "s_waitcnt vmcnt(12)\n"
        "v_smfmac_f32_32x32x32_bf16 v[112:127], v[252:255], v[48:55], v20\n"
        "ds_add_u32 v14, v11\n"
        "v_exp_f32 v60, v60\n"
        "v_exp_f32 v61, v61\n"
        "v_exp_f32 v62, v62\n"
        "v_exp_f32 v63, v63\n"
        "v_exp_f32 v32, v32\n"
        "v_exp_f32 v33, v33\n"
        "v_smfmac_f32_32x32x32_bf16 v[112:127], v[144:147], v[56:63], v20\n"
        "s_mov_b64 exec, s[60:61]\n"
        "ds_read_b128 v[144:147], v9 offset:8704\n"
        "ds_read_b128 v[252:255], v9 offset:11264\n"
        "s_mov_b64 exec, -1\n"
        "s_cmp_eq_u32 s52, 0\n"
        "s_cselect_b32 s64, s65, s64\n"
        "v_exp_f32 v34, v34\n"
        "v_exp_f32 v35, v35\n"
        "v_exp_f32 v36, v36\n"
        "v_exp_f32 v37, v37\n"
        "v_mfma_f32_32x32x16_bf16 v[48:63], v[136:139], v[132:135], v[80:95]\n"
        "ds_read_b128 v[136:139], v8 offset:5120\n"
        "v_add_u32 v28, s56, v2\n"
        "v_exp_f32 v38, v38\n"
        "v_exp_f32 v39, v39\n"
        "v_exp_f32 v40, v40\n"
        "v_exp_f32 v41, v41\n"
        "v_exp_f32 v42, v42\n"
        "v_exp_f32 v43, v43\n"
        "v_smfmac_f32_32x32x32_bf16 v[96:111], v[140:143], v[32:39], v20\n"
        "v_exp_f32 v44, v44\n"
        "v_exp_f32 v45, v45\n"
        "v_exp_f32 v46, v46\n"
        "v_exp_f32 v47, v47\n"
        "v_exp_f32 v48, v48\n"
        "v_exp_f32 v49, v49\n"
        "s_waitcnt lgkmcnt(0)\n"
        "v_smfmac_f32_32x32x32_bf16 v[96:111], v[144:147], v[40:47], v20\n"
        "ds_read_b32 v10, v14\n"
        "ds_read_b32 v24, v25\n"
        "v_add_u32 v29, s64, v2\n"
        "v_exp_f32 v50, v50\n"
        "v_exp_f32 v51, v51\n"
        "v_exp_f32 v52, v52\n"
        "v_exp_f32 v53, v53\n"
        "v_mfma_f32_32x32x16_bf16 v[32:47], v[136:139], v[128:131], v[64:79]\n"
        "v_mov_b32 v22, s45\n"
        "v_exp_f32 v54, v54\n"
        "v_exp_f32 v55, v55\n"
        "v_exp_f32 v56, v56\n"
        "v_exp_f32 v57, v57\n"
        "v_exp_f32 v58, v58\n"
        "v_exp_f32 v59, v59\n"
        "s_waitcnt lgkmcnt(0)\n"
        "v_cmp_gt_u32 vcc, 16, v10\n"
        "v_smfmac_f32_32x32x32_bf16 v[112:127], v[140:143], v[48:55], v20\n"
        "s_cbranch_vccnz 186f\n"
        "187:\n"
        "v_cmp_gt_u32 vcc, 16, v24\n"
        "s_setprio 3\n"
        "s_cbranch_vccz 188f\n"
        "s_setprio 2\n"
        "188:\n"
        "v_exp_f32 v60, v60\n"
        "v_exp_f32 v61, v61\n"
        "v_exp_f32 v62, v62\n"
        "v_exp_f32 v63, v63\n"
        "v_exp_f32 v32, v32\n"
        "v_exp_f32 v33, v33\n"
        "v_smfmac_f32_32x32x32_bf16 v[112:127], v[144:147], v[56:63], v20\n"
        "s_mov_b64 exec, s[60:61]\n"
        "ds_read_b128 v[144:147], v9 offset:11776\n"
        "ds_read_b128 v[140:143], v9 offset:20480\n"
        "s_mov_b64 exec, -1\n"
        "v_writelane_b32 v22, s89, 0\n"
        "v_exp_f32 v34, v34\n"
        "v_exp_f32 v35, v35\n"
        "v_exp_f32 v36, v36\n"
        "v_exp_f32 v37, v37\n"
        "v_mfma_f32_32x32x16_bf16 v[48:63], v[136:139], v[132:135], v[80:95]\n"
        "ds_read_b128 v[136:139], v8 offset:14336\n"
        "s_add_u32 m0, s56, 49152\n"
        "s_mov_b32 s63, 28672\n"
        "buffer_load_dwordx4 v15, s[20:23], s63 offen offset:2048 sc1 lds\n"
        "v_exp_f32 v38, v38\n"
        "v_exp_f32 v39, v39\n"
        "v_exp_f32 v40, v40\n"
        "v_exp_f32 v41, v41\n"
        "v_exp_f32 v42, v42\n"
        "v_exp_f32 v43, v43\n"
        "s_waitcnt vmcnt(5) lgkmcnt(0)\n"
        "v_smfmac_f32_32x32x32_bf16 v[96:111], v[252:255], v[32:39], v20\n"
        "ds_add_u32 v14, v11\n"
        "v_exp_f32 v44, v44\n"
        "v_exp_f32 v45, v45\n"
        "v_exp_f32 v46, v46\n"
        "v_exp_f32 v47, v47\n"
        "v_exp_f32 v48, v48\n"
        "v_exp_f32 v49, v49\n"
        "v_smfmac_f32_32x32x32_bf16 v[96:111], v[144:147], v[40:47], v20\n"
        "ds_read_b64_tr_b16 v[188:189], v148 offset:49152\n"
        "v_exp_f32 v50, v50\n"
        "v_exp_f32 v51, v51\n"
        "v_exp_f32 v52, v52\n"
        "v_exp_f32 v53, v53\n"
        "v_mfma_f32_32x32x16_bf16 v[32:47], v[136:139], v[128:131], v[64:79]\n"
        "ds_read_b64_tr_b16 v[190:191], v148 offset:49664\n"
        "ds_read_b64_tr_b16 v[192:193], v149 offset:49152\n"
        "v_exp_f32 v54, v54\n"
        "v_exp_f32 v55, v55\n"
        "v_exp_f32 v56, v56\n"
        "v_exp_f32 v57, v57\n"
        "v_exp_f32 v58, v58\n"
        "v_exp_f32 v59, v59\n"
        "v_smfmac_f32_32x32x32_bf16 v[112:127], v[252:255], v[48:55], v20\n"
        "ds_read_b64_tr_b16 v[194:195], v149 offset:49664\n"
        "v_exp_f32 v60, v60\n"
        "v_exp_f32 v61, v61\n"
        "v_exp_f32 v62, v62\n"
        "v_exp_f32 v63, v63\n"
        "v_exp_f32 v32, v32\n"
        "v_exp_f32 v33, v33\n"
        "v_smfmac_f32_32x32x32_bf16 v[112:127], v[144:147], v[56:63], v20\n"
        "s_mov_b64 exec, s[60:61]\n"
        "ds_read_b128 v[144:147], v9 offset:20992\n"
        "ds_read_b128 v[252:255], v9 offset:23552\n"
        "s_mov_b64 exec, -1\n"
        "ds_read_b32 v10, v14\n"
        "ds_read_b32 v24, v25\n"
        "s_add_u32 m0, s56, 24576\n"
        "s_movk_i32 s7, 640\n"
        "buffer_load_dwordx4 v12, s[24:27], s7 offen offset:0 lds\n"
        "s_waitcnt vmcnt(5) lgkmcnt(4)\n"
        "v_exp_f32 v34, v34\n"
        "v_exp_f32 v35, v35\n"
        "v_exp_f32 v36, v36\n"
        "v_exp_f32 v37, v37\n"
        "v_mfma_f32_32x32x16_bf16 v[48:63], v[136:139], v[132:135], v[80:95]\n"
        "ds_read_b128 v[136:139], v8 offset:17408\n"
        "ds_read_b64_tr_b16 v[196:197], v148 offset:0\n"
        "ds_read_b64_tr_b16 v[198:199], v148 offset:512\n"
        "ds_read_b64_tr_b16 v[200:201], v149 offset:0\n"
        "v_exp_f32 v38, v38\n"
        "v_exp_f32 v39, v39\n"
        "v_exp_f32 v40, v40\n"
        "v_exp_f32 v41, v41\n"
        "v_exp_f32 v42, v42\n"
        "v_exp_f32 v43, v43\n"
        "s_waitcnt lgkmcnt(3)\n"
        "v_cmp_gt_u32 vcc, 20, v10\n"
        "v_smfmac_f32_32x32x32_bf16 v[96:111], v[140:143], v[32:39], v20\n"
        "s_cbranch_vccnz 189f\n"
        "190:\n"
        "v_cmp_gt_u32 vcc, 20, v24\n"
        "s_setprio 3\n"
        "s_cbranch_vccz 191f\n"
        "s_setprio 2\n"
        "191:\n"
        "ds_read_b64_tr_b16 v[202:203], v149 offset:512\n"
        "s_lshl_b32 s64, s74, 11\n"
        "s_add_u32 s64, s64, s73\n"
        "buffer_store_dwordx4 v[188:191], v2, s[20:23], s64 offen offset:0 sc1\n"
        "s_lshl_b32 s64, s74, 11\n"
        "s_add_u32 s64, s64, s73\n"
        "buffer_store_dwordx4 v[192:195], v2, s[20:23], s64 offen offset:1024 sc1\n"
        "v_exp_f32 v44, v44\n"
        "v_exp_f32 v45, v45\n"
        "v_exp_f32 v46, v46\n"
        "v_exp_f32 v47, v47\n"
        "v_exp_f32 v48, v48\n"
        "v_exp_f32 v49, v49\n"
        "v_smfmac_f32_32x32x32_bf16 v[96:111], v[144:147], v[40:47], v20\n"
        "buffer_load_dwordx4 v13, s[24:27], s7 offen offset:1024 lds\n"
        "s_waitcnt vmcnt(6) lgkmcnt(0)\n"
        "v_exp_f32 v50, v50\n"
        "v_exp_f32 v51, v51\n"
        "v_exp_f32 v52, v52\n"
        "v_exp_f32 v53, v53\n"
        "v_mfma_f32_32x32x16_bf16 v[32:47], v[136:139], v[128:131], v[64:79]\n"
        "ds_read_b64_tr_b16 v[204:205], v148 offset:12288\n"
        "ds_read_b64_tr_b16 v[206:207], v148 offset:12800\n"
        "ds_read_b64_tr_b16 v[208:209], v149 offset:12288\n"
        "v_exp_f32 v54, v54\n"
        "v_exp_f32 v55, v55\n"
        "v_exp_f32 v56, v56\n"
        "v_exp_f32 v57, v57\n"
        "v_exp_f32 v58, v58\n"
        "v_exp_f32 v59, v59\n"
        "v_smfmac_f32_32x32x32_bf16 v[112:127], v[140:143], v[48:55], v20\n"
        "ds_read_b64_tr_b16 v[210:211], v149 offset:12800\n"
        "s_lshl_b32 s64, s75, 11\n"
        "s_add_u32 s64, s64, s73\n"
        "buffer_store_dwordx4 v[196:199], v2, s[20:23], s64 offen offset:0 sc1\n"
        "s_lshl_b32 s64, s75, 11\n"
        "s_add_u32 s64, s64, s73\n"
        "buffer_store_dwordx4 v[200:203], v2, s[20:23], s64 offen offset:1024 sc1\n"
        "v_exp_f32 v60, v60\n"
        "v_exp_f32 v61, v61\n"
        "v_exp_f32 v62, v62\n"
        "v_exp_f32 v63, v63\n"
        "v_exp_f32 v32, v32\n"
        "v_exp_f32 v33, v33\n"
        "v_smfmac_f32_32x32x32_bf16 v[112:127], v[144:147], v[56:63], v20\n"
        "s_mov_b64 exec, s[60:61]\n"
        "ds_read_b128 v[144:147], v9 offset:24064\n"
        "ds_read_b128 v[140:143], v9 offset:32768\n"
        "s_mov_b64 exec, -1\n"
        "s_movk_i32 s7, 768\n"
        "buffer_load_dwordx4 v12, s[24:27], s7 offen offset:0 lds\n"
        "v_exp_f32 v34, v34\n"
        "v_exp_f32 v35, v35\n"
        "v_exp_f32 v36, v36\n"
        "v_exp_f32 v37, v37\n"
        "v_mfma_f32_32x32x16_bf16 v[48:63], v[136:139], v[132:135], v[80:95]\n"
        "ds_read_b128 v[136:139], v8 offset:26624\n"
        "s_waitcnt lgkmcnt(1)\n"
        "s_lshl_b32 s64, s76, 11\n"
        "s_add_u32 s64, s64, s73\n"
        "buffer_store_dwordx4 v[204:207], v2, s[20:23], s64 offen offset:0 sc1\n"
        "v_exp_f32 v38, v38\n"
        "v_exp_f32 v39, v39\n"
        "v_exp_f32 v40, v40\n"
        "v_exp_f32 v41, v41\n"
        "v_exp_f32 v42, v42\n"
        "v_exp_f32 v43, v43\n"
        "s_waitcnt vmcnt(9) lgkmcnt(0)\n"
        "v_smfmac_f32_32x32x32_bf16 v[96:111], v[252:255], v[32:39], v20\n"
        "ds_add_u32 v14, v11\n"
        "s_lshl_b32 s64, s76, 11\n"
        "s_add_u32 s64, s64, s73\n"
        "buffer_store_dwordx4 v[208:211], v2, s[20:23], s64 offen offset:1024 sc1\n"
        "v_exp_f32 v44, v44\n"
        "v_exp_f32 v45, v45\n"
        "v_exp_f32 v46, v46\n"
        "v_exp_f32 v47, v47\n"
        "v_exp_f32 v48, v48\n"
        "v_exp_f32 v49, v49\n"
        "v_smfmac_f32_32x32x32_bf16 v[96:111], v[144:147], v[40:47], v20\n"
        "ds_read_b32 v10, v14\n"
        "ds_read_b32 v24, v25\n"
        "v_exp_f32 v50, v50\n"
        "v_exp_f32 v51, v51\n"
        "v_exp_f32 v52, v52\n"
        "v_exp_f32 v53, v53\n"
        "v_mfma_f32_32x32x16_bf16 v[32:47], v[136:139], v[128:131], v[64:79]\n"
        "buffer_load_dwordx4 v13, s[24:27], s7 offen offset:1024 lds\n"
        "v_exp_f32 v54, v54\n"
        "v_exp_f32 v55, v55\n"
        "v_exp_f32 v56, v56\n"
        "v_exp_f32 v57, v57\n"
        "v_exp_f32 v58, v58\n"
        "v_exp_f32 v59, v59\n"
        "s_waitcnt lgkmcnt(0)\n"
        "v_cmp_gt_u32 vcc, 24, v10\n"
        "v_smfmac_f32_32x32x32_bf16 v[112:127], v[252:255], v[48:55], v20\n"
        "s_cbranch_vccnz 192f\n"
        "193:\n"
        "v_cmp_gt_u32 vcc, 24, v24\n"
        "s_setprio 3\n"
        "s_cbranch_vccz 194f\n"
        "s_setprio 2\n"
        "194:\n"
        "v_exp_f32 v60, v60\n"
        "v_exp_f32 v61, v61\n"
        "v_exp_f32 v62, v62\n"
        "v_exp_f32 v63, v63\n"
        "v_exp_f32 v32, v32\n"
        "v_exp_f32 v33, v33\n"
        "v_smfmac_f32_32x32x32_bf16 v[112:127], v[144:147], v[56:63], v20\n"
        "s_mov_b64 exec, s[60:61]\n"
        "ds_read_b128 v[144:147], v9 offset:33280\n"
        "ds_read_b128 v[252:255], v9 offset:35840\n"
        "s_mov_b64 exec, -1\n"
        "v_exp_f32 v34, v34\n"
        "v_exp_f32 v35, v35\n"
        "v_exp_f32 v36, v36\n"
        "v_exp_f32 v37, v37\n"
        "v_mfma_f32_32x32x16_bf16 v[48:63], v[136:139], v[132:135], v[80:95]\n"
        "ds_read_b128 v[136:139], v8 offset:29696\n"
        "s_add_u32 m0, s56, 0\n"
        "s_mov_b32 s63, 32768\n"
        "buffer_load_dwordx4 v15, s[20:23], s63 offen offset:2048 sc1 lds\n"
        "s_add_u32 m0, s56, 24576\n"
        "s_movk_i32 s7, 896\n"
        "buffer_load_dwordx4 v12, s[24:27], s7 offen offset:0 lds\n"
        "v_exp_f32 v38, v38\n"
        "v_exp_f32 v39, v39\n"
        "v_exp_f32 v40, v40\n"
        "v_exp_f32 v41, v41\n"
        "v_exp_f32 v42, v42\n"
        "v_exp_f32 v43, v43\n"
        "v_smfmac_f32_32x32x32_bf16 v[96:111], v[140:143], v[32:39], v20\n"
        "v_exp_f32 v44, v44\n"
        "v_exp_f32 v45, v45\n"
        "v_exp_f32 v46, v46\n"
        "v_exp_f32 v47, v47\n"
        "v_exp_f32 v48, v48\n"
        "v_exp_f32 v49, v49\n"
        "s_waitcnt lgkmcnt(0)\n"
        "v_smfmac_f32_32x32x32_bf16 v[96:111], v[144:147], v[40:47], v20\n"
        "v_exp_f32 v50, v50\n"
        "v_exp_f32 v51, v51\n"
        "v_exp_f32 v52, v52\n"
        "v_exp_f32 v53, v53\n"
        "v_mfma_f32_32x32x16_bf16 v[32:47], v[136:139], v[128:131], v[64:79]\n"
        "buffer_load_dwordx4 v13, s[24:27], s7 offen offset:1024 lds\n"
        "v_exp_f32 v54, v54\n"
        "v_exp_f32 v55, v55\n"
        "v_exp_f32 v56, v56\n"
        "v_exp_f32 v57, v57\n"
        "v_exp_f32 v58, v58\n"
        "v_exp_f32 v59, v59\n"
        "s_waitcnt vmcnt(13)\n"
        "v_smfmac_f32_32x32x32_bf16 v[112:127], v[140:143], v[48:55], v20\n"
        "ds_add_u32 v14, v11\n"
        "v_exp_f32 v60, v60\n"
        "v_exp_f32 v61, v61\n"
        "v_exp_f32 v62, v62\n"
        "v_exp_f32 v63, v63\n"
        "v_exp_f32 v32, v32\n"
        "v_exp_f32 v33, v33\n"
        "v_smfmac_f32_32x32x32_bf16 v[112:127], v[144:147], v[56:63], v20\n"
        "s_mov_b64 exec, s[60:61]\n"
        "ds_read_b128 v[144:147], v9 offset:36352\n"
        "ds_read_b128 v[140:143], v9 offset:45056\n"
        "s_mov_b64 exec, -1\n"
        "ds_read_b32 v10, v14\n"
        "ds_read_b32 v24, v25\n"
        "v_exp_f32 v34, v34\n"
        "v_exp_f32 v35, v35\n"
        "v_exp_f32 v36, v36\n"
        "v_exp_f32 v37, v37\n"
        "v_mfma_f32_32x32x16_bf16 v[48:63], v[136:139], v[132:135], v[80:95]\n"
        "ds_read_b128 v[136:139], v8 offset:38912\n"
        "s_movk_i32 s7, 1024\n"
        "buffer_load_dwordx4 v12, s[24:27], s7 offen offset:0 lds\n"
        "v_exp_f32 v38, v38\n"
        "v_exp_f32 v39, v39\n"
        "v_exp_f32 v40, v40\n"
        "v_exp_f32 v41, v41\n"
        "v_exp_f32 v42, v42\n"
        "v_exp_f32 v43, v43\n"
        "s_waitcnt lgkmcnt(1)\n"
        "v_cmp_gt_u32 vcc, 28, v10\n"
        "v_smfmac_f32_32x32x32_bf16 v[96:111], v[252:255], v[32:39], v20\n"
        "s_cbranch_vccnz 195f\n"
        "196:\n"
        "v_cmp_gt_u32 vcc, 28, v24\n"
        "s_setprio 3\n"
        "s_cbranch_vccz 197f\n"
        "s_setprio 2\n"
        "197:\n"
        "v_exp_f32 v44, v44\n"
        "v_exp_f32 v45, v45\n"
        "v_exp_f32 v46, v46\n"
        "v_exp_f32 v47, v47\n"
        "v_exp_f32 v48, v48\n"
        "v_exp_f32 v49, v49\n"
        "v_smfmac_f32_32x32x32_bf16 v[96:111], v[144:147], v[40:47], v20\n"
        "s_add_u32 m0, s56, 12288\n"
        "s_mov_b32 s63, 36864\n"
        "buffer_load_dwordx4 v15, s[20:23], s63 offen offset:2048 sc1 lds\n"
        "v_exp_f32 v50, v50\n"
        "v_exp_f32 v51, v51\n"
        "v_exp_f32 v52, v52\n"
        "v_exp_f32 v53, v53\n"
        "s_waitcnt lgkmcnt(0)\n"
        "v_mfma_f32_32x32x16_bf16 v[32:47], v[136:139], v[128:131], v[64:79]\n"
        "s_add_u32 m0, s56, 24576\n"
        "s_nop 0\n"
        "buffer_load_dwordx4 v13, s[24:27], s7 offen offset:1024 lds\n"
        "v_exp_f32 v54, v54\n"
        "v_exp_f32 v55, v55\n"
        "v_exp_f32 v56, v56\n"
        "v_exp_f32 v57, v57\n"
        "v_exp_f32 v58, v58\n"
        "v_exp_f32 v59, v59\n"
        "v_smfmac_f32_32x32x32_bf16 v[112:127], v[252:255], v[48:55], v20\n"
        "v_exp_f32 v60, v60\n"
        "v_exp_f32 v61, v61\n"
        "v_exp_f32 v62, v62\n"
        "v_exp_f32 v63, v63\n"
        "v_exp_f32 v32, v32\n"
        "v_exp_f32 v33, v33\n"
        "v_smfmac_f32_32x32x32_bf16 v[112:127], v[144:147], v[56:63], v20\n"
        "s_mov_b64 exec, s[60:61]\n"
        "ds_read_b128 v[144:147], v9 offset:45568\n"
        "ds_read_b128 v[252:255], v9 offset:48128\n"
        "s_mov_b64 exec, -1\n"
        "v_exp_f32 v34, v34\n"
        "v_exp_f32 v35, v35\n"
        "v_exp_f32 v36, v36\n"
        "v_exp_f32 v37, v37\n"
        "v_mfma_f32_32x32x16_bf16 v[48:63], v[136:139], v[132:135], v[80:95]\n"
        "ds_read_b128 v[136:139], v8 offset:41984\n"
        "s_movk_i32 s7, 1152\n"
        "buffer_load_dwordx4 v12, s[24:27], s7 offen offset:0 lds\n"
        "v_exp_f32 v38, v38\n"
        "v_exp_f32 v39, v39\n"
        "v_exp_f32 v40, v40\n"
        "v_exp_f32 v41, v41\n"
        "v_exp_f32 v42, v42\n"
        "v_exp_f32 v43, v43\n"
        "s_waitcnt vmcnt(8) lgkmcnt(1)\n"
        "v_smfmac_f32_32x32x32_bf16 v[96:111], v[140:143], v[32:39], v20\n"
        "ds_add_u32 v14, v11\n"
        "v_exp_f32 v44, v44\n"
        "v_exp_f32 v45, v45\n"
        "v_exp_f32 v46, v46\n"
        "v_exp_f32 v47, v47\n"
        "v_exp_f32 v48, v48\n"
        "v_exp_f32 v49, v49\n"
        "v_smfmac_f32_32x32x32_bf16 v[96:111], v[144:147], v[40:47], v20\n"
        "v_exp_f32 v50, v50\n"
        "v_exp_f32 v51, v51\n"
        "v_exp_f32 v52, v52\n"
        "v_exp_f32 v53, v53\n"
        "s_waitcnt lgkmcnt(0)\n"
        "v_mfma_f32_32x32x16_bf16 v[32:47], v[136:139], v[128:131], v[64:79]\n"
        "buffer_load_dwordx4 v13, s[24:27], s7 offen offset:1024 lds\n"
        "v_exp_f32 v54, v54\n"
        "v_exp_f32 v55, v55\n"
        "v_exp_f32 v56, v56\n"
        "v_exp_f32 v57, v57\n"
        "v_exp_f32 v58, v58\n"
        "v_exp_f32 v59, v59\n"
        "v_smfmac_f32_32x32x32_bf16 v[112:127], v[140:143], v[48:55], v20\n"
        "v_exp_f32 v60, v60\n"
        "v_exp_f32 v61, v61\n"
        "v_exp_f32 v62, v62\n"
        "v_exp_f32 v63, v63\n"
        "v_exp_f32 v32, v32\n"
        "v_exp_f32 v33, v33\n"
        "v_smfmac_f32_32x32x32_bf16 v[112:127], v[144:147], v[56:63], v20\n"
        "s_mov_b64 exec, s[60:61]\n"
        "ds_read_b128 v[144:147], v9 offset:48640\n"
        "ds_read_b128 v[140:143], v9 offset:57344\n"
        "s_mov_b64 exec, -1\n"
        "ds_read_b32 v10, v14\n"
        "ds_read_b32 v24, v25\n"
        "v_exp_f32 v34, v34\n"
        "v_exp_f32 v35, v35\n"
        "v_exp_f32 v36, v36\n"
        "v_exp_f32 v37, v37\n"
        "v_mfma_f32_32x32x16_bf16 v[48:63], v[136:139], v[132:135], v[80:95]\n"
        "ds_read_b128 v[136:139], v8 offset:51200\n"
        "s_movk_i32 s7, 1280\n"
        "buffer_load_dwordx4 v12, s[24:27], s7 offen offset:0 lds\n"
        "v_exp_f32 v38, v38\n"
        "v_exp_f32 v39, v39\n"
        "v_exp_f32 v40, v40\n"
        "v_exp_f32 v41, v41\n"
        "v_exp_f32 v42, v42\n"
        "v_exp_f32 v43, v43\n"
        "s_waitcnt lgkmcnt(1)\n"
        "v_cmp_gt_u32 vcc, 32, v10\n"
        "v_smfmac_f32_32x32x32_bf16 v[96:111], v[252:255], v[32:39], v20\n"
        "s_cbranch_vccnz 198f\n"
        "199:\n"
        "v_cmp_gt_u32 vcc, 32, v24\n"
        "s_setprio 3\n"
        "s_cbranch_vccz 200f\n"
        "s_setprio 2\n"
        "200:\n"
        "v_exp_f32 v44, v44\n"
        "v_exp_f32 v45, v45\n"
        "v_exp_f32 v46, v46\n"
        "v_exp_f32 v47, v47\n"
        "v_exp_f32 v48, v48\n"
        "v_exp_f32 v49, v49\n"
        "v_smfmac_f32_32x32x32_bf16 v[96:111], v[144:147], v[40:47], v20\n"
        "buffer_store_dword v3, v27, s[20:23], 0 offen sc1\n"
        "v_exp_f32 v50, v50\n"
        "v_exp_f32 v51, v51\n"
        "v_exp_f32 v52, v52\n"
        "v_exp_f32 v53, v53\n"
        "s_waitcnt lgkmcnt(0)\n"
        "v_mfma_f32_32x32x16_bf16 v[32:47], v[136:139], v[128:131], v[64:79]\n"
        "s_add_u32 m0, s56, 24576\n"
        "s_mov_b32 s63, 40960\n"
        "buffer_load_dwordx4 v15, s[20:23], s63 offen offset:2048 sc1 lds\n"
        "s_add_u32 m0, s56, 24576\n"
        "s_nop 0\n"
        "buffer_load_dwordx4 v13, s[24:27], s7 offen offset:1024 lds\n"
        "v_exp_f32 v54, v54\n"
        "v_exp_f32 v55, v55\n"
        "v_exp_f32 v56, v56\n"
        "v_exp_f32 v57, v57\n"
        "v_exp_f32 v58, v58\n"
        "v_exp_f32 v59, v59\n"
        "s_waitcnt vmcnt(11)\n"
        "v_smfmac_f32_32x32x32_bf16 v[112:127], v[252:255], v[48:55], v20\n"
        "ds_add_u32 v14, v11\n"
        "v_exp_f32 v60, v60\n"
        "v_exp_f32 v61, v61\n"
        "v_exp_f32 v62, v62\n"
        "v_exp_f32 v63, v63\n"
        "v_exp_f32 v32, v32\n"
        "v_exp_f32 v33, v33\n"
        "v_smfmac_f32_32x32x32_bf16 v[112:127], v[144:147], v[56:63], v20\n"
        "s_mov_b64 exec, s[60:61]\n"
        "ds_read_b128 v[144:147], v9 offset:57856\n"
        "ds_read_b128 v[252:255], v9 offset:60416\n"
        "s_mov_b64 exec, -1\n"
        "v_exp_f32 v34, v34\n"
        "v_exp_f32 v35, v35\n"
        "v_exp_f32 v36, v36\n"
        "v_exp_f32 v37, v37\n"
        "v_mfma_f32_32x32x16_bf16 v[48:63], v[136:139], v[132:135], v[80:95]\n"
        "ds_read_b128 v[136:139], v8 offset:54272\n"
        "s_movk_i32 s7, 1408\n"
        "buffer_load_dwordx4 v12, s[24:27], s7 offen offset:0 lds\n"
        "v_exp_f32 v38, v38\n"
        "v_exp_f32 v39, v39\n"
        "v_exp_f32 v40, v40\n"
        "v_exp_f32 v41, v41\n"
        "v_exp_f32 v42, v42\n"
        "v_exp_f32 v43, v43\n"
        "v_smfmac_f32_32x32x32_bf16 v[96:111], v[140:143], v[32:39], v20\n"
        "v_exp_f32 v44, v44\n"
        "v_exp_f32 v45, v45\n"
        "v_exp_f32 v46, v46\n"
        "v_exp_f32 v47, v47\n"
        "v_exp_f32 v48, v48\n"
        "v_exp_f32 v49, v49\n"
        "s_waitcnt lgkmcnt(0)\n"
        "v_smfmac_f32_32x32x32_bf16 v[96:111], v[144:147], v[40:47], v20\n"
        "ds_read_b32 v10, v14\n"
        "ds_read_b32 v24, v25\n"
        "v_exp_f32 v50, v50\n"
        "v_exp_f32 v51, v51\n"
        "v_exp_f32 v52, v52\n"
        "v_exp_f32 v53, v53\n"
        "v_mfma_f32_32x32x16_bf16 v[32:47], v[136:139], v[128:131], v[64:79]\n"
        "buffer_load_dwordx4 v13, s[24:27], s7 offen offset:1024 lds\n"
        "v_exp_f32 v54, v54\n"
        "v_exp_f32 v55, v55\n"
        "v_exp_f32 v56, v56\n"
        "v_exp_f32 v57, v57\n"
        "v_exp_f32 v58, v58\n"
        "v_exp_f32 v59, v59\n"
        "s_waitcnt lgkmcnt(0)\n"
        "v_cmp_gt_u32 vcc, 36, v10\n"
        "v_smfmac_f32_32x32x32_bf16 v[112:127], v[140:143], v[48:55], v20\n"
        "s_cbranch_vccnz 201f\n"
        "202:\n"
        "v_cmp_gt_u32 vcc, 36, v24\n"
        "s_setprio 3\n"
        "s_cbranch_vccz 203f\n"
        "s_setprio 2\n"
        "203:\n"
        "v_exp_f32 v60, v60\n"
        "v_exp_f32 v61, v61\n"
        "v_exp_f32 v62, v62\n"
        "v_exp_f32 v63, v63\n"
        "v_exp_f32 v32, v32\n"
        "v_exp_f32 v33, v33\n"
        "v_smfmac_f32_32x32x32_bf16 v[112:127], v[144:147], v[56:63], v20\n"
        "s_mov_b64 exec, s[60:61]\n"
        "ds_read_b128 v[144:147], v9 offset:60928\n"
        "ds_read_b128 v[140:143], v9 offset:8192\n"
        "s_mov_b64 exec, -1\n"
        "v_exp_f32 v34, v34\n"
        "v_exp_f32 v35, v35\n"
        "v_exp_f32 v36, v36\n"
        "v_exp_f32 v37, v37\n"
        "v_mfma_f32_32x32x16_bf16 v[48:63], v[136:139], v[132:135], v[80:95]\n"
        "ds_read_b128 v[136:139], v8 offset:2048\n"
        "s_add_u32 m0, s56, 36864\n"
        "s_mov_b32 s63, 45056\n"
        "buffer_load_dwordx4 v15, s[20:23], s63 offen offset:2048 sc1 lds\n"
        "s_add_u32 m0, s56, 24576\n"
        "s_movk_i32 s7, 1536\n"
        "buffer_load_dwordx4 v12, s[24:27], s7 offen offset:0 lds\n"
        "v_exp_f32 v38, v38\n"
        "v_exp_f32 v39, v39\n"
        "v_exp_f32 v40, v40\n"
        "v_exp_f32 v41, v41\n"
        "v_exp_f32 v42, v42\n"
        "v_exp_f32 v43, v43\n"
        "v_smfmac_f32_32x32x32_bf16 v[96:111], v[252:255], v[32:39], v20\n"
        "v_exp_f32 v44, v44\n"
        "v_exp_f32 v45, v45\n"
        "v_exp_f32 v46, v46\n"
        "v_exp_f32 v47, v47\n"
        "v_exp_f32 v48, v48\n"
        "v_exp_f32 v49, v49\n"
        "s_waitcnt lgkmcnt(0)\n"
        "v_smfmac_f32_32x32x32_bf16 v[96:111], v[144:147], v[40:47], v20\n"
        "v_exp_f32 v50, v50\n"
        "v_exp_f32 v51, v51\n"
        "v_exp_f32 v52, v52\n"
        "v_exp_f32 v53, v53\n"
        "v_mfma_f32_32x32x16_bf16 v[32:47], v[136:139], v[128:131], v[64:79]\n"
        "buffer_load_dwordx4 v13, s[24:27], s7 offen offset:1024 lds\n"
        "v_exp_f32 v54, v54\n"
        "v_exp_f32 v55, v55\n"
        "v_exp_f32 v56, v56\n"
        "v_exp_f32 v57, v57\n"
        "v_exp_f32 v58, v58\n"
        "v_exp_f32 v59, v59\n"
        "s_waitcnt vmcnt(12)\n"
        "v_smfmac_f32_32x32x32_bf16 v[112:127], v[252:255], v[48:55], v20\n"
        "ds_add_u32 v14, v11\n"
        "v_exp_f32 v60, v60\n"
        "v_exp_f32 v61, v61\n"
        "v_exp_f32 v62, v62\n"
        "v_exp_f32 v63, v63\n"
        "v_exp_f32 v32, v32\n"
        "v_exp_f32 v33, v33\n"
        "v_smfmac_f32_32x32x32_bf16 v[112:127], v[144:147], v[56:63], v20\n"
        "s_mov_b64 exec, s[60:61]\n"
        "ds_read_b128 v[144:147], v9 offset:8704\n"
        "ds_read_b128 v[252:255], v9 offset:11264\n"
        "s_mov_b64 exec, -1\n"
        "ds_read_b32 v10, v14\n"
        "ds_read_b32 v24, v25\n"
        "v_exp_f32 v34, v34\n"
        "v_exp_f32 v35, v35\n"
        "v_exp_f32 v36, v36\n"
        "v_exp_f32 v37, v37\n"
        "v_mfma_f32_32x32x16_bf16 v[48:63], v[136:139], v[132:135], v[80:95]\n"
        "ds_read_b128 v[136:139], v8 offset:5120\n"
        "v_exp_f32 v38, v38\n"
        "v_exp_f32 v39, v39\n"
        "v_exp_f32 v40, v40\n"
        "v_exp_f32 v41, v41\n"
        "v_exp_f32 v42, v42\n"
        "v_exp_f32 v43, v43\n"
        "s_waitcnt lgkmcnt(1)\n"
        "v_cmp_gt_u32 vcc, 40, v10\n"
        "v_smfmac_f32_32x32x32_bf16 v[96:111], v[140:143], v[32:39], v20\n"
        "s_cbranch_vccnz 204f\n"
        "205:\n"
        "v_cmp_gt_u32 vcc, 40, v24\n"
        "s_setprio 3\n"
        "s_cbranch_vccz 206f\n"
        "s_setprio 2\n"
        "206:\n"
        "v_exp_f32 v44, v44\n"
        "v_exp_f32 v45, v45\n"
        "v_exp_f32 v46, v46\n"
        "v_exp_f32 v47, v47\n"
        "v_exp_f32 v48, v48\n"
        "v_exp_f32 v49, v49\n"
        "v_smfmac_f32_32x32x32_bf16 v[96:111], v[144:147], v[40:47], v20\n"
        "s_add_u32 m0, s56, 49152\n"
        "s_mov_b32 s63, 49152\n"
        "buffer_load_dwordx4 v15, s[20:23], s63 offen offset:2048 sc1 lds\n"
        "v_exp_f32 v50, v50\n"
        "v_exp_f32 v51, v51\n"
        "v_exp_f32 v52, v52\n"
        "v_exp_f32 v53, v53\n"
        "s_waitcnt lgkmcnt(0)\n"
        "v_mfma_f32_32x32x16_bf16 v[32:47], v[136:139], v[128:131], v[64:79]\n"
        "v_exp_f32 v54, v54\n"
        "v_exp_f32 v55, v55\n"
        "v_exp_f32 v56, v56\n"
        "v_exp_f32 v57, v57\n"
        "v_exp_f32 v58, v58\n"
        "v_exp_f32 v59, v59\n"
        "v_smfmac_f32_32x32x32_bf16 v[112:127], v[140:143], v[48:55], v20\n"
        "v_exp_f32 v60, v60\n"
        "v_exp_f32 v61, v61\n"
        "v_exp_f32 v62, v62\n"
        "v_exp_f32 v63, v63\n"
        "s_nop 0\n"
        "v_exp_f32 v32, v32\n"
        "v_exp_f32 v33, v33\n"
        "v_smfmac_f32_32x32x32_bf16 v[112:127], v[144:147], v[56:63], v20\n"
        "s_mov_b64 exec, s[60:61]\n"
        "ds_read_b128 v[144:147], v9 offset:11776\n"
        "ds_read_b128 v[140:143], v9 offset:20480\n"
        "s_mov_b64 exec, -1\n"
        "v_exp_f32 v34, v34\n"
        "v_exp_f32 v35, v35\n"
        "v_exp_f32 v36, v36\n"
        "v_exp_f32 v37, v37\n"
        "v_mfma_f32_32x32x16_bf16 v[48:63], v[136:139], v[132:135], v[80:95]\n"
        "ds_read_b128 v[136:139], v8 offset:14336\n"
        "v_exp_f32 v38, v38\n"
        "v_exp_f32 v39, v39\n"
        "v_exp_f32 v40, v40\n"
        "v_exp_f32 v41, v41\n"
        "v_exp_f32 v42, v42\n"
        "v_exp_f32 v43, v43\n"
        "s_waitcnt vmcnt(7) lgkmcnt(1)\n"
        "v_smfmac_f32_32x32x32_bf16 v[96:111], v[252:255], v[32:39], v20\n"
        "ds_add_u32 v14, v11\n"
        "v_exp_f32 v44, v44\n"
        "v_exp_f32 v45, v45\n"
        "v_exp_f32 v46, v46\n"
        "v_exp_f32 v47, v47\n"
        "v_exp_f32 v48, v48\n"
        "v_exp_f32 v49, v49\n"
        "v_smfmac_f32_32x32x32_bf16 v[96:111], v[144:147], v[40:47], v20\n"
        "ds_read_b32 v10, v14\n"
        "ds_read_b32 v24, v25\n"
        "v_exp_f32 v50, v50\n"
        "v_exp_f32 v51, v51\n"
        "v_exp_f32 v52, v52\n"
        "v_exp_f32 v53, v53\n"
        "s_waitcnt lgkmcnt(2)\n"
        "v_mfma_f32_32x32x16_bf16 v[32:47], v[136:139], v[128:131], v[64:79]\n"
        "v_exp_f32 v54, v54\n"
        "v_exp_f32 v55, v55\n"
        "v_exp_f32 v56, v56\n"
        "v_exp_f32 v57, v57\n"
        "v_exp_f32 v58, v58\n"
        "v_exp_f32 v59, v59\n"
        "s_waitcnt lgkmcnt(0)\n"
        "v_cmp_gt_u32 vcc, 44, v10\n"
        "v_smfmac_f32_32x32x32_bf16 v[112:127], v[252:255], v[48:55], v20\n"
        "s_cbranch_vccnz 207f\n"
        "208:\n"
        "v_cmp_gt_u32 vcc, 44, v24\n"
        "s_setprio 3\n"
        "s_cbranch_vccz 209f\n"
        "s_setprio 2\n"
        "209:\n"
        "v_exp_f32 v60, v60\n"
        "v_exp_f32 v61, v61\n"
        "v_exp_f32 v62, v62\n"
        "v_exp_f32 v63, v63\n"
        "v_exp_f32 v32, v32\n"
        "v_exp_f32 v33, v33\n"
        "v_smfmac_f32_32x32x32_bf16 v[112:127], v[144:147], v[56:63], v20\n"
        "s_mov_b64 exec, s[60:61]\n"
        "ds_read_b128 v[144:147], v9 offset:20992\n"
        "ds_read_b128 v[252:255], v9 offset:23552\n"
        "s_mov_b64 exec, -1\n"
        "v_exp_f32 v34, v34\n"
        "v_exp_f32 v35, v35\n"
        "v_exp_f32 v36, v36\n"
        "v_exp_f32 v37, v37\n"
        "v_mfma_f32_32x32x16_bf16 v[48:63], v[136:139], v[132:135], v[80:95]\n"
        "ds_read_b128 v[136:139], v8 offset:17408\n"
        "v_exp_f32 v38, v38\n"
        "v_exp_f32 v39, v39\n"
        "v_exp_f32 v40, v40\n"
        "v_exp_f32 v41, v41\n"
        "v_exp_f32 v42, v42\n"
        "v_exp_f32 v43, v43\n"
        "v_smfmac_f32_32x32x32_bf16 v[96:111], v[140:143], v[32:39], v20\n"
        "v_exp_f32 v44, v44\n"
        "v_exp_f32 v45, v45\n"
        "v_exp_f32 v46, v46\n"
        "v_exp_f32 v47, v47\n"
        "v_exp_f32 v48, v48\n"
        "v_exp_f32 v49, v49\n"
        "s_waitcnt lgkmcnt(0)\n"
        "v_smfmac_f32_32x32x32_bf16 v[96:111], v[144:147], v[40:47], v20\n"
        "v_exp_f32 v50, v50\n"
        "v_exp_f32 v51, v51\n"
        "v_exp_f32 v52, v52\n"
        "v_exp_f32 v53, v53\n"
        "v_mfma_f32_32x32x16_bf16 v[32:47], v[136:139], v[128:131], v[64:79]\n"
        "v_exp_f32 v54, v54\n"
        "v_exp_f32 v55, v55\n"
        "v_exp_f32 v56, v56\n"
        "v_exp_f32 v57, v57\n"
        "v_exp_f32 v58, v58\n"
        "v_exp_f32 v59, v59\n"
        "s_waitcnt vmcnt(3)\n"
        "v_smfmac_f32_32x32x32_bf16 v[112:127], v[140:143], v[48:55], v20\n"
        "ds_add_u32 v14, v11\n"
        "v_exp_f32 v60, v60\n"
        "v_exp_f32 v61, v61\n"
        "v_exp_f32 v62, v62\n"
        "v_exp_f32 v63, v63\n"
        "v_exp_f32 v32, v32\n"
        "v_exp_f32 v33, v33\n"
        "v_smfmac_f32_32x32x32_bf16 v[112:127], v[144:147], v[56:63], v20\n"
        "s_mov_b64 exec, s[60:61]\n"
        "ds_read_b128 v[144:147], v9 offset:24064\n"
        "ds_read_b128 v[140:143], v9 offset:32768\n"
        "s_mov_b64 exec, -1\n"
        "v_exp_f32 v34, v34\n"
        "v_exp_f32 v35, v35\n"
        "v_exp_f32 v36, v36\n"
        "v_exp_f32 v37, v37\n"
        "v_mfma_f32_32x32x16_bf16 v[48:63], v[136:139], v[132:135], v[80:95]\n"
        "ds_read_b128 v[136:139], v8 offset:26624\n"
        "v_exp_f32 v38, v38\n"
        "v_exp_f32 v39, v39\n"
        "v_exp_f32 v40, v40\n"
        "v_exp_f32 v41, v41\n"
        "v_exp_f32 v42, v42\n"
        "v_exp_f32 v43, v43\n"
        "v_smfmac_f32_32x32x32_bf16 v[96:111], v[252:255], v[32:39], v20\n"
        "v_exp_f32 v44, v44\n"
        "v_exp_f32 v45, v45\n"
        "v_exp_f32 v46, v46\n"
        "v_exp_f32 v47, v47\n"
        "v_exp_f32 v48, v48\n"
        "v_exp_f32 v49, v49\n"
        "s_waitcnt lgkmcnt(0)\n"
        "v_smfmac_f32_32x32x32_bf16 v[96:111], v[144:147], v[40:47], v20\n"
        "ds_read_b32 v10, v14\n"
        "ds_read_b32 v24, v25\n"
        "v_exp_f32 v50, v50\n"
        "v_exp_f32 v51, v51\n"
        "v_exp_f32 v52, v52\n"
        "v_exp_f32 v53, v53\n"
        "v_mfma_f32_32x32x16_bf16 v[32:47], v[136:139], v[128:131], v[64:79]\n"
        "v_exp_f32 v54, v54\n"
        "v_exp_f32 v55, v55\n"
        "v_exp_f32 v56, v56\n"
        "v_exp_f32 v57, v57\n"
        "v_exp_f32 v58, v58\n"
        "v_exp_f32 v59, v59\n"
        "s_waitcnt lgkmcnt(0)\n"
        "v_cmp_gt_u32 vcc, 48, v10\n"
        "v_smfmac_f32_32x32x32_bf16 v[112:127], v[252:255], v[48:55], v20\n"
        "s_cbranch_vccnz 210f\n"
        "211:\n"
        "v_cmp_gt_u32 vcc, 48, v24\n"
        "s_setprio 3\n"
        "s_cbranch_vccz 212f\n"
        "s_setprio 2\n"
        "212:\n"
        "v_exp_f32 v60, v60\n"
        "v_exp_f32 v61, v61\n"
        "v_exp_f32 v62, v62\n"
        "v_exp_f32 v63, v63\n"
        "v_exp_f32 v32, v32\n"
        "v_exp_f32 v33, v33\n"
        "v_smfmac_f32_32x32x32_bf16 v[112:127], v[144:147], v[56:63], v20\n"
        "s_mov_b64 exec, s[60:61]\n"
        "ds_read_b128 v[144:147], v9 offset:33280\n"
        "ds_read_b128 v[252:255], v9 offset:35840\n"
        "s_mov_b64 exec, -1\n"
        "v_exp_f32 v34, v34\n"
        "v_exp_f32 v35, v35\n"
        "v_exp_f32 v36, v36\n"
        "v_exp_f32 v37, v37\n"
        "v_mfma_f32_32x32x16_bf16 v[48:63], v[136:139], v[132:135], v[80:95]\n"
        "ds_read_b128 v[136:139], v8 offset:29696\n"
        "s_add_u32 m0, s56, 0\n"
        "s_mov_b32 s63, 53248\n"
        "buffer_load_dwordx4 v15, s[20:23], s63 offen offset:2048 sc1 lds\n"
        "v_exp_f32 v38, v38\n"
        "v_exp_f32 v39, v39\n"
        "v_exp_f32 v40, v40\n"
        "v_exp_f32 v41, v41\n"
        "v_exp_f32 v42, v42\n"
        "v_exp_f32 v43, v43\n"
        "v_smfmac_f32_32x32x32_bf16 v[96:111], v[140:143], v[32:39], v20\n"
        "ds_add_u32 v14, v11\n"
        "v_exp_f32 v44, v44\n"
        "v_exp_f32 v45, v45\n"
        "v_exp_f32 v46, v46\n"
        "v_exp_f32 v47, v47\n"
        "v_exp_f32 v48, v48\n"
        "v_exp_f32 v49, v49\n"
        "s_waitcnt lgkmcnt(1)\n"
        "v_smfmac_f32_32x32x32_bf16 v[96:111], v[144:147], v[40:47], v20\n"
        "buffer_load_dword v21, v22, s[20:23], 0 offen sc1\n"
        "v_exp_f32 v50, v50\n"
        "v_exp_f32 v51, v51\n"
        "v_exp_f32 v52, v52\n"
        "v_exp_f32 v53, v53\n"
        "v_mfma_f32_32x32x16_bf16 v[32:47], v[136:139], v[128:131], v[64:79]\n"
        "v_exp_f32 v54, v54\n"
        "v_exp_f32 v55, v55\n"
        "v_exp_f32 v56, v56\n"
        "v_exp_f32 v57, v57\n"
        "v_exp_f32 v58, v58\n"
        "v_exp_f32 v59, v59\n"
        "v_smfmac_f32_32x32x32_bf16 v[112:127], v[140:143], v[48:55], v20\n"
        "v_exp_f32 v60, v60\n"
        "v_exp_f32 v61, v61\n"
        "v_exp_f32 v62, v62\n"
        "v_exp_f32 v63, v63\n"
        "s_nop 0\n"
        "v_exp_f32 v32, v32\n"
        "v_exp_f32 v33, v33\n"
        "v_smfmac_f32_32x32x32_bf16 v[112:127], v[144:147], v[56:63], v20\n"
        "s_mov_b64 exec, s[60:61]\n"
        "ds_read_b128 v[144:147], v9 offset:36352\n"
        "ds_read_b128 v[140:143], v9 offset:45056\n"
        "s_mov_b64 exec, -1\n"
        "ds_read_b32 v10, v14\n"
        "ds_read_b32 v24, v25\n"
        "v_exp_f32 v34, v34\n"
        "v_exp_f32 v35, v35\n"
        "v_exp_f32 v36, v36\n"
        "v_exp_f32 v37, v37\n"
        "v_mfma_f32_32x32x16_bf16 v[48:63], v[136:139], v[132:135], v[80:95]\n"
        "ds_read_b128 v[136:139], v8 offset:38912\n"
        "v_exp_f32 v38, v38\n"
        "v_exp_f32 v39, v39\n"
        "v_exp_f32 v40, v40\n"
        "v_exp_f32 v41, v41\n"
        "v_exp_f32 v42, v42\n"
        "v_exp_f32 v43, v43\n"
        "s_waitcnt lgkmcnt(1)\n"
        "v_cmp_gt_u32 vcc, 52, v10\n"
        "v_smfmac_f32_32x32x32_bf16 v[96:111], v[252:255], v[32:39], v20\n"
        "s_cbranch_vccnz 213f\n"
        "214:\n"
        "v_cmp_gt_u32 vcc, 52, v24\n"
        "s_setprio 3\n"
        "s_cbranch_vccz 215f\n"
        "s_setprio 2\n"
        "215:\n"
        "v_exp_f32 v44, v44\n"
        "v_exp_f32 v45, v45\n"
        "v_exp_f32 v46, v46\n"
        "v_exp_f32 v47, v47\n"
        "v_exp_f32 v48, v48\n"
        "v_exp_f32 v49, v49\n"
        "v_smfmac_f32_32x32x32_bf16 v[96:111], v[144:147], v[40:47], v20\n"
        "s_add_u32 m0, s56, 12288\n"
        "s_mov_b32 s63, 57344\n"
        "buffer_load_dwordx4 v15, s[20:23], s63 offen offset:2048 sc1 lds\n"
        "v_exp_f32 v50, v50\n"
        "v_exp_f32 v51, v51\n"
        "v_exp_f32 v52, v52\n"
        "v_exp_f32 v53, v53\n"
        "s_waitcnt lgkmcnt(0)\n"
        "v_mfma_f32_32x32x16_bf16 v[32:47], v[136:139], v[128:131], v[64:79]\n"
        "v_exp_f32 v54, v54\n"
        "v_exp_f32 v55, v55\n"
        "v_exp_f32 v56, v56\n"
        "v_exp_f32 v57, v57\n"
        "v_exp_f32 v58, v58\n"
        "v_exp_f32 v59, v59\n"
        "v_smfmac_f32_32x32x32_bf16 v[112:127], v[252:255], v[48:55], v20\n"
        "v_exp_f32 v60, v60\n"
        "v_exp_f32 v61, v61\n"
        "v_exp_f32 v62, v62\n"
        "v_exp_f32 v63, v63\n"
        "s_nop 0\n"
        "v_exp_f32 v32, v32\n"
        "v_exp_f32 v33, v33\n"
        "v_smfmac_f32_32x32x32_bf16 v[112:127], v[144:147], v[56:63], v20\n"
        "s_mov_b64 exec, s[60:61]\n"
        "ds_read_b128 v[144:147], v9 offset:45568\n"
        "ds_read_b128 v[252:255], v9 offset:48128\n"
        "s_mov_b64 exec, -1\n"
        "v_exp_f32 v34, v34\n"
        "v_exp_f32 v35, v35\n"
        "v_exp_f32 v36, v36\n"
        "v_exp_f32 v37, v37\n"
        "v_mfma_f32_32x32x16_bf16 v[48:63], v[136:139], v[132:135], v[80:95]\n"
        "ds_read_b128 v[136:139], v8 offset:41984\n"
        "v_exp_f32 v38, v38\n"
        "v_exp_f32 v39, v39\n"
        "v_exp_f32 v40, v40\n"
        "v_exp_f32 v41, v41\n"
        "v_exp_f32 v42, v42\n"
        "v_exp_f32 v43, v43\n"
        "s_waitcnt vmcnt(1) lgkmcnt(1)\n"
        "v_smfmac_f32_32x32x32_bf16 v[96:111], v[140:143], v[32:39], v20\n"
        "ds_add_u32 v14, v11\n"
        "v_exp_f32 v44, v44\n"
        "v_exp_f32 v45, v45\n"
        "v_exp_f32 v46, v46\n"
        "v_exp_f32 v47, v47\n"
        "v_exp_f32 v48, v48\n"
        "v_exp_f32 v49, v49\n"
        "v_smfmac_f32_32x32x32_bf16 v[96:111], v[144:147], v[40:47], v20\n"
        "ds_read_b32 v10, v14\n"
        "ds_read_b32 v24, v25\n"
        "v_exp_f32 v50, v50\n"
        "v_exp_f32 v51, v51\n"
        "v_exp_f32 v52, v52\n"
        "v_exp_f32 v53, v53\n"
        "s_waitcnt lgkmcnt(2)\n"
        "v_mfma_f32_32x32x16_bf16 v[32:47], v[136:139], v[128:131], v[64:79]\n"
        "v_exp_f32 v54, v54\n"
        "v_exp_f32 v55, v55\n"
        "v_exp_f32 v56, v56\n"
        "v_exp_f32 v57, v57\n"
        "v_exp_f32 v58, v58\n"
        "v_exp_f32 v59, v59\n"
        "s_waitcnt lgkmcnt(0)\n"
        "v_cmp_gt_u32 vcc, 56, v10\n"
        "v_smfmac_f32_32x32x32_bf16 v[112:127], v[140:143], v[48:55], v20\n"
        "s_cbranch_vccnz 216f\n"
        "217:\n"
        "v_cmp_gt_u32 vcc, 56, v24\n"
        "s_setprio 3\n"
        "s_cbranch_vccz 218f\n"
        "s_setprio 2\n"
        "218:\n"
        "v_exp_f32 v60, v60\n"
        "v_exp_f32 v61, v61\n"
        "v_exp_f32 v62, v62\n"
        "v_exp_f32 v63, v63\n"
        "v_exp_f32 v32, v32\n"
        "v_exp_f32 v33, v33\n"
        "v_smfmac_f32_32x32x32_bf16 v[112:127], v[144:147], v[56:63], v20\n"
        "s_mov_b64 exec, s[60:61]\n"
        "ds_read_b128 v[144:147], v9 offset:48640\n"
        "ds_read_b128 v[140:143], v9 offset:57344\n"
        "s_mov_b64 exec, -1\n"
        "s_lshl3_add_u32 s64, s5, s48\n"
        "s_lshl_b32 s64, s64, 13\n"
        "v_exp_f32 v34, v34\n"
        "v_exp_f32 v35, v35\n"
        "v_exp_f32 v36, v36\n"
        "v_exp_f32 v37, v37\n"
        "v_mfma_f32_32x32x16_bf16 v[48:63], v[136:139], v[132:135], v[80:95]\n"
        "ds_read_b128 v[136:139], v8 offset:51200\n"
        "s_add_u32 m0, s56, 24576\n"
        "s_mov_b32 s63, 61440\n"
        "buffer_load_dwordx4 v15, s[20:23], s63 offen offset:2048 sc1 lds\n"
        "s_lshr_b32 s65, s50, 2\n"
        "s_lshl_b32 s65, s65, 7\n"
        "v_exp_f32 v38, v38\n"
        "v_exp_f32 v39, v39\n"
        "v_exp_f32 v40, v40\n"
        "v_exp_f32 v41, v41\n"
        "v_exp_f32 v42, v42\n"
        "v_exp_f32 v43, v43\n"
        "v_cmp_ne_u32 vcc, v21, v3\n"
        "v_smfmac_f32_32x32x32_bf16 v[96:111], v[252:255], v[32:39], v20\n"
        "s_cbranch_vccnz 219f\n"
        "220:\n"
        "s_add_u32 s64, s64, s65\n"
        "v_exp_f32 v44, v44\n"
        "v_exp_f32 v45, v45\n"
        "v_exp_f32 v46, v46\n"
        "v_exp_f32 v47, v47\n"
        "v_exp_f32 v48, v48\n"
        "v_exp_f32 v49, v49\n"
        "s_waitcnt lgkmcnt(0)\n"
        "v_smfmac_f32_32x32x32_bf16 v[96:111], v[144:147], v[40:47], v20\n"
        "s_add_u32 s69, s44, 0\n"
        "buffer_load_dwordx4 v[196:199], v2, s[20:23], s69 offen offset:0 sc1\n"
        "v_and_b32 v31, 7, v1\n"
        "v_lshl_add_u32 v30, v31, 4, s64\n"
        "v_exp_f32 v50, v50\n"
        "v_exp_f32 v51, v51\n"
        "v_exp_f32 v52, v52\n"
        "v_exp_f32 v53, v53\n"
        "v_mfma_f32_32x32x16_bf16 v[32:47], v[136:139], v[128:131], v[64:79]\n"
        "buffer_load_dwordx4 v[200:203], v2, s[20:23], s69 offen offset:1024 sc1\n"
        "v_lshrrev_b32 v16, 3, v1\n"
        "v_exp_f32 v54, v54\n"
        "v_exp_f32 v55, v55\n"
        "v_exp_f32 v56, v56\n"
        "v_exp_f32 v57, v57\n"
        "v_exp_f32 v58, v58\n"
        "v_exp_f32 v59, v59\n"
        "v_smfmac_f32_32x32x32_bf16 v[112:127], v[252:255], v[48:55], v20\n"
        "ds_add_u32 v14, v11\n"
        "buffer_load_dwordx4 v[204:207], v2, s[20:23], s69 offen offset:2048 sc1\n"
        "v_lshl_add_u32 v30, v16, 10, v30\n"
        "v_exp_f32 v60, v60\n"
        "v_exp_f32 v61, v61\n"
        "v_exp_f32 v62, v62\n"
        "v_exp_f32 v63, v63\n"
        "v_exp_f32 v32, v32\n"
        "v_exp_f32 v33, v33\n"
        "v_smfmac_f32_32x32x32_bf16 v[112:127], v[144:147], v[56:63], v20\n"
        "s_mov_b64 exec, s[60:61]\n"
        "ds_read_b128 v[144:147], v9 offset:57856\n"
        "ds_read_b128 v[252:255], v9 offset:60416\n"
        "s_mov_b64 exec, -1\n"
        "ds_read_b32 v10, v14\n"
        "ds_read_b32 v24, v25\n"
        "v_exp_f32 v34, v34\n"
        "v_exp_f32 v35, v35\n"
        "v_exp_f32 v36, v36\n"
        "v_exp_f32 v37, v37\n"
        "v_mfma_f32_32x32x16_bf16 v[48:63], v[136:139], v[132:135], v[80:95]\n"
        "ds_read_b128 v[136:139], v8 offset:54272\n"
        "buffer_load_dwordx4 v[16:19], v30, s[8:11], 0 offen\n"
        "v_exp_f32 v38, v38\n"
        "v_exp_f32 v39, v39\n"
        "v_exp_f32 v40, v40\n"
        "v_exp_f32 v41, v41\n"
        "v_exp_f32 v42, v42\n"
        "v_exp_f32 v43, v43\n"
        "s_waitcnt lgkmcnt(1)\n"
        "v_cmp_gt_u32 vcc, 60, v10\n"
        "v_smfmac_f32_32x32x32_bf16 v[96:111], v[140:143], v[32:39], v20\n"
        "s_cbranch_vccnz 221f\n"
        "222:\n"
        "v_cmp_gt_u32 vcc, 60, v24\n"
        "s_setprio 3\n"
        "s_cbranch_vccz 223f\n"
        "s_setprio 2\n"
        "223:\n"
        "v_exp_f32 v44, v44\n"
        "v_exp_f32 v45, v45\n"
        "v_exp_f32 v46, v46\n"
        "v_exp_f32 v47, v47\n"
        "v_exp_f32 v48, v48\n"
        "v_exp_f32 v49, v49\n"
        "v_smfmac_f32_32x32x32_bf16 v[96:111], v[144:147], v[40:47], v20\n"
        "buffer_load_dwordx4 v[208:211], v2, s[20:23], s69 offen offset:3072 sc1\n"
        "v_exp_f32 v50, v50\n"
        "v_exp_f32 v51, v51\n"
        "v_exp_f32 v52, v52\n"
        "v_exp_f32 v53, v53\n"
        "s_waitcnt lgkmcnt(0)\n"
        "v_mfma_f32_32x32x16_bf16 v[32:47], v[136:139], v[128:131], v[64:79]\n"
        "s_add_u32 s69, s44, 4096\n"
        "buffer_load_dwordx4 v[212:215], v2, s[20:23], s69 offen offset:0 sc1\n"
        "v_exp_f32 v54, v54\n"
        "v_exp_f32 v55, v55\n"
        "v_exp_f32 v56, v56\n"
        "v_exp_f32 v57, v57\n"
        "v_exp_f32 v58, v58\n"
        "v_exp_f32 v59, v59\n"
        "v_smfmac_f32_32x32x32_bf16 v[112:127], v[140:143], v[48:55], v20\n"
        "buffer_load_dwordx4 v[216:219], v2, s[20:23], s69 offen offset:1024 sc1\n"
        "v_exp_f32 v60, v60\n"
        "v_exp_f32 v61, v61\n"
        "v_exp_f32 v62, v62\n"
        "v_exp_f32 v63, v63\n"
        "v_exp_f32 v32, v32\n"
        "v_exp_f32 v33, v33\n"
        "v_smfmac_f32_32x32x32_bf16 v[112:127], v[144:147], v[56:63], v20\n"
        "s_mov_b64 exec, s[60:61]\n"
        "ds_read_b128 v[144:147], v9 offset:60928\n"
        "ds_read_b128 v[140:143], v9 offset:8192\n"
        "s_mov_b64 exec, -1\n"
        "v_exp_f32 v34, v34\n"
        "v_exp_f32 v35, v35\n"
        "v_exp_f32 v36, v36\n"
        "v_exp_f32 v37, v37\n"
        "v_mfma_f32_32x32x16_bf16 v[48:63], v[136:139], v[132:135], v[80:95]\n"
        "ds_read_b128 v[136:139], v8 offset:2048\n"
        "buffer_load_dwordx4 v[220:223], v2, s[20:23], s69 offen offset:2048 sc1\n"
        "v_exp_f32 v38, v38\n"
        "v_exp_f32 v39, v39\n"
        "v_exp_f32 v40, v40\n"
        "v_exp_f32 v41, v41\n"
        "v_exp_f32 v42, v42\n"
        "v_exp_f32 v43, v43\n"
        "s_waitcnt vmcnt(9) lgkmcnt(1)\n"
        "v_smfmac_f32_32x32x32_bf16 v[96:111], v[252:255], v[32:39], v20\n"
        "ds_add_u32 v14, v11\n"
        "buffer_load_dwordx4 v[224:227], v2, s[20:23], s69 offen offset:3072 sc1\n"
        "v_exp_f32 v44, v44\n"
        "v_exp_f32 v45, v45\n"
        "v_exp_f32 v46, v46\n"
        "v_exp_f32 v47, v47\n"
        "v_exp_f32 v48, v48\n"
        "v_exp_f32 v49, v49\n"
        "v_smfmac_f32_32x32x32_bf16 v[96:111], v[144:147], v[40:47], v20\n"
        "s_add_u32 s69, s44, 8192\n"
        "buffer_load_dwordx4 v[228:231], v2, s[20:23], s69 offen offset:0 sc1\n"
        "v_exp_f32 v50, v50\n"
        "v_exp_f32 v51, v51\n"
        "v_exp_f32 v52, v52\n"
        "v_exp_f32 v53, v53\n"
        "s_waitcnt lgkmcnt(0)\n"
        "v_mfma_f32_32x32x16_bf16 v[32:47], v[136:139], v[128:131], v[64:79]\n"
        "buffer_load_dwordx4 v[232:235], v2, s[20:23], s69 offen offset:1024 sc1\n"
        "v_exp_f32 v54, v54\n"
        "v_exp_f32 v55, v55\n"
        "v_exp_f32 v56, v56\n"
        "v_exp_f32 v57, v57\n"
        "v_exp_f32 v58, v58\n"
        "v_exp_f32 v59, v59\n"
        "v_smfmac_f32_32x32x32_bf16 v[112:127], v[252:255], v[48:55], v20\n"
        "buffer_load_dwordx4 v[236:239], v2, s[20:23], s69 offen offset:2048 sc1\n"
        "v_exp_f32 v60, v60\n"
        "v_exp_f32 v61, v61\n"
        "v_exp_f32 v62, v62\n"
        "v_exp_f32 v63, v63\n"
        "v_exp_f32 v32, v32\n"
        "v_exp_f32 v33, v33\n"
        "v_smfmac_f32_32x32x32_bf16 v[112:127], v[144:147], v[56:63], v20\n"
        "s_mov_b64 exec, s[60:61]\n"
        "ds_read_b128 v[144:147], v9 offset:8704\n"
        "ds_read_b128 v[252:255], v9 offset:11264\n"
        "s_mov_b64 exec, -1\n"
        "ds_read_b32 v10, v14\n"
        "ds_read_b32 v24, v25\n"
        "v_exp_f32 v34, v34\n"
        "v_exp_f32 v35, v35\n"
        "v_exp_f32 v36, v36\n"
        "v_exp_f32 v37, v37\n"
        "v_mfma_f32_32x32x16_bf16 v[48:63], v[136:139], v[132:135], v[80:95]\n"
        "ds_read_b128 v[136:139], v8 offset:5120\n"
        "buffer_load_dwordx4 v[240:243], v2, s[20:23], s69 offen offset:3072 sc1\n"
        "v_exp_f32 v38, v38\n"
        "v_exp_f32 v39, v39\n"
        "v_exp_f32 v40, v40\n"
        "v_exp_f32 v41, v41\n"
        "v_exp_f32 v42, v42\n"
        "v_exp_f32 v43, v43\n"
        "s_waitcnt lgkmcnt(1)\n"
        "v_cmp_gt_u32 vcc, 64, v10\n"
        "v_smfmac_f32_32x32x32_bf16 v[96:111], v[140:143], v[32:39], v20\n"
        "s_cbranch_vccnz 224f\n"
        "225:\n"
        "v_cmp_gt_u32 vcc, 64, v24\n"
        "s_setprio 3\n"
        "s_cbranch_vccz 226f\n"
        "s_setprio 2\n"
        "226:\n"
        "v_exp_f32 v44, v44\n"
        "v_exp_f32 v45, v45\n"
        "v_exp_f32 v46, v46\n"
        "v_exp_f32 v47, v47\n"
        "v_exp_f32 v48, v48\n"
        "v_exp_f32 v49, v49\n"
        "v_smfmac_f32_32x32x32_bf16 v[96:111], v[144:147], v[40:47], v20\n"
        "s_add_u32 s69, s44, 12288\n"
        "buffer_load_dwordx4 v[244:247], v2, s[20:23], s69 offen offset:0 sc1\n"
        "v_exp_f32 v50, v50\n"
        "v_exp_f32 v51, v51\n"
        "v_exp_f32 v52, v52\n"
        "v_exp_f32 v53, v53\n"
        "s_waitcnt lgkmcnt(0)\n"
        "v_mfma_f32_32x32x16_bf16 v[32:47], v[136:139], v[128:131], v[64:79]\n"
        "buffer_load_dwordx4 v[248:251], v2, s[20:23], s69 offen offset:1024 sc1\n"
        "v_exp_f32 v54, v54\n"
        "v_exp_f32 v55, v55\n"
        "v_exp_f32 v56, v56\n"
        "v_exp_f32 v57, v57\n"
        "v_exp_f32 v58, v58\n"
        "v_exp_f32 v59, v59\n"
        "v_smfmac_f32_32x32x32_bf16 v[112:127], v[140:143], v[48:55], v20\n"
        "buffer_load_dwordx4 v[180:183], v2, s[20:23], s69 offen offset:2048 sc1\n"
        "v_exp_f32 v60, v60\n"
        "v_exp_f32 v61, v61\n"
        "v_exp_f32 v62, v62\n"
        "v_exp_f32 v63, v63\n"
        "v_exp_f32 v32, v32\n"
        "v_exp_f32 v33, v33\n"
        "v_smfmac_f32_32x32x32_bf16 v[112:127], v[144:147], v[56:63], v20\n"
        "s_mov_b64 exec, s[60:61]\n"
        "ds_read_b128 v[144:147], v9 offset:11776\n"
        "ds_read_b128 v[140:143], v9 offset:20480\n"
        "s_mov_b64 exec, -1\n"
        "v_exp_f32 v34, v34\n"
        "v_exp_f32 v35, v35\n"
        "v_exp_f32 v36, v36\n"
        "v_exp_f32 v37, v37\n"
        "v_mfma_f32_32x32x16_bf16 v[48:63], v[136:139], v[132:135], v[80:95]\n"
        "ds_read_b128 v[136:139], v8 offset:14336\n"
        "buffer_load_dwordx4 v[184:187], v2, s[20:23], s69 offen offset:3072 sc1\n"
        "v_exp_f32 v38, v38\n"
        "v_exp_f32 v39, v39\n"
        "v_exp_f32 v40, v40\n"
        "v_exp_f32 v41, v41\n"
        "v_exp_f32 v42, v42\n"
        "v_exp_f32 v43, v43\n"
        "v_smfmac_f32_32x32x32_bf16 v[96:111], v[252:255], v[32:39], v20\n"
        "s_add_u32 s69, s44, 16384\n"
        "buffer_load_dwordx4 v[164:167], v2, s[20:23], s69 offen offset:0 sc1\n"
        "v_exp_f32 v44, v44\n"
        "v_exp_f32 v45, v45\n"
        "v_exp_f32 v46, v46\n"
        "v_exp_f32 v47, v47\n"
        "v_exp_f32 v48, v48\n"
        "v_exp_f32 v49, v49\n"
        "s_waitcnt lgkmcnt(0)\n"
        "v_smfmac_f32_32x32x32_bf16 v[96:111], v[144:147], v[40:47], v20\n"
        "buffer_load_dwordx4 v[168:171], v2, s[20:23], s69 offen offset:1024 sc1\n"
        "s_lshl1_add_u32 s64, s6, s52\n"
        "s_lshl_b32 s64, s64, 7\n"
        "s_add_u32 s64, s64, 983040\n"
        "v_exp_f32 v50, v50\n"
        "v_exp_f32 v51, v51\n"
        "v_exp_f32 v52, v52\n"
        "v_exp_f32 v53, v53\n"
        "v_mfma_f32_32x32x16_bf16 v[32:47], v[136:139], v[128:131], v[64:79]\n"
        "buffer_load_dwordx4 v[172:175], v2, s[20:23], s69 offen offset:2048 sc1\n"
        "s_cmp_eq_u32 s51, 0\n"
        "s_cselect_b32 s64, s64, 2147483632\n"
        "v_writelane_b32 v27, s64, 0\n"
        "s_mul_i32 s64, s48, 7\n"
        "v_exp_f32 v54, v54\n"
        "v_exp_f32 v55, v55\n"
        "v_exp_f32 v56, v56\n"
        "v_exp_f32 v57, v57\n"
        "v_exp_f32 v58, v58\n"
        "v_exp_f32 v59, v59\n"
        "s_waitcnt vmcnt(20)\n"
        "v_smfmac_f32_32x32x32_bf16 v[112:127], v[252:255], v[48:55], v20\n"
        "ds_add_u32 v14, v11\n"
        "buffer_load_dwordx4 v[176:179], v2, s[20:23], s69 offen offset:3072 sc1\n"
        "s_and_b32 s65, s6, 31\n"
        "s_lshl_b32 s65, s65, 8\n"
        "v_exp_f32 v60, v60\n"
        "v_exp_f32 v61, v61\n"
        "v_exp_f32 v62, v62\n"
        "v_exp_f32 v63, v63\n"
        "v_exp_f32 v32, v32\n"
        "v_exp_f32 v33, v33\n"
        "v_smfmac_f32_32x32x32_bf16 v[112:127], v[144:147], v[56:63], v20\n"
        "s_mov_b64 exec, s[60:61]\n"
        "ds_read_b128 v[144:147], v9 offset:20992\n"
        "ds_read_b128 v[252:255], v9 offset:23552\n"
        "s_mov_b64 exec, -1\n"
        "ds_read_b32 v10, v14\n"
        "ds_read_b32 v24, v25\n"
        "s_add_u32 s65, s65, 983040\n"
        "v_bfe_u32 v6, v1, 1, 1\n"
        "v_lshl_add_u32 v5, v6, 7, s65\n"
        "v_exp_f32 v34, v34\n"
        "v_exp_f32 v35, v35\n"
        "v_exp_f32 v36, v36\n"
        "v_exp_f32 v37, v37\n"
        "v_mfma_f32_32x32x16_bf16 v[48:63], v[136:139], v[132:135], v[80:95]\n"
        "ds_read_b128 v[136:139], v8 offset:17408\n"
        "s_add_u32 s69, s44, 20480\n"
        "buffer_load_dwordx4 v[188:191], v2, s[20:23], s69 offen offset:0 sc1\n"
        "v_and_b32 v7, 1, v1\n"
        "v_mul_u32_u24 v7, 6, v7\n"
        "v_add_u32 v7, s64, v7\n"
        "v_exp_f32 v38, v38\n"
        "v_exp_f32 v39, v39\n"
        "v_exp_f32 v40, v40\n"
        "v_exp_f32 v41, v41\n"
        "v_exp_f32 v42, v42\n"
        "v_exp_f32 v43, v43\n"
        "s_waitcnt lgkmcnt(0)\n"
        "v_cmp_gt_u32 vcc, 68, v10\n"
        "v_smfmac_f32_32x32x32_bf16 v[96:111], v[140:143], v[32:39], v20\n"
        "s_cbranch_vccnz 227f\n"
        "228:\n"
        "v_cmp_gt_u32 vcc, 68, v24\n"
        "s_setprio 3\n"
        "s_cbranch_vccz 229f\n"
        "s_setprio 2\n"
        "229:\n"
        "v_lshrrev_b32 v7, 3, v7\n"
        "v_lshl_add_u32 v5, v7, 13, v5\n"
        "v_exp_f32 v44, v44\n"
        "v_exp_f32 v45, v45\n"
        "v_exp_f32 v46, v46\n"
        "v_exp_f32 v47, v47\n"
        "v_exp_f32 v48, v48\n"
        "v_exp_f32 v49, v49\n"
        "v_smfmac_f32_32x32x32_bf16 v[96:111], v[144:147], v[40:47], v20\n"
        "buffer_load_dwordx4 v[192:195], v2, s[20:23], s69 offen offset:1024 sc1\n"
        "v_exp_f32 v50, v50\n"
        "v_exp_f32 v51, v51\n"
        "v_exp_f32 v52, v52\n"
        "v_exp_f32 v53, v53\n"
        "v_mfma_f32_32x32x16_bf16 v[32:47], v[136:139], v[128:131], v[64:79]\n"
        "v_exp_f32 v54, v54\n"
        "v_exp_f32 v55, v55\n"
        "v_exp_f32 v56, v56\n"
        "v_exp_f32 v57, v57\n"
        "v_exp_f32 v58, v58\n"
        "v_exp_f32 v59, v59\n"
        "v_smfmac_f32_32x32x32_bf16 v[112:127], v[140:143], v[48:55], v20\n"
        "v_exp_f32 v60, v60\n"
        "v_exp_f32 v61, v61\n"
        "v_exp_f32 v62, v62\n"
        "v_exp_f32 v63, v63\n"
        "s_setprio 1\n"
        "v_exp_f32 v32, v32\n"
        "v_exp_f32 v33, v33\n"
        "v_smfmac_f32_32x32x32_bf16 v[112:127], v[144:147], v[56:63], v20\n"
        "s_mov_b64 exec, s[60:61]\n"
        "ds_read_b128 v[144:147], v9 offset:24064\n"
        "ds_read_b128 v[140:143], v9 offset:32768\n"
        "s_mov_b64 exec, -1\n"
        "v_exp_f32 v34, v34\n"
        "v_exp_f32 v35, v35\n"
        "v_exp_f32 v36, v36\n"
        "v_exp_f32 v37, v37\n"
        "v_mfma_f32_32x32x16_bf16 v[48:63], v[136:139], v[132:135], v[80:95]\n"
        "ds_read_b128 v[136:139], v8 offset:26624\n"
        "v_exp_f32 v38, v38\n"
        "v_exp_f32 v39, v39\n"
        "v_exp_f32 v40, v40\n"
        "v_exp_f32 v41, v41\n"
        "v_exp_f32 v42, v42\n"
        "v_exp_f32 v43, v43\n"
        "v_smfmac_f32_32x32x32_bf16 v[96:111], v[252:255], v[32:39], v20\n"
        "v_exp_f32 v44, v44\n"
        "v_exp_f32 v45, v45\n"
        "v_exp_f32 v46, v46\n"
        "v_exp_f32 v47, v47\n"
        "v_exp_f32 v48, v48\n"
        "v_exp_f32 v49, v49\n"
        "s_waitcnt lgkmcnt(0)\n"
        "v_smfmac_f32_32x32x32_bf16 v[96:111], v[144:147], v[40:47], v20\n"
        "v_exp_f32 v50, v50\n"
        "v_exp_f32 v51, v51\n"
        "v_exp_f32 v52, v52\n"
        "v_exp_f32 v53, v53\n"
        "v_mfma_f32_32x32x16_bf16 v[32:47], v[136:139], v[128:131], v[64:79]\n"
        "v_exp_f32 v54, v54\n"
        "v_exp_f32 v55, v55\n"
        "v_exp_f32 v56, v56\n"
        "v_exp_f32 v57, v57\n"
        "v_exp_f32 v58, v58\n"
        "v_exp_f32 v59, v59\n"
        "v_smfmac_f32_32x32x32_bf16 v[112:127], v[252:255], v[48:55], v20\n"
        "v_exp_f32 v60, v60\n"
        "v_exp_f32 v61, v61\n"
        "v_exp_f32 v62, v62\n"
        "v_exp_f32 v63, v63\n"
        "s_nop 0\n"
        "v_exp_f32 v32, v32\n"
        "v_exp_f32 v33, v33\n"
        "v_smfmac_f32_32x32x32_bf16 v[112:127], v[144:147], v[56:63], v20\n"
        "s_mov_b64 exec, s[60:61]\n"
        "ds_read_b128 v[144:147], v9 offset:33280\n"
        "ds_read_b128 v[252:255], v9 offset:35840\n"
        "s_mov_b64 exec, -1\n"
        "v_exp_f32 v34, v34\n"
        "v_exp_f32 v35, v35\n"
        "v_exp_f32 v36, v36\n"
        "v_exp_f32 v37, v37\n"
        "v_mfma_f32_32x32x16_bf16 v[48:63], v[136:139], v[132:135], v[80:95]\n"
        "ds_read_b128 v[136:139], v8 offset:29696\n"
        "v_exp_f32 v38, v38\n"
        "v_exp_f32 v39, v39\n"
        "v_exp_f32 v40, v40\n"
        "v_exp_f32 v41, v41\n"
        "v_exp_f32 v42, v42\n"
        "v_exp_f32 v43, v43\n"
        "v_smfmac_f32_32x32x32_bf16 v[96:111], v[140:143], v[32:39], v20\n"
        "v_exp_f32 v44, v44\n"
        "v_exp_f32 v45, v45\n"
        "v_exp_f32 v46, v46\n"
        "v_exp_f32 v47, v47\n"
        "v_exp_f32 v48, v48\n"
        "v_exp_f32 v49, v49\n"
        "s_waitcnt lgkmcnt(0)\n"
        "v_smfmac_f32_32x32x32_bf16 v[96:111], v[144:147], v[40:47], v20\n"
        "v_exp_f32 v50, v50\n"
        "v_exp_f32 v51, v51\n"
        "v_exp_f32 v52, v52\n"
        "v_exp_f32 v53, v53\n"
        "v_mfma_f32_32x32x16_bf16 v[32:47], v[136:139], v[128:131], v[64:79]\n"
        "v_exp_f32 v54, v54\n"
        "v_exp_f32 v55, v55\n"
        "v_exp_f32 v56, v56\n"
        "v_exp_f32 v57, v57\n"
        "v_exp_f32 v58, v58\n"
        "v_exp_f32 v59, v59\n"
        "v_smfmac_f32_32x32x32_bf16 v[112:127], v[140:143], v[48:55], v20\n"
        "v_exp_f32 v60, v60\n"
        "v_exp_f32 v61, v61\n"
        "v_exp_f32 v62, v62\n"
        "v_exp_f32 v63, v63\n"
        "s_setprio 0\n"
        "v_exp_f32 v32, v32\n"
        "v_exp_f32 v33, v33\n"
        "v_smfmac_f32_32x32x32_bf16 v[112:127], v[144:147], v[56:63], v20\n"
        "s_mov_b64 exec, s[60:61]\n"
        "ds_read_b128 v[144:147], v9 offset:36352\n"
        "s_mov_b64 exec, -1\n"
        "v_exp_f32 v34, v34\n"
        "v_exp_f32 v35, v35\n"
        "v_exp_f32 v36, v36\n"
        "v_exp_f32 v37, v37\n"
        "v_mfma_f32_32x32x16_bf16 v[48:63], v[136:139], v[132:135], v[80:95]\n"
        "v_exp_f32 v38, v38\n"
        "v_exp_f32 v39, v39\n"
        "v_exp_f32 v40, v40\n"
        "v_exp_f32 v41, v41\n"
        "v_exp_f32 v42, v42\n"
        "v_exp_f32 v43, v43\n"
        "v_smfmac_f32_32x32x32_bf16 v[96:111], v[252:255], v[32:39], v20\n"
        "v_exp_f32 v44, v44\n"
        "v_exp_f32 v45, v45\n"
        "v_exp_f32 v46, v46\n"
        "v_exp_f32 v47, v47\n"
        "s_nop 0\n"
        "v_exp_f32 v48, v48\n"
        "v_exp_f32 v49, v49\n"
        "s_waitcnt lgkmcnt(0)\n"
        "v_smfmac_f32_32x32x32_bf16 v[96:111], v[144:147], v[40:47], v20\n"
        "v_exp_f32 v50, v50\n"
        "v_exp_f32 v51, v51\n"
        "v_exp_f32 v52, v52\n"
        "v_exp_f32 v53, v53\n"
        "v_exp_f32 v54, v54\n"
        "v_exp_f32 v55, v55\n"
        "v_exp_f32 v56, v56\n"
        "v_exp_f32 v57, v57\n"
        "v_exp_f32 v58, v58\n"
        "v_exp_f32 v59, v59\n"
        "v_smfmac_f32_32x32x32_bf16 v[112:127], v[252:255], v[48:55], v20\n"
        "v_exp_f32 v60, v60\n"
        "v_exp_f32 v61, v61\n"
        "v_exp_f32 v62, v62\n"
        "v_exp_f32 v63, v63\n"
        "s_nop 1\n"
        "v_smfmac_f32_32x32x32_bf16 v[112:127], v[144:147], v[56:63], v20\n"
        "v_add_u32 v28, s55, v2\n"
        "v_xor_b32 v29, 16384, v28\n"
        "s_nop 9\n"
        "ds_write_b128 v28, v[112:115] offset:0\n"
        "ds_write_b128 v28, v[116:119] offset:1024\n"
        "ds_write_b32 v28, v120 offset:2048\n"
        "s_waitcnt vmcnt(0) lgkmcnt(0)\n"
        "s_branch 121b\n"
        "103:\n"
        "s_sub_u32 s59, s59, 1\n"
        "s_cbranch_scc1 100103f\n"
        "s_sleep 2\n"
        "s_mov_b32 s64, 0\n"
        "buffer_load_dwordx4 v[72:75], v14, s[20:23], s64 offen sc1\n"
        "s_mov_b32 s64, 8192\n"
        "buffer_load_dwordx4 v[76:79], v14, s[20:23], s64 offen sc1\n"
        "s_mov_b32 s64, 16384\n"
        "buffer_load_dwordx4 v[120:123], v14, s[20:23], s64 offen sc1\n"
        "s_mov_b32 s64, 24576\n"
        "buffer_load_dwordx4 v[124:127], v14, s[20:23], s64 offen sc1\n"
        "s_waitcnt vmcnt(0)\n"
        "v_xor_b32 v15, v73, v3\n"
        "v_bitop3_b32 v15, v75, v3, v15 bitop3:0xbe\n"
        "v_bitop3_b32 v15, v77, v3, v15 bitop3:0xbe\n"
        "v_bitop3_b32 v15, v79, v3, v15 bitop3:0xbe\n"
        "v_bitop3_b32 v15, v121, v3, v15 bitop3:0xbe\n"
        "v_bitop3_b32 v15, v123, v3, v15 bitop3:0xbe\n"
        "v_bitop3_b32 v15, v125, v3, v15 bitop3:0xbe\n"
        "v_bitop3_b32 v15, v127, v3, v15 bitop3:0xbe\n"
        "v_cmp_ne_u32 vcc, 0, v15\n"
        "s_cbranch_vccnz 103b\n"
        "s_nop 1\n"
        "s_branch 104b\n"
        "100103:\n"
        "s_mov_b32 s59, 0\n"
        "s_branch 104b\n"
        "106:\n"
        "buffer_load_dwordx4 v12, s[24:27], s7 offen offset:0 lds\n"
        "s_branch 107b\n"
        "108:\n"
        "buffer_load_dwordx4 v13, s[24:27], s7 offen offset:1024 lds\n"
        "s_branch 109b\n"
        "110:\n"
        "buffer_load_dwordx4 v12, s[24:27], s7 offen offset:0 lds\n"
        "s_branch 111b\n"
        "112:\n"
        "buffer_load_dwordx4 v13, s[24:27], s7 offen offset:1024 lds\n"
        "s_branch 113b\n"
        "114:\n"
        "buffer_load_dwordx4 v12, s[24:27], s7 offen offset:0 lds\n"
        "s_branch 115b\n"
        "116:\n"
        "buffer_load_dwordx4 v13, s[24:27], s7 offen offset:1024 lds\n"
        "s_branch 117b\n"
        "118:\n"
        "s_sub_u32 s59, s59, 1\n"
        "s_cbranch_scc1 100118f\n"
        "s_sleep 2\n"
        "buffer_load_dword v43, v33, s[20:23], 0 offen sc1\n"
        "s_waitcnt vmcnt(0)\n"
        "v_cmp_ne_u32 vcc, v43, v3\n"
        "s_cbranch_vccnz 118b\n"
        "s_nop 1\n"
        "s_branch 119b\n"
        "100118:\n"
        "s_mov_b32 s59, 0\n"
        "s_branch 119b\n"
        "122:\n"
        "s_sub_u32 s59, s59, 1\n"
        "s_cbranch_scc1 100122f\n"
        "ds_read_b32 v10, v14\n"
        "s_waitcnt lgkmcnt(0)\n"
        "v_cmp_gt_u32 vcc, 4, v10\n"
        "s_cbranch_vccnz 122b\n"
        "s_nop 1\n"
        "s_branch 123b\n"
        "100122:\n"
        "s_mov_b32 s59, 0\n"
        "s_branch 123b\n"
        "125:\n"
        "s_sub_u32 s59, s59, 1\n"
        "s_cbranch_scc1 100125f\n"
        "ds_read_b32 v10, v14\n"
        "s_waitcnt lgkmcnt(0)\n"
        "v_cmp_gt_u32 vcc, 8, v10\n"
        "s_cbranch_vccnz 125b\n"
        "s_nop 1\n"
        "s_branch 126b\n"
        "100125:\n"
        "s_mov_b32 s59, 0\n"
        "s_branch 126b\n"
        "128:\n"
        "s_sub_u32 s59, s59, 1\n"
        "s_cbranch_scc1 100128f\n"
        "ds_read_b32 v10, v14\n"
        "s_waitcnt lgkmcnt(0)\n"
        "v_cmp_gt_u32 vcc, 12, v10\n"
        "s_cbranch_vccnz 128b\n"
        "s_nop 1\n"
        "s_branch 129b\n"
        "100128:\n"
        "s_mov_b32 s59, 0\n"
        "s_branch 129b\n"
        "131:\n"
        "s_sub_u32 s59, s59, 1\n"
        "s_cbranch_scc1 100131f\n"
        "ds_read_b32 v10, v14\n"
        "s_waitcnt lgkmcnt(0)\n"
        "v_cmp_gt_u32 vcc, 16, v10\n"
        "s_cbranch_vccnz 131b\n"
        "s_nop 1\n"
        "s_branch 132b\n"
        "100131:\n"
        "s_mov_b32 s59, 0\n"
        "s_branch 132b\n"
        "134:\n"
        "s_sub_u32 s59, s59, 1\n"
        "s_cbranch_scc1 100134f\n"
        "ds_read_b32 v10, v14\n"
        "s_waitcnt lgkmcnt(0)\n"
        "v_cmp_gt_u32 vcc, 20, v10\n"
        "s_cbranch_vccnz 134b\n"
        "s_nop 1\n"
        "s_branch 135b\n"
        "100134:\n"
        "s_mov_b32 s59, 0\n"
        "s_branch 135b\n"
        "137:\n"
        "s_sub_u32 s59, s59, 1\n"
        "s_cbranch_scc1 100137f\n"
        "ds_read_b32 v10, v14\n"
        "s_waitcnt lgkmcnt(0)\n"
        "v_cmp_gt_u32 vcc, 24, v10\n"
        "s_cbranch_vccnz 137b\n"
        "s_nop 1\n"
        "s_branch 138b\n"
        "100137:\n"
        "s_mov_b32 s59, 0\n"
        "s_branch 138b\n"
        "140:\n"
        "s_sub_u32 s59, s59, 1\n"
        "s_cbranch_scc1 100140f\n"
        "ds_read_b32 v10, v14\n"
        "s_waitcnt lgkmcnt(0)\n"
        "v_cmp_gt_u32 vcc, 28, v10\n"
        "s_cbranch_vccnz 140b\n"
        "s_nop 1\n"
        "s_branch 141b\n"
        "100140:\n"
        "s_mov_b32 s59, 0\n"
        "s_branch 141b\n"
        "143:\n"
        "s_sub_u32 s59, s59, 1\n"
        "s_cbranch_scc1 100143f\n"
        "ds_read_b32 v10, v14\n"
        "s_waitcnt lgkmcnt(0)\n"
        "v_cmp_gt_u32 vcc, 32, v10\n"
        "s_cbranch_vccnz 143b\n"
        "s_nop 1\n"
        "s_branch 144b\n"
        "100143:\n"
        "s_mov_b32 s59, 0\n"
        "s_branch 144b\n"
        "146:\n"
        "s_sub_u32 s59, s59, 1\n"
        "s_cbranch_scc1 100146f\n"
        "ds_read_b32 v10, v14\n"
        "s_waitcnt lgkmcnt(0)\n"
        "v_cmp_gt_u32 vcc, 36, v10\n"
        "s_cbranch_vccnz 146b\n"
        "s_nop 1\n"
        "s_branch 147b\n"
        "100146:\n"
        "s_mov_b32 s59, 0\n"
        "s_branch 147b\n"
        "149:\n"
        "s_sub_u32 s59, s59, 1\n"
        "s_cbranch_scc1 100149f\n"
        "ds_read_b32 v10, v14\n"
        "s_waitcnt lgkmcnt(0)\n"
        "v_cmp_gt_u32 vcc, 40, v10\n"
        "s_cbranch_vccnz 149b\n"
        "s_nop 1\n"
        "s_branch 150b\n"
        "100149:\n"
        "s_mov_b32 s59, 0\n"
        "s_branch 150b\n"
        "152:\n"
        "s_sub_u32 s59, s59, 1\n"
        "s_cbranch_scc1 100152f\n"
        "ds_read_b32 v10, v14\n"
        "s_waitcnt lgkmcnt(0)\n"
        "v_cmp_gt_u32 vcc, 44, v10\n"
        "s_cbranch_vccnz 152b\n"
        "s_nop 1\n"
        "s_branch 153b\n"
        "100152:\n"
        "s_mov_b32 s59, 0\n"
        "s_branch 153b\n"
        "155:\n"
        "s_sub_u32 s59, s59, 1\n"
        "s_cbranch_scc1 100155f\n"
        "ds_read_b32 v10, v14\n"
        "s_waitcnt lgkmcnt(0)\n"
        "v_cmp_gt_u32 vcc, 48, v10\n"
        "s_cbranch_vccnz 155b\n"
        "s_nop 1\n"
        "s_branch 156b\n"
        "100155:\n"
        "s_mov_b32 s59, 0\n"
        "s_branch 156b\n"
        "158:\n"
        "s_sub_u32 s59, s59, 1\n"
        "s_cbranch_scc1 100158f\n"
        "ds_read_b32 v10, v14\n"
        "s_waitcnt lgkmcnt(0)\n"
        "v_cmp_gt_u32 vcc, 52, v10\n"
        "s_cbranch_vccnz 158b\n"
        "s_nop 1\n"
        "s_branch 159b\n"
        "100158:\n"
        "s_mov_b32 s59, 0\n"
        "s_branch 159b\n"
        "161:\n"
        "s_sub_u32 s59, s59, 1\n"
        "s_cbranch_scc1 100161f\n"
        "ds_read_b32 v10, v14\n"
        "s_waitcnt lgkmcnt(0)\n"
        "v_cmp_gt_u32 vcc, 56, v10\n"
        "s_cbranch_vccnz 161b\n"
        "s_nop 1\n"
        "s_branch 162b\n"
        "100161:\n"
        "s_mov_b32 s59, 0\n"
        "s_branch 162b\n"
        "164:\n"
        "s_sub_u32 s59, s59, 1\n"
        "s_cbranch_scc1 100164f\n"
        "s_sleep 2\n"
        "buffer_load_dword v21, v22, s[20:23], 0 offen sc1\n"
        "s_waitcnt vmcnt(0)\n"
        "v_cmp_ne_u32 vcc, v21, v3\n"
        "s_cbranch_vccnz 164b\n"
        "s_nop 1\n"
        "s_branch 165b\n"
        "100164:\n"
        "s_mov_b32 s59, 0\n"
        "s_branch 165b\n"
        "166:\n"
        "s_sub_u32 s59, s59, 1\n"
        "s_cbranch_scc1 100166f\n"
        "ds_read_b32 v10, v14\n"
        "s_waitcnt lgkmcnt(0)\n"
        "v_cmp_gt_u32 vcc, 60, v10\n"
        "s_cbranch_vccnz 166b\n"
        "s_nop 1\n"
        "s_branch 167b\n"
        "100166:\n"
        "s_mov_b32 s59, 0\n"
        "s_branch 167b\n"
        "169:\n"
        "s_sub_u32 s59, s59, 1\n"
        "s_cbranch_scc1 100169f\n"
        "ds_read_b32 v10, v14\n"
        "s_waitcnt lgkmcnt(0)\n"
        "v_cmp_gt_u32 vcc, 64, v10\n"
        "s_cbranch_vccnz 169b\n"
        "s_nop 1\n"
        "s_branch 170b\n"
        "100169:\n"
        "s_mov_b32 s59, 0\n"
        "s_branch 170b\n"
        "172:\n"
        "s_sub_u32 s59, s59, 1\n"
        "s_cbranch_scc1 100172f\n"
        "ds_read_b32 v10, v14\n"
        "ds_read_b32 v24, v25\n"
        "s_waitcnt lgkmcnt(0)\n"
        "v_min_u32 v10, v10, v24\n"
        "v_cmp_gt_u32 vcc, 68, v10\n"
        "s_cbranch_vccnz 172b\n"
        "s_nop 1\n"
        "s_branch 173b\n"
        "100172:\n"
        "s_mov_b32 s59, 0\n"
        "s_branch 173b\n"
        "175:\n"
        "s_sub_u32 s59, s59, 1\n"
        "s_cbranch_scc1 100175f\n"
        "ds_read_b32 v10, v14\n"
        "s_waitcnt lgkmcnt(0)\n"
        "v_cmp_gt_u32 vcc, 72, v10\n"
        "s_cbranch_vccnz 175b\n"
        "s_nop 1\n"
        "s_branch 176b\n"
        "100175:\n"
        "s_mov_b32 s59, 0\n"
        "s_branch 176b\n"
        "177:\n"
        "s_sub_u32 s59, s59, 1\n"
        "s_cbranch_scc1 100177f\n"
        "ds_read_b32 v10, v14\n"
        "s_waitcnt lgkmcnt(0)\n"
        "v_cmp_gt_u32 vcc, 4, v10\n"
        "s_cbranch_vccnz 177b\n"
        "s_nop 1\n"
        "s_branch 178b\n"
        "100177:\n"
        "s_mov_b32 s59, 0\n"
        "s_branch 178b\n"
        "180:\n"
        "s_sub_u32 s59, s59, 1\n"
        "s_cbranch_scc1 100180f\n"
        "ds_read_b32 v10, v14\n"
        "s_waitcnt lgkmcnt(0)\n"
        "v_cmp_gt_u32 vcc, 8, v10\n"
        "s_cbranch_vccnz 180b\n"
        "s_nop 1\n"
        "s_branch 181b\n"
        "100180:\n"
        "s_mov_b32 s59, 0\n"
        "s_branch 181b\n"
        "183:\n"
        "s_sub_u32 s59, s59, 1\n"
        "s_cbranch_scc1 100183f\n"
        "ds_read_b32 v10, v14\n"
        "s_waitcnt lgkmcnt(0)\n"
        "v_cmp_gt_u32 vcc, 12, v10\n"
        "s_cbranch_vccnz 183b\n"
        "s_nop 1\n"
        "s_branch 184b\n"
        "100183:\n"
        "s_mov_b32 s59, 0\n"
        "s_branch 184b\n"
        "186:\n"
        "s_sub_u32 s59, s59, 1\n"
        "s_cbranch_scc1 100186f\n"
        "ds_read_b32 v10, v14\n"
        "s_waitcnt lgkmcnt(0)\n"
        "v_cmp_gt_u32 vcc, 16, v10\n"
        "s_cbranch_vccnz 186b\n"
        "s_nop 1\n"
        "s_branch 187b\n"
        "100186:\n"
        "s_mov_b32 s59, 0\n"
        "s_branch 187b\n"
        "189:\n"
        "s_sub_u32 s59, s59, 1\n"
        "s_cbranch_scc1 100189f\n"
        "ds_read_b32 v10, v14\n"
        "s_waitcnt lgkmcnt(0)\n"
        "v_cmp_gt_u32 vcc, 20, v10\n"
        "s_cbranch_vccnz 189b\n"
        "s_nop 1\n"
        "s_branch 190b\n"
        "100189:\n"
        "s_mov_b32 s59, 0\n"
        "s_branch 190b\n"
        "192:\n"
        "s_sub_u32 s59, s59, 1\n"
        "s_cbranch_scc1 100192f\n"
        "ds_read_b32 v10, v14\n"
        "s_waitcnt lgkmcnt(0)\n"
        "v_cmp_gt_u32 vcc, 24, v10\n"
        "s_cbranch_vccnz 192b\n"
        "s_nop 1\n"
        "s_branch 193b\n"
        "100192:\n"
        "s_mov_b32 s59, 0\n"
        "s_branch 193b\n"
        "195:\n"
        "s_sub_u32 s59, s59, 1\n"
        "s_cbranch_scc1 100195f\n"
        "ds_read_b32 v10, v14\n"
        "s_waitcnt lgkmcnt(0)\n"
        "v_cmp_gt_u32 vcc, 28, v10\n"
        "s_cbranch_vccnz 195b\n"
        "s_nop 1\n"
        "s_branch 196b\n"
        "100195:\n"
        "s_mov_b32 s59, 0\n"
        "s_branch 196b\n"
        "198:\n"
        "s_sub_u32 s59, s59, 1\n"
        "s_cbranch_scc1 100198f\n"
        "ds_read_b32 v10, v14\n"
        "s_waitcnt lgkmcnt(0)\n"
        "v_cmp_gt_u32 vcc, 32, v10\n"
        "s_cbranch_vccnz 198b\n"
        "s_nop 1\n"
        "s_branch 199b\n"
        "100198:\n"
        "s_mov_b32 s59, 0\n"
        "s_branch 199b\n"
        "201:\n"
        "s_sub_u32 s59, s59, 1\n"
        "s_cbranch_scc1 100201f\n"
        "ds_read_b32 v10, v14\n"
        "s_waitcnt lgkmcnt(0)\n"
        "v_cmp_gt_u32 vcc, 36, v10\n"
        "s_cbranch_vccnz 201b\n"
        "s_nop 1\n"
        "s_branch 202b\n"
        "100201:\n"
        "s_mov_b32 s59, 0\n"
        "s_branch 202b\n"
        "204:\n"
        "s_sub_u32 s59, s59, 1\n"
        "s_cbranch_scc1 100204f\n"
        "ds_read_b32 v10, v14\n"
        "s_waitcnt lgkmcnt(0)\n"
        "v_cmp_gt_u32 vcc, 40, v10\n"
        "s_cbranch_vccnz 204b\n"
        "s_nop 1\n"
        "s_branch 205b\n"
        "100204:\n"
        "s_mov_b32 s59, 0\n"
        "s_branch 205b\n"
        "207:\n"
        "s_sub_u32 s59, s59, 1\n"
        "s_cbranch_scc1 100207f\n"
        "ds_read_b32 v10, v14\n"
        "s_waitcnt lgkmcnt(0)\n"
        "v_cmp_gt_u32 vcc, 44, v10\n"
        "s_cbranch_vccnz 207b\n"
        "s_nop 1\n"
        "s_branch 208b\n"
        "100207:\n"
        "s_mov_b32 s59, 0\n"
        "s_branch 208b\n"
        "210:\n"
        "s_sub_u32 s59, s59, 1\n"
        "s_cbranch_scc1 100210f\n"
        "ds_read_b32 v10, v14\n"
        "s_waitcnt lgkmcnt(0)\n"
        "v_cmp_gt_u32 vcc, 48, v10\n"
        "s_cbranch_vccnz 210b\n"
        "s_nop 1\n"
        "s_branch 211b\n"
        "100210:\n"
        "s_mov_b32 s59, 0\n"
        "s_branch 211b\n"
        "213:\n"
        "s_sub_u32 s59, s59, 1\n"
        "s_cbranch_scc1 100213f\n"
        "ds_read_b32 v10, v14\n"
        "s_waitcnt lgkmcnt(0)\n"
        "v_cmp_gt_u32 vcc, 52, v10\n"
        "s_cbranch_vccnz 213b\n"
        "s_nop 1\n"
        "s_branch 214b\n"
        "100213:\n"
        "s_mov_b32 s59, 0\n"
        "s_branch 214b\n"
        "216:\n"
        "s_sub_u32 s59, s59, 1\n"
        "s_cbranch_scc1 100216f\n"
        "ds_read_b32 v10, v14\n"
        "s_waitcnt lgkmcnt(0)\n"
        "v_cmp_gt_u32 vcc, 56, v10\n"
        "s_cbranch_vccnz 216b\n"
        "s_nop 1\n"
        "s_branch 217b\n"
        "100216:\n"
        "s_mov_b32 s59, 0\n"
        "s_branch 217b\n"
        "219:\n"
        "s_sub_u32 s59, s59, 1\n"
        "s_cbranch_scc1 100219f\n"
        "s_sleep 2\n"
        "buffer_load_dword v21, v22, s[20:23], 0 offen sc1\n"
        "s_waitcnt vmcnt(0)\n"
        "v_cmp_ne_u32 vcc, v21, v3\n"
        "s_cbranch_vccnz 219b\n"
        "s_nop 1\n"
        "s_branch 220b\n"
        "100219:\n"
        "s_mov_b32 s59, 0\n"
        "s_branch 220b\n"
        "221:\n"
        "s_sub_u32 s59, s59, 1\n"
        "s_cbranch_scc1 100221f\n"
        "ds_read_b32 v10, v14\n"
        "s_waitcnt lgkmcnt(0)\n"
        "v_cmp_gt_u32 vcc, 60, v10\n"
        "s_cbranch_vccnz 221b\n"
        "s_nop 1\n"
        "s_branch 222b\n"
        "100221:\n"
        "s_mov_b32 s59, 0\n"
        "s_branch 222b\n"
        "224:\n"
        "s_sub_u32 s59, s59, 1\n"
        "s_cbranch_scc1 100224f\n"
        "ds_read_b32 v10, v14\n"
        "s_waitcnt lgkmcnt(0)\n"
        "v_cmp_gt_u32 vcc, 64, v10\n"
        "s_cbranch_vccnz 224b\n"
        "s_nop 1\n"
        "s_branch 225b\n"
        "100224:\n"
        "s_mov_b32 s59, 0\n"
        "s_branch 225b\n"
        "227:\n"
        "s_sub_u32 s59, s59, 1\n"
        "s_cbranch_scc1 100227f\n"
        "ds_read_b32 v10, v14\n"
        "s_waitcnt lgkmcnt(0)\n"
        "v_cmp_gt_u32 vcc, 68, v10\n"
        "s_cbranch_vccnz 227b\n"
        "s_nop 1\n"
        "s_branch 228b\n"
        "100227:\n"
        "s_mov_b32 s59, 0\n"
        "s_branch 228b\n"
        "230:\n"
        "s_waitcnt lgkmcnt(0)\n"
        "v_mov_b32 v64, 0xff800000\n"
        "s_mov_b32 s62, 0\n"
        "232:\n"
        "s_lshr_b32 s63, s62, 1\n"
        "s_lshl_b32 s63, s63, 12\n"
        "s_and_b32 s64, s62, 1\n"
        "s_lshl_b32 s64, s64, 10\n"
        "s_add_u32 s63, s63, s64\n"
        "s_add_u32 s63, s63, s84\n"
        "buffer_load_dwordx4 v[136:139], v2, s[20:23], s63 offen sc1\n"
        "s_waitcnt vmcnt(0)\n"
        "v_mfma_f32_32x32x16_bf16 v[48:63], v[136:139], v[128:131], 0\n"
        "s_nop 11\n"
        "v_max3_f32 v64, v64, v48, v49\n"
        "v_max3_f32 v64, v64, v50, v51\n"
        "v_max3_f32 v64, v64, v52, v53\n"
        "v_max3_f32 v64, v64, v54, v55\n"
        "v_max3_f32 v64, v64, v56, v57\n"
        "v_max3_f32 v64, v64, v58, v59\n"
        "v_max3_f32 v64, v64, v60, v61\n"
        "v_max3_f32 v64, v64, v62, v63\n"
        "s_add_u32 s62, s62, 1\n"
        "s_cmp_lt_u32 s62, 64\n"
        "s_cbranch_scc1 232b\n"
        "v_mov_b32 v65, v64\n"
        "s_nop 1\n"
        "v_permlane32_swap_b32 v64, v65\n"
        "v_max_f32 v64, v64, v65\n"
        "v_sub_f32 v64, 0, v64\n"
        "v_mov_b32 v65, v64\n"
        "v_mov_b32 v66, v64\n"
        "v_mov_b32 v67, v64\n"
        "v_mov_b32 v68, v64\n"
        "v_mov_b32 v69, v64\n"
        "v_mov_b32 v70, v64\n"
        "v_mov_b32 v71, v64\n"
        "v_mov_b32 v72, v64\n"
        "v_mov_b32 v73, v64\n"
        "v_mov_b32 v74, v64\n"
        "v_mov_b32 v75, v64\n"
        "v_mov_b32 v76, v64\n"
        "v_mov_b32 v77, v64\n"
        "v_mov_b32 v78, v64\n"
        "v_mov_b32 v79, v64\n"
        "v_mov_b32 v96, 0\n"
        "v_mov_b32 v97, 0\n"
        "v_mov_b32 v98, 0\n"
        "v_mov_b32 v99, 0\n"
        "v_mov_b32 v100, 0\n"
        "v_mov_b32 v101, 0\n"
        "v_mov_b32 v102, 0\n"
        "v_mov_b32 v103, 0\n"
        "v_mov_b32 v104, 0\n"
        "v_mov_b32 v105, 0\n"
        "v_mov_b32 v106, 0\n"
        "v_mov_b32 v107, 0\n"
        "v_mov_b32 v108, 0\n"
        "v_mov_b32 v109, 0\n"
        "v_mov_b32 v110, 0\n"
        "v_mov_b32 v111, 0\n"
        "v_lshrrev_b32 v8, 5, v1\n"
        "v_lshlrev_b32 v8, 8, v8\n"
        "v_and_b32 v80, 15, v1\n"
        "v_lshl_add_u32 v8, v80, 4, v8\n"
        "s_mov_b32 s62, 0\n"
        "233:\n"
        "s_lshr_b32 s63, s62, 1\n"
        "s_lshl_b32 s63, s63, 12\n"
        "s_and_b32 s64, s62, 1\n"
        "s_lshl_b32 s64, s64, 10\n"
        "s_add_u32 s63, s63, s64\n"
        "s_add_u32 s63, s63, s84\n"
        "buffer_load_dwordx4 v[136:139], v2, s[20:23], s63 offen sc1\n"
        "s_mov_b64 exec, s[60:61]\n"
        "buffer_load_dwordx4 v[140:143], v8, s[20:23], s63 offen offset:2048 sc1\n"
        "buffer_load_dwordx4 v[144:147], v8, s[20:23], s63 offen offset:2560 sc1\n"
        "s_mov_b64 exec, -1\n"
        "s_waitcnt vmcnt(0)\n"
        "v_mfma_f32_32x32x16_bf16 v[48:63], v[136:139], v[128:131], v[64:79]\n"
        "s_nop 11\n"
        "v_exp_f32 v48, v48\n"
        "v_exp_f32 v49, v49\n"
        "v_exp_f32 v50, v50\n"
        "v_exp_f32 v51, v51\n"
        "v_exp_f32 v52, v52\n"
        "v_exp_f32 v53, v53\n"
        "v_exp_f32 v54, v54\n"
        "v_exp_f32 v55, v55\n"
        "v_exp_f32 v56, v56\n"
        "v_exp_f32 v57, v57\n"
        "v_exp_f32 v58, v58\n"
        "v_exp_f32 v59, v59\n"
        "v_exp_f32 v60, v60\n"
        "v_exp_f32 v61, v61\n"
        "v_exp_f32 v62, v62\n"
        "v_exp_f32 v63, v63\n"
        "v_smfmac_f32_32x32x32_bf16 v[96:111], v[140:143], v[48:55], v20\n"
        "s_nop 0\n"
        "v_smfmac_f32_32x32x32_bf16 v[96:111], v[144:147], v[56:63], v20\n"
        "s_add_u32 s62, s62, 1\n"
        "s_cmp_lt_u32 s62, 64\n"
        "s_cbranch_scc1 233b\n"
        "s_nop 8\n"
        "v_mov_b32 v120, v104\n"
        "v_mov_b32 v121, v104\n"
        "s_nop 1\n"
        "v_permlane32_swap_b32 v120, v121\n"
        "v_rcp_f32 v120, v120\n"
        "s_nop 0\n"
        "v_pk_mul_f32 v[64:65], v[120:121], v[96:97] op_sel:[0,0] op_sel_hi:[0,1]\n"
        "v_pk_mul_f32 v[66:67], v[120:121], v[98:99] op_sel:[0,0] op_sel_hi:[0,1]\n"
        "v_pk_mul_f32 v[68:69], v[120:121], v[100:101] op_sel:[0,0] op_sel_hi:[0,1]\n"
        "v_pk_mul_f32 v[70:71], v[120:121], v[102:103] op_sel:[0,0] op_sel_hi:[0,1]\n"
        "v_cvt_pk_bf16_f32 v108, v64, v65\n"
        "v_cvt_pk_bf16_f32 v109, v66, v67\n"
        "v_cvt_pk_bf16_f32 v110, v68, v69\n"
        "v_cvt_pk_bf16_f32 v111, v70, v71\n"
        "buffer_store_dwordx4 v[108:111], v2, s[20:23], s1 offen sc1\n"
        "s_waitcnt vmcnt(0)\n"
        "s_branch 231b\n"
        "234:\n"
        "buffer_load_dword v6, v5, s[20:23], 0 offen sc1\n"
        "s_waitcnt vmcnt(1)\n"
        "s_barrier\n"
        "buffer_store_dword v3, v4, s[20:23], 0 offen sc1\n"
        "s_waitcnt vmcnt(1)\n"
        "v_cmp_ne_u32 vcc, v6, v3\n"
        "s_cbranch_vccnz 236f\n"
        "237:\n"
        "s_add_u32 s64, s71, 32768\n"
        "s_add_u32 s65, s64, 90112\n"
        "buffer_load_dwordx4 v[12:15], v2, s[20:23], s64 offen offset:0 sc1\n"
        "buffer_load_dwordx4 v[16:19], v2, s[20:23], s65 offen offset:0 sc1\n"
        "buffer_load_dwordx4 v[20:23], v2, s[20:23], s64 offen offset:1024 sc1\n"
        "buffer_load_dwordx4 v[24:27], v2, s[20:23], s65 offen offset:1024 sc1\n"
        "buffer_load_dword v6, v4, s[36:39], 0 offen\n"
        "s_branch 235b\n"
        "236:\n"
        "s_sub_u32 s59, s59, 1\n"
        "s_cbranch_scc1 100236f\n"
        "s_sleep 2\n"
        "buffer_load_dword v6, v5, s[20:23], 0 offen sc1\n"
        "s_waitcnt vmcnt(0)\n"
        "v_cmp_ne_u32 vcc, v6, v3\n"
        "s_cbranch_vccnz 236b\n"
        "s_nop 1\n"
        "s_branch 237b\n"
        "100236:\n"
        "s_mov_b32 s59, 0\n"
        "s_branch 237b\n"
        "238:\n"
        "s_sub_u32 s59, s59, 1\n"
        "s_cbranch_scc1 100238f\n"
        "s_sleep 2\n"
        "buffer_load_dword v8, v9, s[20:23], 0 offen sc1\n"
        "s_waitcnt vmcnt(0)\n"
        "v_cmp_ne_u32 vcc, v8, v3\n"
        "s_cbranch_vccnz 238b\n"
        "s_nop 1\n"
        "s_branch 239b\n"
        "100238:\n"
        "s_mov_b32 s59, 0\n"
        "s_branch 239b\n"
        "99:\n"
        : "+{v0}"(tid), "+{s[4:5]}"(args), "+{s6}"(bid)
        :
        : "v1", "v2", "v3", "v4", "v5", "v6", "v7", "v8", "v9", "v10", "v11", "v12", "v13", "v14", "v15", "v16", "v17", "v18", "v19", "v20", "v21", "v22", "v23", "v24", "v25", "v26", "v27", "v28", "v29", "v30", "v31", "v32", "v33", "v34", "v35", "v36", "v37", "v38", "v39", "v40", "v41", "v42", "v43", "v44", "v45", "v46", "v47", "v48", "v49", "v50", "v51", "v52", "v53", "v54", "v55", "v56", "v57", "v58", "v59", "v60", "v61", "v62", "v63", "v64", "v65", "v66", "v67", "v68", "v69", "v70", "v71", "v72", "v73", "v74", "v75", "v76", "v77", "v78", "v79", "v80", "v81", "v82", "v83", "v84", "v85", "v86", "v87", "v88", "v89", "v90", "v91", "v92", "v93", "v94", "v95", "v96", "v97", "v98", "v99", "v100", "v101", "v102", "v103", "v104", "v105", "v106", "v107", "v108", "v109", "v110", "v111", "v112", "v113", "v114", "v115", "v116", "v117", "v118", "v119", "v120", "v121", "v122", "v123", "v124", "v125", "v126", "v127", "v128", "v129", "v130", "v131", "v132", "v133", "v134", "v135", "v136", "v137", "v138", "v139", "v140", "v141", "v142", "v143", "v144", "v145", "v146", "v147", "v148", "v149", "v150", "v151", "v152", "v153", "v154", "v155", "v156", "v157", "v158", "v159", "v160", "v161", "v162", "v163", "v164", "v165", "v166", "v167", "v168", "v169", "v170", "v171", "v172", "v173", "v174", "v175", "v176", "v177", "v178", "v179", "v180", "v181", "v182", "v183", "v184", "v185", "v186", "v187", "v188", "v189", "v190", "v191", "v192", "v193", "v194", "v195", "v196", "v197", "v198", "v199", "v200", "v201", "v202", "v203", "v204", "v205", "v206", "v207", "v208", "v209", "v210", "v211", "v212", "v213", "v214", "v215", "v216", "v217", "v218", "v219", "v220", "v221", "v222", "v223", "v224", "v225", "v226", "v227", "v228", "v229", "v230", "v231", "v232", "v233", "v234", "v235", "v236", "v237", "v238", "v239", "v240", "v241", "v242", "v243", "v244", "v245", "v246", "v247", "v248", "v249", "v250", "v251", "v252", "v253", "v254", "v255", "s0", "s1", "s2", "s3", "s7", "s8", "s9", "s10", "s11", "s12", "s13", "s14", "s15", "s16", "s17", "s18", "s19", "s20", "s21", "s22", "s23", "s24", "s25", "s26", "s27", "s28", "s29", "s30", "s31", "s32", "s33", "s34", "s35", "s36", "s37", "s38", "s39", "s40", "s41", "s42", "s43", "s44", "s45", "s46", "s47", "s48", "s49", "s50", "s51", "s52", "s53", "s54", "s55", "s56", "s57", "s58", "s59", "s60", "s61", "s62", "s63", "s64", "s65", "s66", "s67", "s68", "s69", "s70", "s71", "s72", "s73", "s74", "s75", "s76", "s77", "s78", "s79", "s80", "s81", "s82", "s83", "s84", "s85", "s86", "s87", "s88", "s89", "vcc", "scc", "memory");
}
extern "C" void kernel_launch(void* const* d_in, const int* in_sizes, int n_in, void* d_out, int out_size, void* d_ws, size_t ws_size, hipStream_t stream) {
    (void)hipFuncSetAttribute((const void*)fused_block, hipFuncAttributeMaxDynamicSharedMemorySize, LDS_BYTES);
    fused_block<<<256, 512, LDS_BYTES, stream>>>(d_in[0], d_in[1], d_in[2], d_in[4], d_in[5], d_in[6], d_in[7], d_out, d_ws);
}
```
